```python
import torch

def _softmax(x, axis=-1):
    return torch.softmax(x, dim=axis)
def _gelu(x):
    return torch.nn.functional.gelu(x, approximate="tanh")
def _silu(x):
    return torch.nn.functional.silu(x)
def _sigmoid(x):
    return torch.sigmoid(x)
def _layernorm(x, axis=-1, eps=1e-5):
    m = x.mean(dim=axis, keepdim=True)
    v = x.var(dim=axis, keepdim=True, unbiased=False)
    return (x - m) / torch.sqrt(v + eps)
def _scatter_add(tgt, idx, src, axis=0):
    return tgt.clone().index_add_(axis, idx, src)
def _segment_sum(src, seg, n):
    out = torch.zeros((n,) + src.shape[1:], dtype=src.dtype)
    return out.index_add_(0, seg, src)
def _pad(x, pw):
    flat = tuple(v for pair in reversed(pw) for v in pair)
    return torch.nn.functional.pad(x, flat)

kernel_name = 'interleave_layers__sliding_block__dense_activation_relu__prompt'

def setup_inputs():

    _w = (lambda a: (lambda t: t.to(torch.bfloat16) if t.is_floating_point() else t)(torch.as_tensor(a)))
    def _A_setup():
        B, S, D = 1, 2048, 448
        return {
            "x": _w((lambda *s: torch.randn(*s, dtype=torch.bfloat16) * 0.05)(B, S, D)),
            "wqkv": _w((lambda *s: torch.randn(*s, dtype=torch.bfloat16) * 0.05)(D, 3 * D)),
            "wo": _w((lambda *s: torch.randn(*s, dtype=torch.bfloat16) * 0.05)(D, D)),
        }
    _A_in = _A_setup()

    def _B_setup():
        B, S, D, Dff = 1, 2048, 448, 1792
        return {
            "x": _w((lambda *s: torch.randn(*s, dtype=torch.bfloat16) * 0.05)(B, S, D)),
            "w1": _w((lambda *s: torch.randn(*s, dtype=torch.bfloat16) * 0.05)(D, Dff)),
            "w2": _w((lambda *s: torch.randn(*s, dtype=torch.bfloat16) * 0.05)(Dff, D)),
        }
    _B_in = _B_setup()

    return {
        "A_x": _A_in["x"],
        "A_wqkv": _A_in["wqkv"],
        "A_wo": _A_in["wo"],
        "B_x": _B_in["x"],
        "B_w1": _B_in["w1"],
        "B_w2": _B_in["w2"],
    }

def reference(A_x, A_wqkv, A_wo, B_x, B_w1, B_w2):
    def _A_ref(x, wqkv, wo):
        B, S, D = x.shape
        H = 16; Dh = D // H; W = 256
        qkv = (lambda x, *d: x.permute(*d))((x @ wqkv).reshape(B, S, 3, H, Dh), 2, 0, 3, 1, 4)
        q, k, v = qkv[0], qkv[1], qkv[2]
        scores = torch.einsum("bhqd,bhkd->bhqk", q, k) * (Dh ** -0.5)
        idx = torch.arange(S)
        mask = (idx[:, None] - idx[None, :] >= 0) & (idx[:, None] - idx[None, :] < W)
        scores = torch.where(mask[None, None], scores, -1e9)
        p = _softmax(scores, axis=-1)
        o = torch.einsum("bhqk,bhkd->bhqd", p, v)
        return (lambda x, *d: x.permute(*d))(o, 0, 2, 1, 3).reshape(B, S, D) @ wo

    def _B_ref(x, w1, w2):
        h = torch.einsum("bsd,df->bsf", x, w1)
        zero = torch.zeros_like(h)
        h = torch.maximum(h, zero)
        return torch.einsum("bsf,fd->bsd", h, w2)

    x = A_x
    x = x + _A_ref(_layernorm(x, axis=-1), A_wqkv, A_wo)
    x = x + _B_ref(_layernorm(x, axis=-1), B_w1, B_w2)
    x = x + _A_ref(_layernorm(x, axis=-1), A_wqkv, A_wo)
    x = x + _B_ref(_layernorm(x, axis=-1), B_w1, B_w2)
    return x
```

```cpp
#include <hip/hip_runtime.h>
#include <hip/hip_bf16.h>
#include <stdint.h>

#define GAS __attribute__((address_space(1)))
#define LAS __attribute__((address_space(3)))
#define DI __device__ __forceinline__
typedef unsigned v4u __attribute__((ext_vector_type(4)));
typedef unsigned v2u __attribute__((ext_vector_type(2)));
typedef float f4 __attribute__((ext_vector_type(4)));
typedef float f2 __attribute__((ext_vector_type(2)));
typedef __bf16 bf8 __attribute__((ext_vector_type(8)));
typedef __bf16 bf2 __attribute__((ext_vector_type(2)));
typedef short s2 __attribute__((ext_vector_type(2)));
typedef GAS unsigned gu32;
typedef __amdgpu_buffer_rsrc_t rsrc_t;
#define RLX_AGENT __ATOMIC_RELAXED, __HIP_MEMORY_SCOPE_AGENT

constexpr int S_ = 2048, D_ = 448, DFF = 1792;
constexpr unsigned OFF_CTRL = 0;
constexpr unsigned OFF_TMO = 256 * 512;
constexpr unsigned OFF_WQ = 256 * 512 + 4096;
constexpr unsigned OFF_W1 = OFF_WQ + 16 * 84 * 1024;
constexpr unsigned OFF_WO = OFF_W1 + 16 * 98 * 1024;
constexpr unsigned OFF_W2 = OFF_WO + 8 * 56 * 1024;
constexpr unsigned OFF_CSQ = OFF_W2 + 8 * 196 * 1024;
constexpr unsigned OFF_CS1 = OFF_CSQ + 16 * 96 * 4;
constexpr unsigned XT = 15 * 1024;
constexpr unsigned OFF_X = (OFF_CS1 + DFF * 4 + 4095) / 4096 * 4096;
constexpr unsigned XBUF = 128 * XT;
constexpr unsigned OFF_KV = OFF_X + 4 * XBUF;
constexpr unsigned KVBUF = 16 * 64 * 4096;
constexpr unsigned OFF_O = OFF_KV + 2 * KVBUF;
constexpr unsigned OBUF = 128 * 16 * 1024;
constexpr unsigned OFF_H = OFF_O + 2 * OBUF;
constexpr unsigned HT = 56 * 1024, HBUF = 128 * HT;
constexpr unsigned WS_END = OFF_H + 2 * HBUF;
constexpr unsigned OFF_STAMP = WS_END;

constexpr int L_KV = 0;
constexpr int L_WKV = 72 * 1024, L_WQ = 128 * 1024, L_WO = 72 * 1024;
constexpr int L_MISC = 156 * 1024;
constexpr int L_CSQ = L_MISC, L_CS1 = L_MISC + 512, L_ST = L_MISC + 1024;
constexpr int LDS_BYTES = 160 * 1024;
constexpr int w1_slot(int f) { return f < 48 ? f : (f < 72 ? f : 128 + (f - 72)); }

constexpr unsigned kSpinCap = 1u << 18;
constexpr float NEG = -1e30f;

__host__ __device__ inline int xfeat(int u, int e) {
  const int c = u / 7, i = u % 7;
  int f;
  if (i < 4) f = (e < 4) ? 4 * i + e : 16 + 4 * i + (e - 4);
  else if (i < 6) f = (e < 4) ? 32 + 4 * (i - 4) + e : 48 + 4 * (i - 4) + (e - 4);
  else f = 40 + e;
  return 56 * c + f;
}
__host__ __device__ inline int hfeat(int u, int e) {
  const int j = u / 14, i = u % 14;
  int f;
  if (i < 12) f = 32 * (i >> 2) + ((e < 4) ? 4 * (i & 3) + e : 16 + 4 * (i & 3) + (e - 4));
  else f = 96 + 8 * (i - 12) + e;
  return 112 * j + f;
}
__host__ __device__ inline int dperm(int q, int e) { return (e < 4) ? 4 * q + e : 16 + 4 * q + (e - 4); }

DI void mfma(f4& d, v4u a, v4u b) { asm("v_mfma_f32_16x16x32_bf16 %0, %1, %2, %0" : "+v"(d) : "v"(a), "v"(b)); }
DI void mfma_n(f4& d, v4u a, v4u b) { asm("s_nop 1\n\tv_mfma_f32_16x16x32_bf16 %0, %1, %2, %0" : "+v"(d) : "v"(a), "v"(b)); }
DI void mfma0(f4& d, v4u a, v4u b) { asm("v_mfma_f32_16x16x32_bf16 %0, %1, %2, 0" : "=&v"(d) : "v"(a), "v"(b)); }
DI void mfma0_n(f4& d, v4u a, v4u b) { asm("s_nop 1\n\tv_mfma_f32_16x16x32_bf16 %0, %1, %2, 0" : "=&v"(d) : "v"(a), "v"(b)); }
DI void mfmac(f4& d, v4u a, v4u b, f4 c) { asm("v_mfma_f32_16x16x32_bf16 %0, %1, %2, %3" : "=&v"(d) : "v"(a), "v"(b), "v"(c)); }
DI void mfma_a(f4& d, v4u a, v4u b) { asm("v_mfma_f32_16x16x32_bf16 %0, %1, %2, %0" : "+v"(d) : "a"(a), "v"(b)); }
DI void mfma0_a(f4& d, v4u a, v4u b) { asm("v_mfma_f32_16x16x32_bf16 %0, %1, %2, 0" : "=&v"(d) : "a"(a), "v"(b)); }
DI void mfence(f4& a) { asm("s_nop 7" : "+v"(a)); }
DI void mfence(f4& a, f4& b) { asm("s_nop 7" : "+v"(a), "+v"(b)); }
DI void mfence(f4& a, f4& b, f4& c, f4& d) { asm("s_nop 7" : "+v"(a), "+v"(b), "+v"(c), "+v"(d)); }
DI void mfence(f4& a, f4& b, f4& c, f4& d, f4& e) { asm("s_nop 7" : "+v"(a), "+v"(b), "+v"(c), "+v"(d), "+v"(e)); }
DI void mfence(f4& a, f4& b, f4& c, f4& d, f4& e, f4& f) { asm("s_nop 7" : "+v"(a), "+v"(b), "+v"(c), "+v"(d), "+v"(e), "+v"(f)); }
DI void mfence(f4& a, f4& b, f4& c, f4& d, f4& e, f4& f, f4& g, f4& h) {
  asm("s_nop 7" : "+v"(a), "+v"(b), "+v"(c), "+v"(d), "+v"(e), "+v"(f), "+v"(g), "+v"(h));
}
DI void vfence(f4& a) { asm("s_nop 1" : "+v"(a)); }
DI unsigned opaque(unsigned x) { x = __builtin_amdgcn_readfirstlane(x); asm volatile("" : "+s"(x)); return x; }
DI unsigned pk2(float a, float b) {
  f2 v = {a, b};
  return __builtin_bit_cast(unsigned, __builtin_convertvector(v, bf2));
}
DI v4u pk8(f4 a, f4 b) { return v4u{pk2(a.x, a.y), pk2(a.z, a.w), pk2(b.x, b.y), pk2(b.z, b.w)}; }
DI float bflo(unsigned u) { return __builtin_bit_cast(float, u << 16); }
DI float bfhi(unsigned u) { return __builtin_bit_cast(float, u & 0xffff0000u); }
DI unsigned relu2(unsigned u) {
  return __builtin_bit_cast(unsigned, __builtin_elementwise_max(__builtin_bit_cast(s2, u), s2{0, 0}));
}
DI unsigned f2u(float x) { return __builtin_bit_cast(unsigned, x); }
DI float u2f(unsigned x) { return __builtin_bit_cast(float, x); }
DI float rowsum4(float x) {
  auto a = __builtin_amdgcn_permlane16_swap(f2u(x), f2u(x), false, false);
  x = u2f(a[0]) + u2f(a[1]);
  auto b = __builtin_amdgcn_permlane32_swap(f2u(x), f2u(x), false, false);
  return u2f(b[0]) + u2f(b[1]);
}
DI float rowmax4(float x) {
  auto a = __builtin_amdgcn_permlane16_swap(f2u(x), f2u(x), false, false);
  x = fmaxf(u2f(a[0]), u2f(a[1]));
  auto b = __builtin_amdgcn_permlane32_swap(f2u(x), f2u(x), false, false);
  return fmaxf(u2f(b[0]), u2f(b[1]));
}
DI float halfsum(float x) {
  auto b = __builtin_amdgcn_permlane32_swap(f2u(x), f2u(x), false, false);
  return u2f(b[0]) + u2f(b[1]);
}
DI float bcast_row3(float x) {
  auto b = __builtin_amdgcn_permlane32_swap(f2u(x), f2u(x), false, false);
  auto a = __builtin_amdgcn_permlane16_swap(b[1], b[1], false, false);
  return u2f(a[1]);
}

#define SCHED_FENCE() __builtin_amdgcn_sched_barrier(0)
#define WAIT_VM(n) asm volatile("s_waitcnt vmcnt(" #n ")" ::: "memory")
#define WAIT_LGKM0() asm volatile("s_waitcnt lgkmcnt(0)" ::: "memory")
#define BARRIER() do { asm volatile("s_waitcnt lgkmcnt(0)\n\ts_barrier" ::: "memory"); } while (0)

DI v4u ld16(rsrc_t rs, unsigned voff, unsigned sbase, unsigned off) {
  return __builtin_amdgcn_raw_buffer_load_b128(rs, voff + (off & 4095u), sbase + (off & ~4095u), 16);
}
DI v2u ld8(rsrc_t rs, unsigned voff, unsigned sbase, unsigned off) {
  return __builtin_amdgcn_raw_buffer_load_b64(rs, voff + (off & 4095u), sbase + (off & ~4095u), 16);
}
DI void st16(v4u v, rsrc_t rs, unsigned voff, unsigned sbase, unsigned off) {
  __builtin_amdgcn_raw_buffer_store_b128(v, rs, voff + (off & 4095u), sbase + (off & ~4095u), 16);
  asm volatile("s_nop 1" :: "v"(v) : "memory");
}
DI void st8(v2u v, rsrc_t rs, unsigned voff, unsigned sbase, unsigned off) {
  __builtin_amdgcn_raw_buffer_store_b64(v, rs, voff + (off & 4095u), sbase + (off & ~4095u), 16);
}

#define DMA_HEAD "s_mov_b32 %0, m0\n\ts_mov_b32 m0, %4\n\ts_nop 0\n\t"
#define DMA_OP(o) "buffer_load_dwordx4 %1, %2, %3 offen offset:" #o " lds\n\t"
#define DMA_TAIL "s_mov_b32 m0, %0"
#define DMA_ARGS : "=&s"(keep) : "v"(voff), "s"(rs), "s"(soff), "s"(ldsaddr) : "memory"
template <int N> DI void dma(rsrc_t rs, unsigned voff, unsigned soff, unsigned ldsaddr) {
  unsigned keep;
  if constexpr (N == 1) asm volatile(DMA_HEAD DMA_OP(0) DMA_TAIL DMA_ARGS);
  if constexpr (N == 2) asm volatile(DMA_HEAD DMA_OP(0) DMA_OP(1024) DMA_TAIL DMA_ARGS);
  if constexpr (N == 3) asm volatile(DMA_HEAD DMA_OP(0) DMA_OP(1024) DMA_OP(2048) DMA_TAIL DMA_ARGS);
  if constexpr (N >= 4) asm volatile(DMA_HEAD DMA_OP(0) DMA_OP(1024) DMA_OP(2048) DMA_OP(3072) DMA_TAIL DMA_ARGS);
  if constexpr (N > 4) dma<N - 4>(rs, voff, soff + 4096, ldsaddr + 4096);
}
template <int LO, int HI> DI void ldw_a(v4u (&dst)[49], rsrc_t rs, unsigned voff, unsigned sbase) {
  if constexpr (LO < HI) {
    asm volatile("buffer_load_dwordx4 %0, %1, %2, %3 offen offset:%4"
                 : "+a"(dst[LO]) : "v"(voff), "s"(rs), "s"(sbase + ((LO * 1024) & ~4095u)), "i"((LO * 1024) & 4095) : "memory");
    ldw_a<LO + 1, HI>(dst, rs, voff, sbase);
  }
}

DI void poll_ge(gu32* word, unsigned want, gu32* tmo) {
#pragma unroll 1
  for (unsigned spins = 0;;) {
    if (__hip_atomic_load(word, RLX_AGENT) >= want) return;
    if (++spins > kSpinCap) { __hip_atomic_store(tmo, 1u, RLX_AGENT); return; }
    if ((spins & 1023u) == 0u && __hip_atomic_load(tmo, RLX_AGENT) != 0u) return;
  }
}

#define STAMP(i) do {} while (0)

constexpr int NF_WQ = 16 * 84, NF_W1 = 16 * 98, NF_WO = 8 * 56, NF_W2 = 8 * 196, NF_X = 128 * 14;
constexpr int NF_ALL = NF_WQ + NF_W1 + NF_WO + NF_W2 + NF_X;
constexpr int N_CS = 96 + 112;
constexpr int N_TASK = NF_ALL + N_CS + 128;
constexpr int PREP_WAVES = 16;

__global__ void __launch_bounds__(PREP_WAVES * 64) k_prep(const uint16_t* __restrict__ x, const uint16_t* __restrict__ wqkv,
                                                          const uint16_t* __restrict__ wo, const uint16_t* __restrict__ w1,
                                                          const uint16_t* __restrict__ w2, unsigned char* __restrict__ ws) {
  const int tid = threadIdx.x, lane = tid & 63, q = lane >> 4, r = lane & 15;
  if (tid < 128) ((unsigned*)(ws + OFF_CTRL))[blockIdx.x * 128 + tid] = 0u;
  if (blockIdx.x == 0 && tid == 128) *(unsigned*)(ws + OFF_TMO) = 0u;
  const int nw = gridDim.x * PREP_WAVES;
  for (int task = blockIdx.x + gridDim.x * (tid >> 6); task < N_TASK; task += nw) {
    int f = task;
    if (f < NF_ALL) {
      const uint16_t* src; int ld, col; bool ok = true; int rows[8]; bool rok[8];
      unsigned dst;
#pragma unroll
      for (int e = 0; e < 8; ++e) rok[e] = true;
      if (f < NF_WQ) {
        const int j = f / 84, ff = f % 84;
        int s, n;
        if (ff < 56) { s = ff >> 2; n = 2 + (ff & 3); } else { s = (ff - 56) >> 1; n = (ff - 56) & 1; }
        const int d = 16 * (n & 1) + r;
        ok = d < 28; col = (n >> 1) * D_ + 28 * j + (ok ? d : 0); src = wqkv; ld = 3 * D_;
#pragma unroll
        for (int e = 0; e < 8; ++e) rows[e] = xfeat(4 * s + q, e);
        dst = OFF_WQ + f * 1024;
      } else if ((f -= NF_WQ) < NF_W1) {
        const int j = f / 98, ff = f % 98, s = ff / 7, n = ff % 7;
        col = 112 * j + 16 * n + r; src = w1; ld = DFF;
#pragma unroll
        for (int e = 0; e < 8; ++e) rows[e] = xfeat(4 * s + q, e);
        dst = OFF_W1 + f * 1024;
      } else if ((f -= NF_W1) < NF_WO) {
        const int c = f / 56, ff = f % 56;
        int head;
        if (ff < 48) { head = ff / 3; col = 56 * c + 16 * (ff % 3) + r; } else { head = 2 * (ff - 48) + (r >> 3); col = 56 * c + 48 + (r & 7); }
        src = wo; ld = D_;
#pragma unroll
        for (int e = 0; e < 8; ++e) { const int d = dperm(q, e); rok[e] = d < 28; rows[e] = 28 * head + (rok[e] ? d : 0); }
        dst = OFF_WO + f * 1024;
      } else if ((f -= NF_WO) < NF_W2) {
        const int c = f / 196, ff = f % 196, w = ff / 49, i = ff % 49;
        int s;
        if (i < 42) { s = 14 * w + i / 3; col = 56 * c + 16 * (i % 3) + r; } else { s = 14 * w + 2 * (i - 42) + (r >> 3); col = 56 * c + 48 + (r & 7); }
        src = w2; ld = D_;
#pragma unroll
        for (int e = 0; e < 8; ++e) rows[e] = hfeat(4 * s + q, e);
        dst = OFF_W2 + f * 1024;
      } else {
        f -= NF_W2;
        const int tile = f / 14, s = f % 14;
        const uint16_t* row = x + (size_t)(16 * tile + r) * D_;
        const v2u a = *(const v2u*)(row + xfeat(4 * s + q, 0)), b = *(const v2u*)(row + xfeat(4 * s + q, 4));
        *(v4u*)(ws + OFF_X + tile * XT + s * 1024 + lane * 16) = v4u{a.x, a.y, b.x, b.y};
        continue;
      }
      unsigned v[8];
#pragma unroll
      for (int e = 0; e < 8; ++e) v[e] = src[(size_t)rows[e] * ld + col];
#pragma unroll
      for (int e = 0; e < 8; ++e) v[e] = (ok && rok[e]) ? v[e] : 0u;
      *(v4u*)(ws + dst + lane * 16) = v4u{v[0] | (v[1] << 16), v[2] | (v[3] << 16), v[4] | (v[5] << 16), v[6] | (v[7] << 16)};
    } else if ((f -= NF_ALL) < N_CS) {
      const uint16_t* src; int ld, col; bool ok = true; unsigned dst;
      if (f < 96) {
        const int j = f / 6, n = f % 6, d = 16 * (n & 1) + r;
        ok = d < 28; col = (n >> 1) * D_ + 28 * j + (ok ? d : 0); src = wqkv; ld = 3 * D_; dst = OFF_CSQ + (f * 16 + r) * 4;
      } else {
        col = (f - 96) * 16 + r; src = w1; ld = DFF; dst = OFF_CS1 + col * 4;
      }
      float sum = 0.f;
#pragma unroll 16
      for (int k = 112 * q; k < 112 * q + 112; ++k) sum += bflo(src[(size_t)k * ld + col]);
      sum = rowsum4(sum);
      if (q == 0) *(float*)(ws + dst) = ok ? sum : 0.f;
    } else {
      const int tile = f - N_CS;
      const uint16_t* row = x + (size_t)(16 * tile + r) * D_ + 112 * q;
#pragma unroll
      for (int h = 0; h < 2; ++h) {
        float s1 = 0.f, s2_ = 0.f;
#pragma unroll
        for (int i = 0; i < 7; ++i) {
          const v4u v = *(const v4u*)(row + 56 * h + 8 * i);
#pragma unroll
          for (int k = 0; k < 4; ++k) { const float a = bflo(v[k]), b = bfhi(v[k]); s1 += a + b; s2_ += a * a + b * b; }
        }
        *(f2*)(ws + OFF_X + tile * XT + 14 * 1024 + (2 * q + h) * 128 + r * 8) = f2{s1, s2_};
      }
    }
  }
}

struct Stats { float mean, rstd; };
DI Stats finish_stats(v2u a, v2u b) {
  float s1 = rowsum4(u2f(a.x) + u2f(b.x)), s2_ = rowsum4(u2f(a.y) + u2f(b.y));
  const float mean = s1 * (1.0f / D_);
  const float var = fmaxf(s2_ * (1.0f / D_) - mean * mean, 0.f);
  return Stats{mean, __builtin_amdgcn_rsqf(var + 1e-5f)};
}

template <bool LAST>
DI void publish_x(f4 (&xr)[4], const f4 (&v)[4], rsrc_t rs, unsigned base, int c, int lane, GAS unsigned char* outp) {
  const int q = lane >> 4;
  unsigned p[4][2];
#pragma unroll
  for (int n = 0; n < 4; ++n) {
    const f4 t = xr[n] + v[n];
    p[n][0] = pk2(t.x, t.y); p[n][1] = pk2(t.z, t.w);
  }
  if (LAST) {
#pragma unroll
    for (int n = 0; n < 3; ++n) *(GAS v2u*)(outp + 32 * n) = v2u{p[n][0], p[n][1]};
    if (q < 2) *(GAS v2u*)(outp + 96) = v2u{p[3][0], p[3][1]};
    return;
  }
  st16(v4u{p[0][0], p[0][1], p[1][0], p[1][1]}, rs, lane * 16, base + c * 1792, 0);
  auto s0 = __builtin_amdgcn_permlane16_swap(p[2][0], p[2][0], false, false);
  auto s1 = __builtin_amdgcn_permlane16_swap(p[2][1], p[2][1], false, false);
  const unsigned h0 = q < 2 ? p[3][0] : s0[1], h1 = q < 2 ? p[3][1] : s1[1];
  if (lane < 48) st16(v4u{p[2][0], p[2][1], h0, h1}, rs, lane * 16, base + c * 1792, 1024);
  float su = 0.f, sq = 0.f;
#pragma unroll
  for (int n = 0; n < 4; ++n) {
    xr[n] = f4{bflo(p[n][0]), bfhi(p[n][0]), bflo(p[n][1]), bfhi(p[n][1])};
    f4 t = xr[n];
    if (n == 3 && q >= 2) t = f4{0.f, 0.f, 0.f, 0.f};
    su += (t.x + t.y) + (t.z + t.w);
    sq += (t.x * t.x + t.y * t.y) + (t.z * t.z + t.w * t.w);
  }
  su = rowsum4(su); sq = rowsum4(sq);
  if (lane < 16) st8(v2u{f2u(su), f2u(sq)}, rs, lane * 8, base + 14 * 1024 + c * 128, 0);
}

__global__ void __launch_bounds__(256, 1) k_main(const uint16_t* __restrict__ xin, uint16_t* __restrict__ out, unsigned char* __restrict__ ws) {
  extern __shared__ __attribute__((aligned(16))) unsigned char smem[];
  LAS unsigned char* L = (LAS unsigned char*)smem;
  const int tid = threadIdx.x, w = __builtin_amdgcn_readfirstlane(tid >> 6);
  int lane, q, t;
  unsigned l16, lst;
  LAS v4u *Lf, *Lg, *Lh;
#define RELANE() do { lane = tid & 63; asm volatile("" : "+v"(lane)); __builtin_assume(lane >= 0 && lane < 64); q = lane >> 4; t = lane & 15; l16 = lane * 16; lst = q * 256 + t * 8; \
    Lf = (LAS v4u*)(L + l16); Lg = (LAS v4u*)(L + l16 + 65536); Lh = (LAS v4u*)(L + l16 + 131072); asm volatile("" : "+v"(Lg), "+v"(Lh)); } while (0)
  RELANE();
  const int b = blockIdx.x, g = 2 * (b & 7) + (b >> 7), j = (b >> 3) & 15;
  const int hf = j >> 3, c = j & 7;
  const rsrc_t rs = __builtin_amdgcn_make_buffer_rsrc(ws, 0, WS_END, 0x00020000);
  gu32* ctrl = (gu32*)(ws + OFF_CTRL);
  gu32* tmo = (gu32*)(ws + OFF_TMO);
  gu32* my_grp = ctrl + (g * 16 + j) * 128;
  gu32* my_halo = my_grp + 64;
#define LFRAG(slot) ((slot) < 64 ? Lf[(slot) * 64] : (slot) < 128 ? Lg[((slot) - 64) * 64] : Lh[((slot) - 128) * 64])
  STAMP(0);

  dma<21>(rs, l16, OFF_WQ + (j * 84 + w * 21) * 1024, L_WKV + w * 21 * 1024);
  if (tid < 96) ((LAS float*)(L + L_CSQ))[tid] = ((const GAS float*)(ws + OFF_CSQ))[j * 96 + tid];
  else if (tid >= 128 && tid < 240) ((LAS float*)(L + L_CS1))[tid - 128] = ((const GAS float*)(ws + OFF_CS1))[j * 112 + tid - 128];
  const int rtile = 8 * g + 4 * hf + w;
  const unsigned roff = (16 * rtile + t) * (D_ * 2) + (56 * c + 4 * q) * 2;
  f4 xr[4];
#pragma unroll
  for (int n = 0; n < 4; ++n) {
    const v2u v = *(const GAS v2u*)((const GAS unsigned char*)xin + roff + (n < 3 ? 32 * n : 96 - (q >= 2 ? 16 : 0)));
    xr[n] = f4{bflo(v.x), bfhi(v.x), bflo(v.y), bfhi(v.y)};
  }
  v4u w2[49];
#pragma unroll
  for (int i = 0; i < 49; ++i) asm volatile("" : "=a"(w2[i]));
  unsigned gcount = 0;

#pragma unroll 1
  for (int layer = 0; layer < 2; ++layer) {
    if (layer) {
      gcount += 16;
      if (tid == 0) poll_ge(my_grp, gcount, tmo);
    }
    WAIT_VM(0);
    BARRIER();
    STAMP(1 + 16 * layer);
    RELANE();
    v4u xf[2][14];
    v4u qf[2];
    {
      const unsigned xb = opaque(OFF_X + (layer ? 2 : 0) * XBUF + (8 * g + 2 * w) * XT);
      v2u sa[2], sb[2];
#pragma unroll
      for (int tt = 0; tt < 2; ++tt) {
        sa[tt] = ld8(rs, lst, xb, tt * XT + 14 * 1024);
        sb[tt] = ld8(rs, lst, xb, tt * XT + 14 * 1024 + 128);
      }
#pragma unroll
      for (int s = 0; s < 14; ++s)
#pragma unroll
        for (int tt = 0; tt < 2; ++tt) { xf[tt][s] = ld16(rs, l16, xb, tt * XT + s * 1024); SCHED_FENCE(); }
      v4u wf[2][4];
#pragma unroll
      for (int n = 0; n < 4; ++n) wf[0][n] = LFRAG(72 + n);
      SCHED_FENCE();
      Stats st[2];
      LAS float* sx = (LAS float*)(L + L_ST + w * 256);
#pragma unroll
      for (int tt = 0; tt < 2; ++tt) {
        st[tt] = finish_stats(sa[tt], sb[tt]);
        if (q == 0) { sx[16 * tt + t] = st[tt].rstd; sx[32 + 16 * tt + t] = st[tt].mean; }
      }
      SCHED_FENCE();
      f4 acc[2][4];
#pragma unroll
      for (int s = 0; s < 14; ++s) {
#pragma unroll
        for (int n = 0; n < 4; ++n) {
          if (s + 1 < 14) wf[(s + 1) & 1][n] = LFRAG(72 + (s + 1) * 4 + n);
#pragma unroll
          for (int tt = 0; tt < 2; ++tt) {
            const v4u a = n < 2 ? wf[s & 1][n] : xf[tt][s], bb = n < 2 ? xf[tt][s] : wf[s & 1][n];
            if (s == 0) mfma0(acc[tt][n], a, bb); else mfma(acc[tt][n], a, bb);
          }
          SCHED_FENCE();
        }
      }
      const LAS float* cs = (const LAS float*)(L + L_CSQ);
      v4u kf[2], vf[2];
      {
        const f4 c0 = *(const LAS f4*)(cs + 32 + 4 * q), c1 = *(const LAS f4*)(cs + 48 + 4 * q);
        f4 rv[2], mv[2];
#pragma unroll
        for (int tt = 0; tt < 2; ++tt) { rv[tt] = *(const LAS f4*)(sx + 16 * tt + 4 * q); mv[tt] = *(const LAS f4*)(sx + 32 + 16 * tt + 4 * q); }
        mfence(acc[0][0], acc[0][1], acc[0][2], acc[0][3], acc[1][0], acc[1][1], acc[1][2], acc[1][3]);
#pragma unroll
        for (int tt = 0; tt < 2; ++tt)
          kf[tt] = pk8((acc[tt][0] - st[tt].mean * c0) * st[tt].rstd, (acc[tt][1] - st[tt].mean * c1) * st[tt].rstd);
#pragma unroll
        for (int n = 0; n < 2; ++n) {
          const float cv = cs[64 + 16 * n + t];
          vf[n] = pk8((acc[0][2 + n] - mv[0] * cv) * rv[0], (acc[1][2 + n] - mv[1] * cv) * rv[1]);
        }
        if (t == 12) vf[1] = v4u{0x3F803F80u, 0x3F803F80u, 0x3F803F80u, 0x3F803F80u};
      }
      const unsigned kvb = opaque(OFF_KV + layer * KVBUF + (j * 64 + 4 * g + w) * 4096);
      st16(kf[0], rs, l16, kvb, 0); st16(kf[1], rs, l16, kvb, 1024); st16(vf[0], rs, l16, kvb, 2048); st16(vf[1], rs, l16, kvb, 3072);
      LAS v4u* own = Lf + (8 + w) * 4 * 64;
      own[0] = kf[0]; own[64] = kf[1]; own[128] = vf[0]; own[192] = vf[1];
      if (layer == 0) {
        ldw_a<0, 9>(w2, rs, l16, opaque(OFF_W2 + (c * 196 + w * 49) * 1024));
        WAIT_VM(9);
      } else WAIT_VM(0);
      BARRIER();
      if (tid < 2 && g + 1 + tid < 16) __hip_atomic_fetch_add(my_halo + (1 + tid) * 16 * 128, 1u, RLX_AGENT);
      STAMP(2 + 16 * layer);
      dma<14>(rs, l16, opaque(OFF_WO + (c * 56 + w * 14) * 1024), opaque(L_WO + w * 14 * 1024));
      f4 aq[2][2];
#pragma unroll
      for (int n = 0; n < 2; ++n) wf[0][n] = LFRAG(128 + n);
#pragma unroll
      for (int s = 0; s < 14; ++s) {
#pragma unroll
        for (int n = 0; n < 2; ++n) {
          if (s + 1 < 14) wf[(s + 1) & 1][n] = LFRAG(128 + (s + 1) * 2 + n);
#pragma unroll
          for (int tt = 0; tt < 2; ++tt) {
            if (s == 0) mfma0(aq[tt][n], wf[s & 1][n], xf[tt][s]); else mfma(aq[tt][n], wf[s & 1][n], xf[tt][s]);
          }
          SCHED_FENCE();
        }
      }
      const f4 c0 = *(const LAS f4*)(cs + 4 * q), c1 = *(const LAS f4*)(cs + 16 + 4 * q);
      const float qs = 0.18898223650461362f * 1.4426950408889634f;
      mfence(aq[0][0], aq[0][1], aq[1][0], aq[1][1]);
#pragma unroll
      for (int tt = 0; tt < 2; ++tt) {
        const float sc = st[tt].rstd * qs;
        qf[tt] = pk8((aq[tt][0] - st[tt].mean * c0) * sc, (aq[tt][1] - st[tt].mean * c1) * sc);
      }
    }
    if (g > 0 && tid == 0) poll_ge(my_halo, (layer + 1) * (g > 1 ? 2 : 1), tmo);
    BARRIER();
    STAMP(3 + 16 * layer);
    RELANE();
    {
      v4u hv[8];
      const int blk0 = 4 * g - 8 + 2 * w;
      if (blk0 >= 0) {
        const unsigned hb = opaque(OFF_KV + layer * KVBUF + (j * 64 + blk0) * 4096);
#pragma unroll
        for (int i = 0; i < 8; ++i) hv[i] = ld16(rs, l16, hb, i * 1024);
      } else {
#pragma unroll
        for (int i = 0; i < 8; ++i) hv[i] = v4u{0u, 0u, 0u, 0u};
      }
      LAS v4u* hal = Lf + w * 8 * 64;
#pragma unroll
      for (int i = 0; i < 8; ++i) hal[i * 64] = hv[i];
      dma<6>(rs, l16, opaque(OFF_W1 + (j * 98 + 48 + w * 6) * 1024), opaque((48 + w * 6) * 1024));
      {
        const int o7 = w == 3 ? 19 : 7 * w;
        dma<7>(rs, l16, opaque(OFF_W1 + (j * 98 + 72 + o7) * 1024), opaque((128 + o7) * 1024));
      }
      if (layer == 0) ldw_a<9, 22>(w2, rs, l16, opaque(OFF_W2 + (c * 196 + w * 49) * 1024));
      BARRIER();
      STAMP(4 + 16 * layer);
      const LAS v4u* kv = Lf + w * 4 * 64;
      float m[2] = {0.f, 0.f};
      f4 o[2][2];
      float inv[2];
      for (int attempt = 0;; ++attempt) {
        f4 cfull[2], cdiag[2], cold[2];
#pragma unroll
        for (int tq = 0; tq < 2; ++tq)
#pragma unroll
          for (int r = 0; r < 4; ++r) {
            cfull[tq][r] = -m[tq];
            cdiag[tq][r] = (4 * q + r <= t) ? -m[tq] : NEG;
            cold[tq][r] = (4 * q + r > t) ? -m[tq] : NEG;
          }
        v4u kb[2][2], vb[2][2];
        f4 sc[2][2][2];
        kb[0][0] = kv[0]; kb[0][1] = kv[64]; vb[0][0] = kv[128]; vb[0][1] = kv[192];
        kb[1][0] = kv[256]; kb[1][1] = kv[320];
        asm("s_nop 1" : "+v"(cfull[0]), "+v"(cfull[1]), "+v"(cdiag[0]), "+v"(cdiag[1]), "+v"(cold[0]), "+v"(cold[1]), "+v"(qf[0]), "+v"(qf[1]));
        mfmac(sc[0][0][0], kb[0][0], qf[0], cold[0]);
        mfmac(sc[0][0][1], kb[0][1], qf[0], cfull[0]);
        mfmac(sc[0][1][1], kb[0][1], qf[1], cold[1]);
        mfence(sc[0][0][0], sc[0][0][1], sc[0][1][1], cfull[0]);
        SCHED_FENCE();
#pragma unroll
        for (int bi = 0; bi < 9; ++bi) {
          const int p = bi & 1, pn = p ^ 1;
          const bool nx = bi + 1 < 9, last = bi + 1 == 8;
          if (nx) { vb[pn][0] = kv[((bi + 1) * 4 + 2) * 64]; vb[pn][1] = kv[((bi + 1) * 4 + 3) * 64]; }
          v4u pf[2];
          if (nx) mfmac(sc[pn][0][0], kb[pn][0], qf[0], last ? cdiag[0] : cfull[0]);
#pragma unroll
          for (int r = 0; r < 4; ++r) sc[p][0][0][r] = __builtin_amdgcn_exp2f(sc[p][0][0][r]);
          SCHED_FENCE();
          if (nx && !last) mfmac(sc[pn][0][1], kb[pn][1], qf[0], cfull[0]);
          if (bi != 8) {
#pragma unroll
            for (int r = 0; r < 4; ++r) sc[p][0][1][r] = __builtin_amdgcn_exp2f(sc[p][0][1][r]);
          } else sc[p][0][1] = f4{0.f, 0.f, 0.f, 0.f};
          SCHED_FENCE();
          if (nx) mfmac(sc[pn][1][0], kb[pn][0], qf[1], cfull[1]);
          pf[0] = pk8(sc[p][0][0], sc[p][0][1]);
          if (bi != 0) {
#pragma unroll
            for (int r = 0; r < 4; ++r) sc[p][1][0][r] = __builtin_amdgcn_exp2f(sc[p][1][0][r]);
          } else sc[p][1][0] = f4{0.f, 0.f, 0.f, 0.f};
          SCHED_FENCE();
          if (nx) mfmac(sc[pn][1][1], kb[pn][1], qf[1], last ? cdiag[1] : cfull[1]);
#pragma unroll
          for (int r = 0; r < 4; ++r) sc[p][1][1][r] = __builtin_amdgcn_exp2f(sc[p][1][1][r]);
          SCHED_FENCE();
          if (bi == 0) mfma0(o[0][0], vb[p][0], pf[0]); else mfma(o[0][0], vb[p][0], pf[0]);
          pf[1] = pk8(sc[p][1][0], sc[p][1][1]);
          SCHED_FENCE();
          if (bi == 0) mfma0(o[0][1], vb[p][1], pf[0]); else mfma(o[0][1], vb[p][1], pf[0]);
          if (bi + 2 < 9) { kb[p][0] = kv[((bi + 2) * 4 + 0) * 64]; kb[p][1] = kv[((bi + 2) * 4 + 1) * 64]; }
          SCHED_FENCE();
          if (bi == 0) mfma0_n(o[1][0], vb[p][0], pf[1]); else mfma_n(o[1][0], vb[p][0], pf[1]);
          if (bi == 0) mfma0(o[1][1], vb[p][1], pf[1]); else mfma(o[1][1], vb[p][1], pf[1]);
          SCHED_FENCE();
        }
        mfence(o[0][0], o[0][1], o[1][0], o[1][1]);
        asm volatile("" :: "v"(cfull[0]), "v"(cfull[1]), "v"(cdiag[0]), "v"(cdiag[1]), "v"(cold[0]), "v"(cold[1]), "v"(o[0][0]));
        bool safe = true;
#pragma unroll
        for (int tq = 0; tq < 2; ++tq) {
          const float l = bcast_row3(o[tq][1][0]);
          safe = safe && (l > 8.67e-19f) && (l < 1.15e18f);
          inv[tq] = __builtin_amdgcn_rcpf(l);
        }
        if (attempt || __builtin_amdgcn_ballot_w64(!safe) == 0ull) break;
#pragma unroll
        for (int tq = 0; tq < 2; ++tq) m[tq] = NEG;
#pragma unroll 1
        for (int bi = 0; bi < 9; ++bi) {
          if (4 * g + w - 8 + bi < 0) continue;
          const v4u k0 = kv[(bi * 4 + 0) * 64], k1 = kv[(bi * 4 + 1) * 64];
#pragma unroll
          for (int tq = 0; tq < 2; ++tq) {
            f4 s0, s1;
            mfma0(s0, k0, qf[tq]); mfma0(s1, k1, qf[tq]);
            mfence(s0, s1);
#pragma unroll
            for (int r = 0; r < 4; ++r) {
              const int d0 = 256 + 16 * tq + t - (32 * bi + 4 * q + r), d1 = d0 - 16;
              if (d0 >= 0 && d0 < 256) m[tq] = fmaxf(m[tq], s0[r]);
              if (d1 >= 0 && d1 < 256) m[tq] = fmaxf(m[tq], s1[r]);
            }
          }
        }
#pragma unroll
        for (int tq = 0; tq < 2; ++tq) m[tq] = rowmax4(m[tq]);
      }
      const unsigned ob = opaque(OFF_O + layer * OBUF + ((8 * g + 2 * w) * 16 + j) * 1024);
#pragma unroll
      for (int tq = 0; tq < 2; ++tq) st16(pk8(o[tq][0] * inv[tq], o[tq][1] * inv[tq]), rs, l16, ob, tq * 16 * 1024);
    }
    if (layer == 0) {
      ldw_a<22, 31>(w2, rs, l16, opaque(OFF_W2 + (c * 196 + w * 49) * 1024));
      WAIT_VM(9);
    } else WAIT_VM(0);
    BARRIER();
    if (tid < 16) __hip_atomic_fetch_add(ctrl + (g * 16 + tid) * 128, 1u, RLX_AGENT);
    STAMP(5 + 16 * layer);
    dma<12>(rs, l16, opaque(OFF_W1 + (j * 98 + w * 12) * 1024), opaque(w * 12 * 1024));
    gcount += 16;
    if (tid == 0) poll_ge(my_grp, gcount, tmo);
    WAIT_VM(0);
    BARRIER();
    STAMP(6 + 16 * layer);
    RELANE();
    {
      const unsigned ob = opaque(OFF_O + layer * OBUF + rtile * 16 * 1024);
      v4u of[16];
#pragma unroll
      for (int h = 0; h < 16; ++h) { of[h] = ld16(rs, l16, ob, h * 1024); SCHED_FENCE(); }
      v4u wo[2][7];
#pragma unroll
      for (int n = 0; n < 6; ++n) wo[0][n] = LFRAG(72 + n);
      wo[0][6] = LFRAG(72 + 48);
      SCHED_FENCE();
      f4 acc[5];
#pragma unroll
      for (int p = 0; p < 8; ++p) {
#pragma unroll
        for (int n = 0; n < 8; ++n) {
          const int h = 2 * p + (n >> 2), ai = (n & 3) < 3 ? (n & 3) : 3 + (n >> 2), wi = (n & 3) < 3 ? 3 * (n >> 2) + (n & 3) : 6;
          if (p + 1 < 8 && n < 7) wo[(p + 1) & 1][n] = n < 6 ? LFRAG(72 + (p + 1) * 6 + n) : LFRAG(72 + 48 + p + 1);
          if (p == 0 && (n < 4 || n == 7)) mfma0(acc[ai], wo[p & 1][wi], of[h]); else mfma(acc[ai], wo[p & 1][wi], of[h]);
          SCHED_FENCE();
        }
      }
      mfence(acc[0], acc[1], acc[2], acc[3], acc[4]);
      f4 v[4] = {acc[0], acc[1], acc[2], acc[3]};
#pragma unroll
      for (int r = 0; r < 4; ++r) v[3][r] = halfsum(q < 2 ? acc[3][r] : acc[4][r]);
      publish_x<false>(xr, v, rs, opaque(OFF_X + (layer ? 3 : 1) * XBUF + rtile * XT), c, lane, nullptr);
    }
    if (layer == 0) {
      ldw_a<31, 40>(w2, rs, l16, opaque(OFF_W2 + (c * 196 + w * 49) * 1024));
      WAIT_VM(9);
    } else WAIT_VM(0);
    BARRIER();
    if (tid < 16) __hip_atomic_fetch_add(ctrl + (g * 16 + tid) * 128, 1u, RLX_AGENT);
    STAMP(7 + 16 * layer);
    if (layer == 0) dma<14>(rs, l16, opaque(OFF_WQ + (j * 84 + w * 14) * 1024), opaque(L_WKV + w * 14 * 1024));
    gcount += 16;
    if (tid == 0) poll_ge(my_grp, gcount, tmo);
    WAIT_VM(0);
    BARRIER();
    STAMP(8 + 16 * layer);
    RELANE();
    float rstd1;
    {
      const unsigned xc = opaque(OFF_X + (layer ? 3 : 1) * XBUF + (8 * g + 2 * w) * XT);
      const unsigned xrb = opaque(OFF_X + (layer ? 3 : 1) * XBUF + rtile * XT + 14 * 1024);
      v2u sa[3], sb[3];
#pragma unroll
      for (int tt = 0; tt < 2; ++tt) {
        sa[tt] = ld8(rs, lst, xc, tt * XT + 14 * 1024);
        sb[tt] = ld8(rs, lst, xc, tt * XT + 14 * 1024 + 128);
      }
      sa[2] = ld8(rs, lst, xrb, 0); sb[2] = ld8(rs, lst, xrb, 128);
#pragma unroll
      for (int s = 0; s < 14; ++s)
#pragma unroll
        for (int tt = 0; tt < 2; ++tt) { xf[tt][s] = ld16(rs, l16, xc, tt * XT + s * 1024); SCHED_FENCE(); }
      v4u wf[2][4];
#pragma unroll
      for (int n = 0; n < 4; ++n) wf[0][n] = LFRAG(w1_slot(n));
      SCHED_FENCE();
      float mean[2];
      mean[0] = finish_stats(sa[0], sb[0]).mean; mean[1] = finish_stats(sa[1], sb[1]).mean;
      rstd1 = finish_stats(sa[2], sb[2]).rstd;
      const LAS float* cs = (const LAS float*)(L + L_CS1);
      const unsigned hb = opaque(OFF_H + layer * HBUF + (8 * g + 2 * w) * HT + j * 3584);
      f4 acc[2][7];
#pragma unroll
      for (int n = 4; n < 7; ++n) {
        const f4 cv = *(const LAS f4*)(cs + 16 * n + 4 * q);
        acc[0][n] = cv * -mean[0]; acc[1][n] = cv * -mean[1];
      }
      SCHED_FENCE();
#pragma unroll
      for (int s = 0; s < 14; ++s)
#pragma unroll
        for (int n = 0; n < 4; ++n) {
          if (s + 1 < 14) wf[(s + 1) & 1][n] = LFRAG(w1_slot((s + 1) * 7 + n));
          else if (n < 3) wf[0][n] = LFRAG(w1_slot(4 + n));
#pragma unroll
          for (int tt = 0; tt < 2; ++tt) {
            if (s == 0) mfma0(acc[tt][n], wf[s & 1][n], xf[tt][s]); else mfma(acc[tt][n], wf[s & 1][n], xf[tt][s]);
          }
          SCHED_FENCE();
        }
      if (layer == 0) ldw_a<40, 49>(w2, rs, l16, opaque(OFF_W2 + (c * 196 + w * 49) * 1024));
      SCHED_FENCE();
#pragma unroll
      for (int s = 0; s < 14; ++s) {
#pragma unroll
        for (int n = 0; n < 3; ++n) {
          if (s + 1 < 14) wf[(s + 1) & 1][n] = LFRAG(w1_slot((s + 1) * 7 + 4 + n));
#pragma unroll
          for (int tt = 0; tt < 2; ++tt) mfma(acc[tt][4 + n], wf[s & 1][n], xf[tt][s]);
          if (s >= 2 && s < 10 && n < 2) {
            const int u = (s - 2) >> 1, tt = u >> 1, p = u & 1, nn = 2 * p + (s & 1);
            if (n == 0) {
              const f4 cv = *(const LAS f4*)(cs + 16 * nn + 4 * q);
              acc[tt][nn] = acc[tt][nn] - mean[tt] * cv;
            } else if (s & 1) {
              v4u hv = pk8(acc[tt][2 * p], acc[tt][2 * p + 1]);
              hv = v4u{relu2(hv.x), relu2(hv.y), relu2(hv.z), relu2(hv.w)};
              st16(hv, rs, l16, hb, tt * HT + p * 1024);
            }
          }
          SCHED_FENCE();
        }
      }
      mfence(acc[0][4], acc[0][5], acc[0][6], acc[1][4], acc[1][5], acc[1][6]);
#pragma unroll
      for (int tt = 0; tt < 2; ++tt) {
        v4u hv = pk8(acc[tt][4], acc[tt][5]);
        hv = v4u{relu2(hv.x), relu2(hv.y), relu2(hv.z), relu2(hv.w)};
        st16(hv, rs, l16, hb, tt * HT + 2048);
      }
      {
        const unsigned a0 = relu2(pk2(acc[0][6].x, acc[0][6].y)), a1 = relu2(pk2(acc[0][6].z, acc[0][6].w));
        const unsigned b0 = relu2(pk2(acc[1][6].x, acc[1][6].y)), b1 = relu2(pk2(acc[1][6].z, acc[1][6].w));
        auto s0 = __builtin_amdgcn_permlane16_swap(a0, b0, false, false);
        auto s1 = __builtin_amdgcn_permlane16_swap(a1, b1, false, false);
        st16(v4u{s0[0], s1[0], s0[1], s1[1]}, rs, (q & 1) * HT + (q >> 1) * 256 + t * 16, hb, 3072);
      }
    }
    WAIT_VM(0);
    BARRIER();
    if (tid < 16) __hip_atomic_fetch_add(ctrl + (g * 16 + tid) * 128, 1u, RLX_AGENT);
    STAMP(9 + 16 * layer);
    if (layer == 0) dma<7>(rs, l16, opaque(OFF_WQ + (j * 84 + 56 + w * 7) * 1024), opaque(L_WQ + w * 7 * 1024));
    gcount += 16;
    if (tid == 0) poll_ge(my_grp, gcount, tmo);
    BARRIER();
    STAMP(10 + 16 * layer);
    RELANE();
    {
      constexpr int WIN = 7;
      const unsigned hb = opaque(OFF_H + layer * HBUF + (8 * g + 4 * hf) * HT + w * 14 * 1024);
      v4u hw[WIN][4];
#pragma unroll
      for (int s = 0; s < WIN; ++s)
#pragma unroll
        for (int tt = 0; tt < 4; ++tt) { hw[s][tt] = ld16(rs, l16, hb, tt * HT + s * 1024); SCHED_FENCE(); }
      SCHED_FENCE();
      f4 acc[4][5];
#pragma unroll
      for (int s = 0; s < 14; ++s) {
#pragma unroll
        for (int tt = 0; tt < 4; ++tt) {
          const v4u cur = hw[s % WIN][tt];
#pragma unroll
          for (int n = 0; n < 3; ++n) {
            if (s == 0) mfma0_a(acc[tt][n], w2[s * 3 + n], cur); else mfma_a(acc[tt][n], w2[s * 3 + n], cur);
          }
          if (s < 2) mfma0_a(acc[tt][3 + (s & 1)], w2[42 + (s >> 1)], cur); else mfma_a(acc[tt][3 + (s & 1)], w2[42 + (s >> 1)], cur);
          SCHED_FENCE();
          if (s + WIN < 14) hw[s % WIN][tt] = ld16(rs, l16, hb, tt * HT + (s + WIN) * 1024);
          SCHED_FENCE();
        }
      }
#pragma unroll
      for (int tt = 0; tt < 4; ++tt) mfence(acc[tt][0], acc[tt][1], acc[tt][2], acc[tt][3], acc[tt][4]);
      LAS f4* red = (LAS f4*)(L + l16);
#pragma unroll
      for (int tt = 0; tt < 4; ++tt) {
#pragma unroll
        for (int r = 0; r < 4; ++r) acc[tt][3][r] = q < 2 ? acc[tt][3][r] : acc[tt][4][r];
      }
      f4 v[4];
#pragma unroll
      for (int tt = 0; tt < 4; ++tt) {
        if (tt != w) {
          const int slot = (tt * 3 + ((w - tt - 1) & 3)) * 4;
#pragma unroll
          for (int n = 0; n < 4; ++n) red[(slot + n) * 64] = acc[tt][n];
        } else {
#pragma unroll
          for (int n = 0; n < 4; ++n) v[n] = acc[tt][n];
        }
      }
      BARRIER();
#pragma unroll
      for (int i = 0; i < 3; ++i)
#pragma unroll
        for (int n = 0; n < 4; ++n) v[n] += red[((w * 3 + i) * 4 + n) * 64];
#pragma unroll
      for (int r = 0; r < 4; ++r) v[3][r] = halfsum(v[3][r]);
#pragma unroll
      for (int n = 0; n < 4; ++n) v[n] *= rstd1;
      if (layer == 0) {
        publish_x<false>(xr, v, rs, opaque(OFF_X + 2 * XBUF + rtile * XT), c, lane, nullptr);
        WAIT_VM(0);
        BARRIER();
        if (tid < 16) __hip_atomic_fetch_add(ctrl + (g * 16 + tid) * 128, 1u, RLX_AGENT);
      } else {
        publish_x<true>(xr, v, rs, 0, c, lane, (GAS unsigned char*)out + ((16 * rtile + t) * (D_ * 2) + (56 * c + 4 * q) * 2));
      }
    }
    STAMP(11 + 16 * layer);
  }
}

extern "C" void kernel_launch(void* const* d_in, const int* in_sizes, int n_in, void* d_out, int out_size, void* d_ws, size_t ws_size,
                              hipStream_t stream) {
  const uint16_t* x = (const uint16_t*)d_in[0];
  (void)hipFuncSetAttribute((const void*)k_main, hipFuncAttributeMaxDynamicSharedMemorySize, LDS_BYTES);
  k_prep<<<256, PREP_WAVES * 64, 0, stream>>>(x, (const uint16_t*)d_in[1], (const uint16_t*)d_in[2], (const uint16_t*)d_in[4],
                                              (const uint16_t*)d_in[5], (unsigned char*)d_ws);
  k_main<<<256, 256, LDS_BYTES, stream>>>(x, (uint16_t*)d_out, (unsigned char*)d_ws);
}
```

```cpp
#include <hip/hip_runtime.h>
#include <hip/hip_bf16.h>
#include <stdint.h>

constexpr int LDS_BYTES = 160 * 1024;

__global__ void __launch_bounds__(256, 1) k_main(const void* xin, void* out, void* ws, const void* wqkv, const void* wo, const void* w1, const void* w2) {
  const unsigned w = __builtin_amdgcn_readfirstlane(threadIdx.x >> 6);
  asm volatile(
"; prologue\n"
"s_mov_b64 s[20:21], %0\n"
"s_and_b32 s21, s21, 0xffff\n"
"s_mov_b32 s22, 0x2691000\n"
"s_mov_b32 s23, 0x20000\n"
"s_mov_b32 s49, 0x40100\n"
"buffer_load_dwordx2 v[190:191], off, s[20:23], s49 sc1\n"
"s_mov_b32 s48, %3\n"
"s_mov_b32 s96, %4\n"
"s_bfe_u32 s34, s48, 0x40003\n"
"s_and_b32 s97, s48, 7\n"
"s_lshl_b32 s97, s97, 1\n"
"s_lshr_b32 s49, s48, 7\n"
"s_add_u32 s97, s97, s49\n"
"s_lshr_b32 s36, s34, 3\n"
"s_and_b32 s35, s34, 7\n"
"s_lshl_b32 s67, s96, 10\n"
"v_mbcnt_lo_u32_b32 v182, -1, 0\n"
"v_mbcnt_hi_u32_b32 v182, -1, v182\n"
"v_lshlrev_b32 v0, 4, v182\n"
"v_lshrrev_b32 v183, 4, v182\n"
"v_and_b32 v184, 15, v182\n"
"s_mov_b64 s[24:25], %1\n"
"s_and_b32 s25, s25, 0xffff\n"
"s_mov_b32 s26, 0x1c0000\n"
"s_mov_b32 s27, 0x20000\n"
"s_lshl_b32 m0, s96, 13\n"
"s_mov_b32 s61, 0\n"
"s_cmp_lt_u32 s96, 2\n"
"s_cbranch_scc1 .Lkd_10\n"
"v_and_b32 v185, 7, v182\n"
"v_lshrrev_b32 v186, 3, v182\n"
"v_mul_u32_u24 v66, 0x380, v185\n"
"v_mul_u32_u24 v67, 0x70, v186\n"
"v_add_u32 v66, v66, v67\n"
"s_lshl_b32 s48, s97, 4\n"
"s_add_u32 s48, s48, s34\n"
"s_mul_i32 s48, s48, 0x1c00\n"
"buffer_load_dwordx4 v[38:41], v66, s[24:27], s48 offen offset:0\n"
"buffer_load_dwordx4 v[42:45], v66, s[24:27], s48 offen offset:16\n"
"buffer_load_dwordx4 v[46:49], v66, s[24:27], s48 offen offset:32\n"
"buffer_load_dwordx4 v[50:53], v66, s[24:27], s48 offen offset:48\n"
"buffer_load_dwordx4 v[54:57], v66, s[24:27], s48 offen offset:64\n"
"buffer_load_dwordx4 v[58:61], v66, s[24:27], s48 offen offset:80\n"
"buffer_load_dwordx4 v[62:65], v66, s[24:27], s48 offen offset:96\n"
"s_branch .Lkj0_5\n"
".Lkd_10:\n"
"s_cmp_ge_u32 s97, 14\n"
"s_cbranch_scc1 .Lkj0_5\n"
"s_cmp_lg_u32 s96, 0\n"
"s_cbranch_scc1 .Lkj0_5\n"
"s_mov_b64 s[72:73], %5\n"
"s_and_b32 s73, s73, 0xffff\n"
"s_mov_b32 s74, 0x126000\n"
"s_mov_b32 s75, 0x20000\n"
"v_mul_u32_u24 v185, 0xa80, v183\n"
"v_lshrrev_b32 v186, 2, v184\n"
"v_min_u32 v186, 2, v186\n"
"v_mul_u32_u24 v186, 0x380, v186\n"
"v_and_b32 v187, 3, v184\n"
"v_lshl_add_u32 v185, v187, 4, v185\n"
"v_add_u32 v68, v185, v186\n"
"s_mul_i32 s48, s97, 0x15000\n"
"s_mul_i32 s49, s34, 56\n"
"s_add_u32 s48, s48, s49\n"
"s_mov_b32 s61, 4\n"
"buffer_load_dwordx4 v68, s[72:75], s48 offen lds\n"
"s_add_u32 m0, m0, 0x400\n"
"s_add_u32 s48, s48, 0x2a00\n"
"buffer_load_dwordx4 v68, s[72:75], s48 offen lds\n"
"s_add_u32 m0, m0, 0x400\n"
"s_add_u32 s48, s48, 0x2a00\n"
"buffer_load_dwordx4 v68, s[72:75], s48 offen lds\n"
"s_add_u32 m0, m0, 0x400\n"
"s_add_u32 s48, s48, 0x2a00\n"
"buffer_load_dwordx4 v68, s[72:75], s48 offen lds\n"
"s_add_u32 m0, m0, 0x400\n"
"s_add_u32 s48, s48, 0x2a00\n"
"buffer_load_dwordx4 v68, s[72:75], s48 offen lds\n"
"s_add_u32 m0, m0, 0x400\n"
"s_add_u32 s48, s48, 0x2a00\n"
"buffer_load_dwordx4 v68, s[72:75], s48 offen lds\n"
"s_add_u32 m0, m0, 0x400\n"
"s_add_u32 s48, s48, 0x2a00\n"
"buffer_load_dwordx4 v68, s[72:75], s48 offen lds\n"
"s_add_u32 m0, m0, 0x400\n"
"s_add_u32 s48, s48, 0x2a00\n"
"buffer_load_dwordx4 v68, s[72:75], s48 offen lds\n"
"s_add_u32 m0, m0, 0x400\n"
"s_add_u32 s48, s48, 0x2a00\n"
".Lkj0_5:\n"
"s_mov_b64 s[28:29], %2\n"
"s_and_b32 s29, s29, 0xffff\n"
"s_mov_b32 s30, 0x1c0000\n"
"s_mov_b32 s31, 0x20000\n"
"s_lshl_b32 s37, s97, 3\n"
"s_lshl_b32 s63, s36, 2\n"
"s_add_u32 s37, s37, s63\n"
"s_add_u32 s37, s37, s96\n"
"s_mov_b32 s38, -1\n"
"s_mov_b32 s39, 0\n"
"s_mov_b32 s40, -1\n"
"s_mov_b32 s41, 0xffff\n"
"s_mov_b32 s42, 0x10001000\n"
"s_mov_b32 s43, 0x10001000\n"
"s_lshl_b32 s52, s97, 14\n"
"s_lshl_b32 s53, s34, 10\n"
"s_add_u32 s53, s53, s52\n"
"s_lshr_b32 s68, s96, 1\n"
"s_lshl_b32 s68, s68, 7\n"
"s_add_u32 s68, s68, s53\n"
"s_lshr_b32 s69, s35, 1\n"
"s_lshl_b32 s69, s69, 8\n"
"s_and_b32 s63, s35, 1\n"
"s_lshl_b32 s63, s63, 3\n"
"s_add_u32 s69, s69, s63\n"
"s_add_u32 s69, s69, 0x3800\n"
"s_mul_i32 s64, s34, 0x15000\n"
"s_add_u32 s64, s64, 0x41000\n"
"s_mul_i32 s65, s34, 0x18800\n"
"s_add_u32 s65, s65, 0x191000\n"
"s_mul_i32 s66, s35, 0xe000\n"
"s_add_u32 s66, s66, 0x319000\n"
"v_add_u32 v1, 0x10000, v0\n"
"v_add_u32 v2, 0x20000, v0\n"
"v_lshlrev_b32 v36, 4, v184\n"
"v_and_b32 v185, 1, v183\n"
"v_lshl_add_u32 v36, v185, 2, v36\n"
"v_lshlrev_b32 v4, 4, v183\n"
"v_add_u32 v4, 0x27000, v4\n"
"v_lshlrev_b32 v5, 2, v184\n"
"v_add_u32 v5, 0x27000, v5\n"
"s_lshl_b32 s63, s96, 8\n"
"s_add_u32 s63, s63, 0x400\n"
"v_add_u32 v7, s63, v4\n"
"v_and_b32 v6, 31, v182\n"
"v_lshlrev_b32 v6, 2, v6\n"
"v_add_u32 v6, 0x27000, v6\n"
"v_add_u32 v6, s63, v6\n"
"v_mul_u32_u24 v8, 0xe000, v185\n"
"v_lshrrev_b32 v186, 1, v183\n"
"v_lshl_add_u32 v8, v186, 8, v8\n"
"v_lshl_add_u32 v8, v184, 4, v8\n"
"v_mov_b32 v10, 1\n"
"v_lshlrev_b32 v11, 10, v182\n"
"v_mov_b32 v37, 0x3b124925\n"
"s_mul_i32 s63, s37, 0x3800\n"
"s_mul_i32 s51, s35, 0x70\n"
"s_add_u32 s63, s63, s51\n"
"v_mul_u32_u24 v188, 0x380, v184\n"
"v_add_u32 v188, s63, v188\n"
"v_lshl_add_u32 v9, v183, 4, v188\n"
"v_lshl_add_u32 v187, v185, 4, v188\n"
"v_lshlrev_b32 v194, 4, v183\n"
"v_min_u32 v194, 40, v194\n"
"v_add_u32 v194, v194, v188\n"
"s_mov_b32 s54, 0x389000\n"
"s_mul_i32 s51, s35, 0x31000\n"
"s_add_u32 s54, s54, s51\n"
"s_mul_i32 s51, s96, 0xc400\n"
"s_add_u32 s54, s54, s51\n"
"v_lshl_add_u32 v188, v183, 2, 0\n"
"v_cmp_le_u32 vcc, v188, v184\n"
"v_mov_b32 v189, 0xf149f2ca\n"
"s_nop 0\n"
"v_cndmask_b32_e64 v12, v189, 0, vcc\n"
"v_cndmask_b32_e64 v16, 0, v189, vcc\n"
"v_lshl_add_u32 v188, v183, 2, 1\n"
"v_cmp_le_u32 vcc, v188, v184\n"
"v_mov_b32 v189, 0xf149f2ca\n"
"s_nop 0\n"
"v_cndmask_b32_e64 v13, v189, 0, vcc\n"
"v_cndmask_b32_e64 v17, 0, v189, vcc\n"
"v_lshl_add_u32 v188, v183, 2, 2\n"
"v_cmp_le_u32 vcc, v188, v184\n"
"v_mov_b32 v189, 0xf149f2ca\n"
"s_nop 0\n"
"v_cndmask_b32_e64 v14, v189, 0, vcc\n"
"v_cndmask_b32_e64 v18, 0, v189, vcc\n"
"v_lshl_add_u32 v188, v183, 2, 3\n"
"v_cmp_le_u32 vcc, v188, v184\n"
"v_mov_b32 v189, 0xf149f2ca\n"
"s_nop 0\n"
"v_cndmask_b32_e64 v15, v189, 0, vcc\n"
"v_cndmask_b32_e64 v19, 0, v189, vcc\n"
"v_bfe_u32 v192, v182, 2, 2\n"
"v_and_b32 v193, 3, v182\n"
"buffer_load_dwordx4 v[28:31], v9, s[24:27], 0 offen\n"
"buffer_load_dwordx2 v[32:33], v194, s[24:27], 0 offen offset:64\n"
"buffer_load_dwordx2 v[34:35], v187, s[24:27], 0 offen offset:72\n"
"s_lshl_b32 s48, s34, 3\n"
"s_add_u32 s48, s48, s52\n"
"v_lshlrev_b32 v185, 10, v184\n"
"v_add_u32 v185, s48, v185\n"
"s_lshl_b32 s47, s34, 10\n"
"s_lshl_b32 s48, s97, 3\n"
"s_add_u32 s47, s47, s48\n"
"v_lshlrev_b32 v186, 14, v184\n"
"v_add_u32 v186, s47, v186\n"
"v_add_u32 v188, 0x80, v186\n"
"s_mov_b64 s[56:57], 0xffff\n"
"v_cndmask_b32_e64 v76, v188, v185, s[56:57]\n"
"v_and_b32 v188, 1, v183\n"
"v_lshlrev_b32 v189, 7, v188\n"
"v_add_u32 v186, v186, v189\n"
"s_xor_b32 s48, s34, 8\n"
"s_sub_i32 s48, s48, s34\n"
"s_lshl_b32 s48, s48, 10\n"
"v_mul_lo_u32 v188, s48, v188\n"
"v_add_u32 v254, v186, v188\n"
"s_cmp_ge_u32 s96, 2\n"
"s_cbranch_scc1 .Lkr3_13\n"
"s_cmp_lg_u32 s61, 4\n"
"s_cbranch_scc1 .Lke_19\n"
"v_lshl_add_u32 v185, v183, 3, v192\n"
"v_lshlrev_b32 v185, 8, v185\n"
"v_lshl_add_u32 v66, v193, 4, v185\n"
"s_lshl_b32 s45, s97, 12\n"
"s_add_u32 s45, s45, s64\n"
"s_lshl_b32 s44, s97, 11\n"
"s_add_u32 s44, s44, s64\n"
"s_add_u32 s44, s44, 0xe000\n"
"s_mov_b32 s56, 0xf000f000\n"
"s_mov_b32 s57, 0xf000f000\n"
"s_waitcnt vmcnt(3)\n"
"ds_read_b64_tr_b16 v[46:47], v66 offset:64\n"
"ds_read_b64_tr_b16 v[48:49], v66 offset:1088\n"
"ds_read_b64_tr_b16 v[54:55], v66 offset:128\n"
"ds_read_b64_tr_b16 v[56:57], v66 offset:1152\n"
"ds_read_b64_tr_b16 v[38:39], v66\n"
"ds_read_b64_tr_b16 v[40:41], v66 offset:1024\n"
"ds_read_b64_tr_b16 v[50:51], v66 offset:72\n"
"ds_read_b64_tr_b16 v[52:53], v66 offset:1096\n"
"ds_read_b64_tr_b16 v[58:59], v66 offset:136\n"
"ds_read_b64_tr_b16 v[60:61], v66 offset:1160\n"
"ds_read_b64_tr_b16 v[42:43], v66 offset:8\n"
"ds_read_b64_tr_b16 v[44:45], v66 offset:1032\n"
"s_waitcnt lgkmcnt(10)\n"
"buffer_store_dwordx4 v[46:49], v0, s[20:23], s45 offen sc1\n"
"s_waitcnt lgkmcnt(8)\n"
"buffer_store_dwordx4 v[54:57], v0, s[20:23], s45 offen offset:2048 sc1\n"
"s_waitcnt lgkmcnt(6)\n"
"buffer_store_dwordx4 v[38:41], v0, s[20:23], s44 offen sc1\n"
"s_waitcnt lgkmcnt(0)\n"
"s_mov_b64 exec, s[56:57]\n"
"v_mov_b32 v50, 0\n"
"v_mov_b32 v51, 0\n"
"v_mov_b32 v52, 0\n"
"v_mov_b32 v53, 0\n"
"v_mov_b32 v58, 0\n"
"v_mov_b32 v59, 0\n"
"v_mov_b32 v60, 0\n"
"v_mov_b32 v61, 0\n"
"v_mov_b32 v42, 0\n"
"v_mov_b32 v43, 0\n"
"v_mov_b32 v44, 0\n"
"v_mov_b32 v45, 0\n"
"s_mov_b64 exec, -1\n"
"buffer_store_dwordx4 v[50:53], v0, s[20:23], s45 offen offset:1024 sc1\n"
"buffer_store_dwordx4 v[58:61], v0, s[20:23], s45 offen offset:3072 sc1\n"
"buffer_store_dwordx4 v[42:45], v0, s[20:23], s44 offen offset:1024 sc1\n"
"s_branch .Lkei_21\n"
".Lkr3_13:\n"
"v_and_b32 v185, 7, v182\n"
"v_lshrrev_b32 v186, 3, v182\n"
"s_lshl_b32 s48, s97, 3\n"
"s_lshr_b32 s49, s34, 1\n"
"s_add_u32 s48, s48, s49\n"
"s_mul_i32 s48, s48, 0x3c00\n"
"s_and_b32 s49, s34, 1\n"
"s_lshl_b32 s49, s49, 7\n"
"s_add_u32 s48, s48, s49\n"
"s_add_u32 s48, s48, 0x511000\n"
"s_cmp_eq_u32 s96, 2\n"
"s_cbranch_scc1 .Lks_17\n"
"v_mul_u32_u24 v66, 0x700, v186\n"
"v_lshl_add_u32 v66, v185, 4, v66\n"
"v_mov_b32 v74, 0\n"
"v_lshlrev_b32 v67, 7, v182\n"
"s_mov_b64 exec, 13\n"
"buffer_store_dword v74, v67, s[20:23], s53 offen sc1\n"
"s_mov_b64 exec, -1\n"
"s_waitcnt vmcnt(4)\n"
"buffer_store_dwordx4 v[38:41], v66, s[20:23], s48 offen sc1\n"
"buffer_store_dwordx4 v[42:45], v66, s[20:23], s48 offen offset:256 sc1\n"
"buffer_store_dwordx4 v[46:49], v66, s[20:23], s48 offen offset:512 sc1\n"
"buffer_store_dwordx4 v[50:53], v66, s[20:23], s48 offen offset:768 sc1\n"
"buffer_store_dwordx4 v[54:57], v66, s[20:23], s48 offen offset:1024 sc1\n"
"buffer_store_dwordx4 v[58:61], v66, s[20:23], s48 offen offset:1280 sc1\n"
"buffer_store_dwordx4 v[62:65], v66, s[20:23], s48 offen offset:1536 sc1\n"
"s_branch .Lkei_21\n"
".Lks_17:\n"
"s_mov_b32 s50, 0x3f803f80\n"
"v_mov_b32 v72, 0\n"
"v_mov_b32 v73, 0\n"
"v_mov_b32 v70, 0\n"
"v_mov_b32 v71, 0\n"
"v_lshrrev_b32 v68, 1, v186\n"
"v_lshlrev_b32 v68, 8, v68\n"
"v_and_b32 v186, 1, v186\n"
"v_lshl_add_u32 v68, v186, 3, v68\n"
"v_lshl_add_u32 v68, v185, 4, v68\n"
"s_add_u32 s48, s48, 0x3800\n"
"s_waitcnt vmcnt(3)\n"
"v_dot2_f32_bf16 v72, v38, s50, v72\n"
"v_dot2_f32_bf16 v73, v38, v38, v73\n"
"v_dot2_f32_bf16 v70, v39, s50, v70\n"
"v_dot2_f32_bf16 v71, v39, v39, v71\n"
"v_dot2_f32_bf16 v72, v40, s50, v72\n"
"v_dot2_f32_bf16 v73, v40, v40, v73\n"
"v_dot2_f32_bf16 v70, v41, s50, v70\n"
"v_dot2_f32_bf16 v71, v41, v41, v71\n"
"v_dot2_f32_bf16 v72, v42, s50, v72\n"
"v_dot2_f32_bf16 v73, v42, v42, v73\n"
"v_dot2_f32_bf16 v70, v43, s50, v70\n"
"v_dot2_f32_bf16 v71, v43, v43, v71\n"
"v_dot2_f32_bf16 v72, v44, s50, v72\n"
"v_dot2_f32_bf16 v73, v44, v44, v73\n"
"v_dot2_f32_bf16 v70, v45, s50, v70\n"
"v_dot2_f32_bf16 v71, v45, v45, v71\n"
"v_dot2_f32_bf16 v72, v46, s50, v72\n"
"v_dot2_f32_bf16 v73, v46, v46, v73\n"
"v_dot2_f32_bf16 v70, v47, s50, v70\n"
"v_dot2_f32_bf16 v71, v47, v47, v71\n"
"v_dot2_f32_bf16 v72, v48, s50, v72\n"
"v_dot2_f32_bf16 v73, v48, v48, v73\n"
"v_dot2_f32_bf16 v70, v49, s50, v70\n"
"v_dot2_f32_bf16 v71, v49, v49, v71\n"
"v_dot2_f32_bf16 v72, v50, s50, v72\n"
"v_dot2_f32_bf16 v73, v50, v50, v73\n"
"v_dot2_f32_bf16 v70, v51, s50, v70\n"
"v_dot2_f32_bf16 v71, v51, v51, v71\n"
"v_dot2_f32_bf16 v72, v52, s50, v72\n"
"v_dot2_f32_bf16 v73, v52, v52, v73\n"
"v_dot2_f32_bf16 v70, v53, s50, v70\n"
"v_dot2_f32_bf16 v71, v53, v53, v71\n"
"v_dot2_f32_bf16 v72, v54, s50, v72\n"
"v_dot2_f32_bf16 v73, v54, v54, v73\n"
"v_dot2_f32_bf16 v70, v55, s50, v70\n"
"v_dot2_f32_bf16 v71, v55, v55, v71\n"
"v_dot2_f32_bf16 v72, v56, s50, v72\n"
"v_dot2_f32_bf16 v73, v56, v56, v73\n"
"v_dot2_f32_bf16 v70, v57, s50, v70\n"
"v_dot2_f32_bf16 v71, v57, v57, v71\n"
"v_dot2_f32_bf16 v72, v58, s50, v72\n"
"v_dot2_f32_bf16 v73, v58, v58, v73\n"
"v_dot2_f32_bf16 v70, v59, s50, v70\n"
"v_dot2_f32_bf16 v71, v59, v59, v71\n"
"v_dot2_f32_bf16 v72, v60, s50, v72\n"
"v_dot2_f32_bf16 v73, v60, v60, v73\n"
"v_dot2_f32_bf16 v70, v61, s50, v70\n"
"v_dot2_f32_bf16 v71, v61, v61, v71\n"
"v_dot2_f32_bf16 v72, v62, s50, v72\n"
"v_dot2_f32_bf16 v73, v62, v62, v73\n"
"v_dot2_f32_bf16 v70, v63, s50, v70\n"
"v_dot2_f32_bf16 v71, v63, v63, v71\n"
"v_dot2_f32_bf16 v72, v64, s50, v72\n"
"v_dot2_f32_bf16 v73, v64, v64, v73\n"
"v_dot2_f32_bf16 v70, v65, s50, v70\n"
"v_dot2_f32_bf16 v71, v65, v65, v71\n"
"s_nop 1\n"
"v_add_f32 v72, v72, v70\n"
"v_add_f32 v73, v73, v71\n"
"buffer_store_dwordx2 v[72:73], v68, s[20:23], s48 offen sc1\n"
"s_cmp_ge_u32 s97, 14\n"
"s_cbranch_scc1 .Lkei_21\n"
"s_mov_b64 s[72:73], %8\n"
"s_and_b32 s73, s73, 0xffff\n"
"s_mov_b32 s74, 0x188000\n"
"s_mov_b32 s75, 0x20000\n"
"s_lshl_b32 s62, s97, 1\n"
"s_add_u32 s62, s62, s36\n"
"s_mul_i32 s48, s62, 0xe000\n"
"s_mov_b32 s47, 0\n"
"s_branch .Lkt2b_3\n"
".Lke_19:\n"
"s_cmp_ge_u32 s97, 14\n"
"s_cbranch_scc1 .Lkwo_8\n"
"s_mov_b64 s[72:73], %7\n"
"s_and_b32 s73, s73, 0xffff\n"
"s_mov_b32 s74, 0x188000\n"
"s_mov_b32 s75, 0x20000\n"
"v_mul_u32_u24 v185, 0xe00, v183\n"
"v_lshl_add_u32 v68, v184, 4, v185\n"
"s_mul_i32 s48, s97, 0x1c000\n"
"s_mul_i32 s49, s34, 0xe0\n"
"s_mov_b32 s60, 0x3800\n"
"s_mov_b32 s47, 0\n"
"s_mov_b32 s61, 1\n"
"s_branch .Lke1_20\n"
".Lkwo_8:\n"
"s_mov_b64 s[72:73], %6\n"
"s_and_b32 s73, s73, 0xffff\n"
"s_mov_b32 s74, 0x62000\n"
"s_mov_b32 s75, 0x20000\n"
"s_sub_u32 s62, s97, 14\n"
"s_lshl_b32 s62, s62, 1\n"
"s_add_u32 s62, s62, s36\n"
"s_lshl_b32 s62, s62, 1\n"
"s_add_u32 s62, s62, s96\n"
"s_mul_i32 s48, s62, 0xc400\n"
"s_mov_b32 s47, 0xe00\n"
".Lkt2b_3:\n"
"v_lshrrev_b32 v185, 3, v182\n"
"v_mul_u32_u24 v185, 0x380, v185\n"
"v_and_b32 v186, 7, v182\n"
"v_lshl_add_u32 v68, v186, 4, v185\n"
"s_mul_i32 s49, s35, 0x70\n"
"s_mov_b32 s60, 0x1c00\n"
"s_mov_b32 s61, 2\n"
".Lke1_20:\n"
"s_add_u32 s48, s48, s49\n"
"s_barrier\n"
"buffer_load_dwordx4 v68, s[72:75], s48 offen lds\n"
"s_add_u32 m0, m0, 0x400\n"
"s_add_u32 s48, s48, s60\n"
"buffer_load_dwordx4 v68, s[72:75], s48 offen lds\n"
"s_add_u32 m0, m0, 0x400\n"
"s_add_u32 s48, s48, s60\n"
"buffer_load_dwordx4 v68, s[72:75], s48 offen lds\n"
"s_add_u32 m0, m0, 0x400\n"
"s_add_u32 s48, s48, s60\n"
"buffer_load_dwordx4 v68, s[72:75], s48 offen lds\n"
"s_add_u32 m0, m0, 0x400\n"
"s_add_u32 s48, s48, s60\n"
"s_sub_u32 s48, s48, s47\n"
"buffer_load_dwordx4 v68, s[72:75], s48 offen lds\n"
"s_add_u32 m0, m0, 0x400\n"
"s_add_u32 s48, s48, s60\n"
"buffer_load_dwordx4 v68, s[72:75], s48 offen lds\n"
"s_add_u32 m0, m0, 0x400\n"
"s_add_u32 s48, s48, s60\n"
"buffer_load_dwordx4 v68, s[72:75], s48 offen lds\n"
"s_add_u32 m0, m0, 0x400\n"
"s_add_u32 s48, s48, s60\n"
"buffer_load_dwordx4 v68, s[72:75], s48 offen lds\n"
"s_add_u32 m0, m0, 0x400\n"
"s_add_u32 s48, s48, s60\n"
"s_waitcnt vmcnt(8)\n"
"s_branch .Lkgo_15\n"
".Lkei_21:\n"
"s_barrier\n"
"s_waitcnt vmcnt(0)\n"
".Lkgo_15:\n"
"v_readfirstlane_b32 s70, v190\n"
"v_readfirstlane_b32 s71, v191\n"
"s_add_u32 s70, s70, 1\n"
"s_addc_u32 s71, s71, 0\n"
"v_mov_b32 v252, s70\n"
"v_mov_b32 v253, s71\n"
"s_barrier\n"
"s_cmp_lg_u32 s96, 0\n"
"s_cbranch_scc1 .Lkw0_22\n"
"s_mov_b64 exec, s[38:39]\n"
"buffer_store_dwordx2 v[252:253], v76, s[20:23], 0 offen offset:512 sc1\n"
"s_mov_b64 exec, -1\n"
".Lkw0_22:\n"
"s_mov_b32 s44, 0x511000\n"
"s_mul_i32 s51, s97, 0x1e000\n"
"s_add_u32 s44, s44, s51\n"
"s_mul_i32 s51, s96, 0x7800\n"
"s_add_u32 s44, s44, s51\n"
"s_add_u32 s45, s44, 0x3c00\n"
"s_add_u32 s49, s44, 0x3800\n"
"s_add_u32 s50, s45, 0x3800\n"
"v_lshrrev_b32 v185, 1, v0\n"
"v_and_b32 v185, 0xff, v185\n"
"buffer_load_dwordx2 v[190:191], v185, s[20:23], s53 offen offset:512 sc1\n"
"s_mov_b32 s55, 0\n"
"s_waitcnt vmcnt(0)\n"
".Lktpoll_23:\n"
"v_cmp_ne_u64 vcc, s[70:71], v[190:191]\n"
"s_add_u32 s55, s55, 1\n"
"s_nop 0\n"
"s_cbranch_vccz .Lktdone_24\n"
"buffer_load_dwordx2 v[190:191], v185, s[20:23], s53 offen offset:512 sc1\n"
"s_cmp_lt_u32 s55, 0x40000\n"
"s_waitcnt vmcnt(0)\n"
"s_cbranch_scc1 .Lktpoll_23\n"
"s_mov_b32 s48, 0x40000\n"
"buffer_store_dword v10, off, s[20:23], s48 sc1\n"
".Lktdone_24:\n"
"; A1 layer 0\n"
"s_add_u32 m0, s67, 0x12000\n"
"s_add_u32 s60, s67, s64\n"
"buffer_load_dwordx4 v0, s[20:23], s60 offen sc1 lds\n"
"s_add_u32 m0, m0, 0x1000\n"
"s_add_u32 s60, s60, 0x1000\n"
"buffer_load_dwordx4 v0, s[20:23], s60 offen sc1 lds\n"
"s_add_u32 m0, m0, 0x1000\n"
"s_add_u32 s60, s60, 0x1000\n"
"buffer_load_dwordx4 v0, s[20:23], s60 offen sc1 lds\n"
"s_add_u32 m0, m0, 0x1000\n"
"s_add_u32 s60, s60, 0x1000\n"
"buffer_load_dwordx4 v0, s[20:23], s60 offen sc1 lds\n"
"s_add_u32 m0, m0, 0x1000\n"
"s_add_u32 s60, s60, 0x1000\n"
"buffer_load_dwordx4 v0, s[20:23], s60 offen sc1 lds\n"
"s_add_u32 m0, m0, 0x1000\n"
"s_add_u32 s60, s60, 0x1000\n"
"buffer_load_dwordx4 v0, s[20:23], s60 offen sc1 lds\n"
"buffer_load_dwordx4 v[38:41], v0, s[20:23], s44 offen sc1\n"
"buffer_load_dwordx4 v[42:45], v0, s[20:23], s45 offen sc1\n"
"buffer_load_dwordx4 v[206:209], v0, s[20:23], s49 offen sc1\n"
"buffer_load_dwordx4 v[210:213], v0, s[20:23], s50 offen sc1\n"
"s_add_u32 m0, m0, 0x1000\n"
"s_add_u32 s60, s60, 0x1000\n"
"buffer_load_dwordx4 v0, s[20:23], s60 offen sc1 lds\n"
"buffer_load_dwordx4 v[46:49], v0, s[20:23], s44 offen offset:1024 sc1\n"
"buffer_load_dwordx4 v[50:53], v0, s[20:23], s45 offen offset:1024 sc1\n"
"s_add_u32 m0, m0, 0x1000\n"
"s_add_u32 s60, s60, 0x1000\n"
"buffer_load_dwordx4 v0, s[20:23], s60 offen sc1 lds\n"
"buffer_load_dwordx4 v[54:57], v0, s[20:23], s44 offen offset:2048 sc1\n"
"buffer_load_dwordx4 v[58:61], v0, s[20:23], s45 offen offset:2048 sc1\n"
"s_add_u32 m0, m0, 0x1000\n"
"s_add_u32 s60, s60, 0x1000\n"
"buffer_load_dwordx4 v0, s[20:23], s60 offen sc1 lds\n"
"buffer_load_dwordx4 v[62:65], v0, s[20:23], s44 offen offset:3072 sc1\n"
"buffer_load_dwordx4 v[66:69], v0, s[20:23], s45 offen offset:3072 sc1\n"
"s_add_u32 m0, m0, 0x1000\n"
"s_add_u32 s60, s60, 0x1000\n"
"buffer_load_dwordx4 v0, s[20:23], s60 offen sc1 lds\n"
"s_cmp_eq_u32 s61, 2\n"
"s_cbranch_scc1 .Lkr2_12\n"
"s_cmp_lg_u32 s61, 1\n"
"s_cbranch_scc1 .Lkpo_16\n"
"v_lshl_add_u32 v185, v183, 3, v192\n"
"v_lshlrev_b32 v185, 8, v185\n"
"v_add_u32 v185, 0x2000, v185\n"
"v_lshl_add_u32 v178, v193, 4, v185\n"
"v_lshl_add_u32 v179, v193, 3, v185\n"
"s_mul_i32 s58, s97, 0x1c00\n"
"s_add_u32 s58, s58, s65\n"
"s_add_u32 s47, s58, 0x1800\n"
"s_nop 0\n"
"ds_read_b64_tr_b16 v[150:151], v178\n"
"ds_read_b64_tr_b16 v[152:153], v178 offset:1024\n"
"ds_read_b64_tr_b16 v[154:155], v178 offset:8\n"
"ds_read_b64_tr_b16 v[156:157], v178 offset:1032\n"
"ds_read_b64_tr_b16 v[158:159], v178 offset:64\n"
"ds_read_b64_tr_b16 v[160:161], v178 offset:1088\n"
"ds_read_b64_tr_b16 v[162:163], v178 offset:72\n"
"ds_read_b64_tr_b16 v[164:165], v178 offset:1096\n"
"ds_read_b64_tr_b16 v[166:167], v178 offset:128\n"
"ds_read_b64_tr_b16 v[168:169], v178 offset:1152\n"
"ds_read_b64_tr_b16 v[170:171], v178 offset:136\n"
"ds_read_b64_tr_b16 v[172:173], v178 offset:1160\n"
"ds_read_b64_tr_b16 v[174:175], v179 offset:192\n"
"ds_read_b64_tr_b16 v[176:177], v179 offset:1216\n"
"s_waitcnt lgkmcnt(0)\n"
"s_branch .Lkj1_6\n"
".Lkr2_12:\n"
"v_lshl_add_u32 v185, v183, 3, v192\n"
"s_lshl_b32 s48, s96, 13\n"
"v_lshlrev_b32 v185, 7, v185\n"
"v_add_u32 v185, s48, v185\n"
"v_lshl_add_u32 v178, v193, 4, v185\n"
"v_lshlrev_b32 v188, 4, v193\n"
"v_min_u32 v188, 40, v188\n"
"v_add_u32 v180, v188, v185\n"
"v_lshrrev_b32 v188, 1, v193\n"
"v_lshl_add_u32 v185, v188, 12, v185\n"
"v_and_b32 v188, 1, v193\n"
"v_lshl_add_u32 v179, v188, 4, v185\n"
"s_nop 0\n"
"ds_read_b64_tr_b16 v[150:151], v178\n"
"ds_read_b64_tr_b16 v[152:153], v178 offset:512\n"
"ds_read_b64_tr_b16 v[154:155], v178 offset:8\n"
"ds_read_b64_tr_b16 v[156:157], v178 offset:520\n"
"ds_read_b64_tr_b16 v[158:159], v180 offset:64\n"
"ds_read_b64_tr_b16 v[160:161], v180 offset:576\n"
"ds_read_b64_tr_b16 v[162:163], v178 offset:4096\n"
"ds_read_b64_tr_b16 v[164:165], v178 offset:4608\n"
"ds_read_b64_tr_b16 v[166:167], v178 offset:4104\n"
"ds_read_b64_tr_b16 v[168:169], v178 offset:4616\n"
"ds_read_b64_tr_b16 v[170:171], v180 offset:4160\n"
"ds_read_b64_tr_b16 v[172:173], v180 offset:4672\n"
"ds_read_b64_tr_b16 v[174:175], v179 offset:72\n"
"ds_read_b64_tr_b16 v[176:177], v179 offset:584\n"
"s_mul_i32 s58, s62, 0x1800\n"
"s_add_u32 s58, s58, s66\n"
"s_lshl_b32 s47, s62, 10\n"
"s_add_u32 s47, s47, s66\n"
"s_add_u32 s47, s47, 0xc000\n"
"s_waitcnt lgkmcnt(0)\n"
"s_cmp_eq_u32 s96, 2\n"
"s_cbranch_scc1 .Lkr2b_14\n"
"s_andn2_b64 exec, exec, s[40:41]\n"
"v_mov_b32 v152, 0\n"
"v_mov_b32 v153, 0\n"
"v_mov_b32 v156, 0\n"
"v_mov_b32 v157, 0\n"
"v_mov_b32 v160, 0\n"
"v_mov_b32 v161, 0\n"
"v_mov_b32 v164, 0\n"
"v_mov_b32 v165, 0\n"
"v_mov_b32 v168, 0\n"
"v_mov_b32 v169, 0\n"
"v_mov_b32 v172, 0\n"
"v_mov_b32 v173, 0\n"
"v_mov_b32 v176, 0\n"
"v_mov_b32 v177, 0\n"
"s_mov_b64 exec, -1\n"
"s_branch .Lkj1_6\n"
".Lkr2b_14:\n"
"s_mul_i32 s48, s62, 0x2493\n"
"s_lshr_b32 s48, s48, 16\n"
"s_mul_i32 s49, s48, 7\n"
"s_sub_u32 s49, s62, s49\n"
"s_mul_i32 s48, s48, 49\n"
"s_mul_i32 s50, s35, 0xc4\n"
"s_add_u32 s48, s48, s50\n"
"s_lshl_b32 s48, s48, 10\n"
"s_add_u32 s48, s48, 0x389000\n"
"s_mul_i32 s58, s49, 0x1800\n"
"s_add_u32 s58, s58, s48\n"
"s_lshl_b32 s47, s49, 10\n"
"s_add_u32 s47, s47, s48\n"
"s_add_u32 s47, s47, 0xa800\n"
".Lkj1_6:\n"
"s_add_u32 s59, s58, 0x1000\n"
"buffer_store_dwordx4 v[150:153], v0, s[20:23], s58 offen sc1\n"
"buffer_store_dwordx4 v[154:157], v0, s[20:23], s58 offen offset:1024 sc1\n"
"buffer_store_dwordx4 v[158:161], v0, s[20:23], s58 offen offset:2048 sc1\n"
"buffer_store_dwordx4 v[162:165], v0, s[20:23], s58 offen offset:3072 sc1\n"
"buffer_store_dwordx4 v[166:169], v0, s[20:23], s59 offen sc1\n"
"buffer_store_dwordx4 v[170:173], v0, s[20:23], s59 offen offset:1024 sc1\n"
"buffer_store_dwordx4 v[174:177], v0, s[20:23], s47 offen sc1\n"
".Lkpo_16:\n"
"v_mov_b32 v205, 0x3f803f80\n"
"v_accvgpr_write_b32 a252, v205\n"
"v_accvgpr_write_b32 a253, v205\n"
"v_accvgpr_write_b32 a254, v205\n"
"v_accvgpr_write_b32 a255, v205\n"
"v_lshlrev_b32 v20, 16, v28\n"
"v_and_b32 v21, 0xffff0000, v28\n"
"v_lshlrev_b32 v22, 16, v29\n"
"v_and_b32 v23, 0xffff0000, v29\n"
"v_lshlrev_b32 v24, 16, v30\n"
"v_and_b32 v25, 0xffff0000, v30\n"
"v_lshlrev_b32 v26, 16, v31\n"
"v_and_b32 v27, 0xffff0000, v31\n"
"v_lshlrev_b32 v28, 16, v32\n"
"v_and_b32 v29, 0xffff0000, v32\n"
"v_lshlrev_b32 v30, 16, v33\n"
"v_and_b32 v31, 0xffff0000, v33\n"
"v_lshlrev_b32 v32, 16, v34\n"
"v_and_b32 v33, 0xffff0000, v34\n"
"v_lshlrev_b32 v34, 16, v35\n"
"v_and_b32 v35, 0xffff0000, v35\n"
"v_add_u32 v246, s67, v1\n"
"s_lshl_b32 s48, s96, 6\n"
"v_add_u32 v247, s48, v4\n"
"s_waitcnt vmcnt(16)\n"
"s_barrier\n"
"ds_read_b128 v[182:185], v1 offset:8192\n"
"ds_read_b128 v[186:189], v1 offset:9216\n"
"ds_read_b128 v[190:193], v1 offset:10240\n"
"ds_read_b128 v[194:197], v1 offset:11264\n"
"s_waitcnt vmcnt(13) lgkmcnt(3)\n"
"v_mfma_f32_16x16x32_bf16 v[150:153], v[182:185], v[38:41], 0\n"
"s_waitcnt vmcnt(12)\n"
"v_mfma_f32_16x16x32_bf16 v[166:169], v[182:185], v[42:45], 0\n"
"s_waitcnt lgkmcnt(2)\n"
"ds_read_b128 v[198:201], v1 offset:12288\n"
"ds_read_b128 v[226:229], v246 offset:8192\n"
"s_add_u32 s44, s44, 0x1000\n"
"buffer_load_dwordx4 v[70:73], v0, s[20:23], s44 offen sc1\n"
"v_mfma_f32_16x16x32_bf16 v[154:157], v[186:189], v[38:41], 0\n"
"v_mfma_f32_16x16x32_bf16 v[170:173], v[186:189], v[42:45], 0\n"
"s_waitcnt lgkmcnt(0)\n"
"v_mfma_f32_16x16x32_bf16 v[242:245], v[226:229], a[252:255], 0\n"
"ds_read_b128 v[202:205], v1 offset:13312\n"
"s_add_u32 s45, s45, 0x1000\n"
"buffer_load_dwordx4 v[74:77], v0, s[20:23], s45 offen sc1\n"
"v_mfma_f32_16x16x32_bf16 v[158:161], v[38:41], v[190:193], 0\n"
"v_mfma_f32_16x16x32_bf16 v[174:177], v[42:45], v[190:193], 0\n"
"ds_read_b128 v[182:185], v1 offset:14336\n"
"ds_read_b128 v[230:233], v246 offset:12288\n"
"s_add_u32 m0, m0, 0x1000\n"
"s_add_u32 s60, s60, 0x1000\n"
"buffer_load_dwordx4 v0, s[20:23], s60 offen sc1 lds\n"
"v_mfma_f32_16x16x32_bf16 v[162:165], v[38:41], v[194:197], 0\n"
"v_mfma_f32_16x16x32_bf16 v[178:181], v[42:45], v[194:197], 0\n"
"s_waitcnt lgkmcnt(0)\n"
"v_mfma_f32_16x16x32_bf16 v[242:245], v[230:233], a[252:255], v[242:245]\n"
"s_waitcnt vmcnt(11)\n"
"ds_read_b128 v[186:189], v1 offset:15360\n"
"buffer_load_dwordx4 v[78:81], v0, s[20:23], s44 offen offset:1024 sc1\n"
"v_mfma_f32_16x16x32_bf16 v[150:153], v[198:201], v[46:49], v[150:153]\n"
"s_waitcnt vmcnt(11)\n"
"v_mfma_f32_16x16x32_bf16 v[166:169], v[198:201], v[50:53], v[166:169]\n"
"ds_read_b128 v[190:193], v1 offset:16384\n"
"ds_read_b128 v[226:229], v246 offset:16384\n"
"buffer_load_dwordx4 v[82:85], v0, s[20:23], s45 offen offset:1024 sc1\n"
"v_add_f32 v206, v206, v208\n"
"v_add_f32 v210, v210, v212\n"
"v_mfma_f32_16x16x32_bf16 v[154:157], v[202:205], v[46:49], v[154:157]\n"
"v_mfma_f32_16x16x32_bf16 v[170:173], v[202:205], v[50:53], v[170:173]\n"
"s_waitcnt lgkmcnt(0)\n"
"v_mfma_f32_16x16x32_bf16 v[242:245], v[226:229], a[252:255], v[242:245]\n"
"ds_read_b128 v[194:197], v1 offset:17408\n"
"s_add_u32 m0, m0, 0x1000\n"
"s_add_u32 s60, s60, 0x1000\n"
"buffer_load_dwordx4 v0, s[20:23], s60 offen sc1 lds\n"
"v_add_f32 v207, v207, v209\n"
"v_add_f32 v211, v211, v213\n"
"v_mfma_f32_16x16x32_bf16 v[158:161], v[46:49], v[182:185], v[158:161]\n"
"v_mfma_f32_16x16x32_bf16 v[174:177], v[50:53], v[182:185], v[174:177]\n"
"ds_read_b128 v[198:201], v1 offset:18432\n"
"ds_read_b128 v[230:233], v246 offset:20480\n"
"buffer_load_dwordx4 v[86:89], v0, s[20:23], s44 offen offset:2048 sc1\n"
"v_permlane16_swap_b32 v206, v210\n"
"v_permlane16_swap_b32 v207, v211\n"
"v_mfma_f32_16x16x32_bf16 v[162:165], v[46:49], v[186:189], v[162:165]\n"
"v_mfma_f32_16x16x32_bf16 v[178:181], v[50:53], v[186:189], v[178:181]\n"
"s_waitcnt lgkmcnt(0)\n"
"v_mfma_f32_16x16x32_bf16 v[242:245], v[230:233], a[252:255], v[242:245]\n"
"s_waitcnt vmcnt(12)\n"
"ds_read_b128 v[202:205], v1 offset:19456\n"
"buffer_load_dwordx4 v[90:93], v0, s[20:23], s45 offen offset:2048 sc1\n"
"v_add_f32 v206, v206, v210\n"
"v_add_f32 v207, v207, v211\n"
"v_mfma_f32_16x16x32_bf16 v[150:153], v[190:193], v[54:57], v[150:153]\n"
"s_waitcnt vmcnt(12)\n"
"v_mfma_f32_16x16x32_bf16 v[166:169], v[190:193], v[58:61], v[166:169]\n"
"ds_read_b128 v[182:185], v1 offset:20480\n"
"s_add_u32 m0, m0, 0x1000\n"
"s_add_u32 s60, s60, 0x1000\n"
"buffer_load_dwordx4 v0, s[20:23], s60 offen sc1 lds\n"
"v_permlane32_swap_b32 v206, v207\n"
"v_add_f32 v206, v206, v207\n"
"v_mfma_f32_16x16x32_bf16 v[154:157], v[194:197], v[54:57], v[154:157]\n"
"v_mfma_f32_16x16x32_bf16 v[170:173], v[194:197], v[58:61], v[170:173]\n"
"ds_read_b128 v[186:189], v1 offset:21504\n"
"buffer_load_dwordx4 v[94:97], v0, s[20:23], s44 offen offset:3072 sc1\n"
"v_mov_b32 v207, v206\n"
"s_nop 1\n"
"v_permlane32_swap_b32 v206, v207\n"
"v_mfma_f32_16x16x32_bf16 v[158:161], v[54:57], v[198:201], v[158:161]\n"
"v_mfma_f32_16x16x32_bf16 v[174:177], v[58:61], v[198:201], v[174:177]\n"
"s_waitcnt lgkmcnt(2)\n"
"ds_read_b128 v[190:193], v1 offset:22528\n"
"buffer_load_dwordx4 v[98:101], v0, s[20:23], s45 offen offset:3072 sc1\n"
"v_mul_f32 v206, 0x3b124925, v206\n"
"v_mul_f32 v210, v206, v206\n"
"v_mfma_f32_16x16x32_bf16 v[162:165], v[54:57], v[202:205], v[162:165]\n"
"v_mfma_f32_16x16x32_bf16 v[178:181], v[58:61], v[202:205], v[178:181]\n"
"s_waitcnt vmcnt(14)\n"
"s_barrier\n"
"ds_read_b128 v[194:197], v1 offset:23552\n"
"s_add_u32 m0, m0, 0x1000\n"
"s_add_u32 s60, s60, 0x1000\n"
"buffer_load_dwordx4 v0, s[20:23], s60 offen sc1 lds\n"
"v_fma_f32 v210, v207, v37, -v210\n"
"v_max_f32 v210, 0, v210\n"
"s_waitcnt vmcnt(14) lgkmcnt(3)\n"
"v_mfma_f32_16x16x32_bf16 v[150:153], v[182:185], v[62:65], v[150:153]\n"
"s_waitcnt vmcnt(13)\n"
"v_mfma_f32_16x16x32_bf16 v[166:169], v[182:185], v[66:69], v[166:169]\n"
"s_waitcnt lgkmcnt(2)\n"
"ds_read_b128 v[198:201], v1 offset:24576\n"
"s_add_u32 s44, s44, 0x1000\n"
"buffer_load_dwordx4 v[102:105], v0, s[20:23], s44 offen sc1\n"
"v_add_f32 v210, 0x3727c5ac, v210\n"
"v_rsq_f32 v216, v210\n"
"v_mfma_f32_16x16x32_bf16 v[154:157], v[186:189], v[62:65], v[154:157]\n"
"v_mfma_f32_16x16x32_bf16 v[170:173], v[186:189], v[66:69], v[170:173]\n"
"s_waitcnt lgkmcnt(2)\n"
"ds_read_b128 v[202:205], v1 offset:25600\n"
"ds_read_b128 v[226:229], v246 offset:24576\n"
"s_add_u32 s45, s45, 0x1000\n"
"buffer_load_dwordx4 v[106:109], v0, s[20:23], s45 offen sc1\n"
"v_mul_f32_e64 v214, v216, -v206\n"
"ds_write_b32 v6, v216\n"
"v_mfma_f32_16x16x32_bf16 v[158:161], v[62:65], v[190:193], v[158:161]\n"
"v_mfma_f32_16x16x32_bf16 v[174:177], v[66:69], v[190:193], v[174:177]\n"
"s_waitcnt lgkmcnt(1)\n"
"v_mfma_f32_16x16x32_bf16 v[242:245], v[226:229], a[252:255], v[242:245]\n"
"ds_read_b128 v[182:185], v1 offset:26624\n"
"buffer_load_dwordx4 v[110:113], v0, s[20:23], s44 offen offset:1024 sc1\n"
"ds_write_b32 v6, v214 offset:128\n"
"v_mov_b32 v217, v216\n"
"v_mfma_f32_16x16x32_bf16 v[162:165], v[62:65], v[194:197], v[162:165]\n"
"v_mfma_f32_16x16x32_bf16 v[178:181], v[66:69], v[194:197], v[178:181]\n"
"s_waitcnt vmcnt(14)\n"
"ds_read_b128 v[186:189], v1 offset:27648\n"
"ds_read_b128 v[230:233], v246 offset:28672\n"
"buffer_load_dwordx4 v[114:117], v0, s[20:23], s45 offen offset:1024 sc1\n"
"v_mov_b32 v215, v214\n"
"v_permlane16_swap_b32 v216, v217\n"
"v_mfma_f32_16x16x32_bf16 v[150:153], v[198:201], v[70:73], v[150:153]\n"
"s_waitcnt vmcnt(14)\n"
"v_mfma_f32_16x16x32_bf16 v[166:169], v[198:201], v[74:77], v[166:169]\n"
"s_waitcnt lgkmcnt(0)\n"
"v_mfma_f32_16x16x32_bf16 v[242:245], v[230:233], a[252:255], v[242:245]\n"
"ds_read_b128 v[190:193], v1 offset:28672\n"
"buffer_load_dwordx4 v[118:121], v0, s[20:23], s44 offen offset:2048 sc1\n"
"v_permlane16_swap_b32 v214, v215\n"
"ds_read_b128 v[218:221], v7\n"
"v_mfma_f32_16x16x32_bf16 v[154:157], v[202:205], v[70:73], v[154:157]\n"
"v_mfma_f32_16x16x32_bf16 v[170:173], v[202:205], v[74:77], v[170:173]\n"
"ds_read_b128 v[194:197], v1 offset:29696\n"
"ds_read_b128 v[226:229], v246 offset:32768\n"
"buffer_load_dwordx4 v[122:125], v0, s[20:23], s45 offen offset:2048 sc1\n"
"ds_read_b128 v[222:225], v7 offset:64\n"
"ds_read_b128 v[234:237], v7 offset:128\n"
"v_mfma_f32_16x16x32_bf16 v[158:161], v[70:73], v[182:185], v[158:161]\n"
"v_mfma_f32_16x16x32_bf16 v[174:177], v[74:77], v[182:185], v[174:177]\n"
"s_waitcnt lgkmcnt(2)\n"
"v_mfma_f32_16x16x32_bf16 v[242:245], v[226:229], a[252:255], v[242:245]\n"
"ds_read_b128 v[198:201], v1 offset:30720\n"
"buffer_load_dwordx4 v[126:129], v0, s[20:23], s44 offen offset:3072 sc1\n"
"ds_read_b128 v[238:241], v7 offset:192\n"
"v_mul_f32 v206, v226, v214\n"
"v_mfma_f32_16x16x32_bf16 v[162:165], v[70:73], v[186:189], v[162:165]\n"
"v_mfma_f32_16x16x32_bf16 v[178:181], v[74:77], v[186:189], v[178:181]\n"
"s_waitcnt vmcnt(15)\n"
"ds_read_b128 v[202:205], v1 offset:31744\n"
"ds_read_b128 v[230:233], v246 offset:36864\n"
"buffer_load_dwordx4 v[130:133], v0, s[20:23], s45 offen offset:3072 sc1\n"
"v_mfma_f32_16x16x32_bf16 v[150:153], v[190:193], v[78:81], v[150:153]\n"
"s_waitcnt vmcnt(15)\n"
"v_mfma_f32_16x16x32_bf16 v[166:169], v[190:193], v[82:85], v[166:169]\n"
"s_waitcnt lgkmcnt(0)\n"
"v_mfma_f32_16x16x32_bf16 v[242:245], v[230:233], a[252:255], v[242:245]\n"
"ds_read_b128 v[182:185], v1 offset:32768\n"
"s_add_u32 s44, s44, 0x1000\n"
"buffer_load_dwordx4 v[134:137], v0, s[20:23], s44 offen sc1\n"
"v_mfma_f32_16x16x32_bf16 v[154:157], v[194:197], v[78:81], v[154:157]\n"
"v_mfma_f32_16x16x32_bf16 v[170:173], v[194:197], v[82:85], v[170:173]\n"
"ds_read_b128 v[186:189], v1 offset:33792\n"
"ds_read_b128 v[226:229], v246 offset:40960\n"
"s_add_u32 s45, s45, 0x1000\n"
"buffer_load_dwordx4 v[138:141], v0, s[20:23], s45 offen sc1\n"
"v_mfma_f32_16x16x32_bf16 v[158:161], v[78:81], v[198:201], v[158:161]\n"
"v_mfma_f32_16x16x32_bf16 v[174:177], v[82:85], v[198:201], v[174:177]\n"
"s_waitcnt lgkmcnt(0)\n"
"v_mfma_f32_16x16x32_bf16 v[242:245], v[226:229], a[252:255], v[242:245]\n"
"ds_read_b128 v[190:193], v1 offset:34816\n"
"buffer_load_dwordx4 v[142:145], v0, s[20:23], s44 offen offset:1024 sc1\n"
"v_mfma_f32_16x16x32_bf16 v[162:165], v[78:81], v[202:205], v[162:165]\n"
"v_mfma_f32_16x16x32_bf16 v[178:181], v[82:85], v[202:205], v[178:181]\n"
"s_waitcnt vmcnt(16)\n"
"ds_read_b128 v[194:197], v1 offset:35840\n"
"buffer_load_dwordx4 v[146:149], v0, s[20:23], s45 offen offset:1024 sc1\n"
"v_mfma_f32_16x16x32_bf16 v[150:153], v[182:185], v[86:89], v[150:153]\n"
"s_waitcnt vmcnt(16)\n"
"v_mfma_f32_16x16x32_bf16 v[166:169], v[182:185], v[90:93], v[166:169]\n"
"ds_read_b128 v[198:201], v1 offset:36864\n"
"s_add_u32 m0, s67, 0x20000\n"
"s_add_u32 s60, s67, s64\n"
"s_add_u32 s60, s60, 0xe000\n"
"buffer_load_dwordx4 v0, s[20:23], s60 offen sc1 lds\n"
"v_mfma_f32_16x16x32_bf16 v[154:157], v[186:189], v[86:89], v[154:157]\n"
"v_mfma_f32_16x16x32_bf16 v[170:173], v[186:189], v[90:93], v[170:173]\n"
"s_waitcnt lgkmcnt(2)\n"
"ds_read_b128 v[202:205], v1 offset:37888\n"
"s_add_u32 m0, m0, 0x1000\n"
"s_add_u32 s60, s60, 0x1000\n"
"buffer_load_dwordx4 v0, s[20:23], s60 offen sc1 lds\n"
"v_mfma_f32_16x16x32_bf16 v[158:161], v[86:89], v[190:193], v[158:161]\n"
"v_mfma_f32_16x16x32_bf16 v[174:177], v[90:93], v[190:193], v[174:177]\n"
"s_waitcnt lgkmcnt(2)\n"
"ds_read_b128 v[182:185], v1 offset:38912\n"
"s_add_u32 m0, m0, 0x1000\n"
"s_add_u32 s60, s60, 0x1000\n"
"buffer_load_dwordx4 v0, s[20:23], s60 offen sc1 lds\n"
"v_mfma_f32_16x16x32_bf16 v[162:165], v[86:89], v[194:197], v[162:165]\n"
"v_mfma_f32_16x16x32_bf16 v[178:181], v[90:93], v[194:197], v[178:181]\n"
"s_waitcnt vmcnt(17) lgkmcnt(2)\n"
"ds_read_b128 v[186:189], v1 offset:39936\n"
"s_add_u32 m0, m0, 0x1000\n"
"s_add_u32 s60, s60, 0x1000\n"
"buffer_load_dwordx4 v0, s[20:23], s60 offen sc1 lds\n"
"v_mfma_f32_16x16x32_bf16 v[150:153], v[198:201], v[94:97], v[150:153]\n"
"s_waitcnt vmcnt(17)\n"
"v_mfma_f32_16x16x32_bf16 v[166:169], v[198:201], v[98:101], v[166:169]\n"
"s_waitcnt lgkmcnt(2)\n"
"ds_read_b128 v[190:193], v1 offset:40960\n"
"s_add_u32 m0, m0, 0x1000\n"
"s_add_u32 s60, s60, 0x1000\n"
"buffer_load_dwordx4 v0, s[20:23], s60 offen sc1 lds\n"
"v_mfma_f32_16x16x32_bf16 v[154:157], v[202:205], v[94:97], v[154:157]\n"
"v_mfma_f32_16x16x32_bf16 v[170:173], v[202:205], v[98:101], v[170:173]\n"
"s_waitcnt lgkmcnt(2)\n"
"ds_read_b128 v[194:197], v1 offset:41984\n"
"s_add_u32 m0, m0, 0x1000\n"
"s_add_u32 s60, s60, 0x1000\n"
"buffer_load_dwordx4 v0, s[20:23], s60 offen sc1 lds\n"
"v_mfma_f32_16x16x32_bf16 v[158:161], v[94:97], v[182:185], v[158:161]\n"
"v_mfma_f32_16x16x32_bf16 v[174:177], v[98:101], v[182:185], v[174:177]\n"
"s_waitcnt lgkmcnt(2)\n"
"ds_read_b128 v[198:201], v1 offset:43008\n"
"s_add_u32 m0, m0, 0x1000\n"
"s_add_u32 s60, s60, 0x1000\n"
"buffer_load_dwordx4 v0, s[20:23], s60 offen sc1 lds\n"
"v_mfma_f32_16x16x32_bf16 v[162:165], v[94:97], v[186:189], v[162:165]\n"
"v_mfma_f32_16x16x32_bf16 v[178:181], v[98:101], v[186:189], v[178:181]\n"
"s_waitcnt vmcnt(15)\n"
"s_barrier\n"
"s_cmp_lg_u32 s96, 0\n"
"s_cbranch_scc1 .Lkw0_25\n"
"s_mov_b64 exec, s[38:39]\n"
"buffer_store_dwordx2 v[252:253], v254, s[20:23], 0 offen offset:768 sc1\n"
"s_mov_b64 exec, -1\n"
".Lkw0_25:\n"
"ds_read_b128 v[202:205], v1 offset:44032\n"
"s_waitcnt lgkmcnt(3)\n"
"v_mfma_f32_16x16x32_bf16 v[150:153], v[190:193], v[102:105], v[150:153]\n"
"v_mfma_f32_16x16x32_bf16 v[166:169], v[190:193], v[106:109], v[166:169]\n"
"s_waitcnt lgkmcnt(2)\n"
"ds_read_b128 v[182:185], v1 offset:45056\n"
"v_mfma_f32_16x16x32_bf16 v[154:157], v[194:197], v[102:105], v[154:157]\n"
"v_mfma_f32_16x16x32_bf16 v[170:173], v[194:197], v[106:109], v[170:173]\n"
"s_waitcnt lgkmcnt(2)\n"
"ds_read_b128 v[186:189], v1 offset:46080\n"
"ds_read_b128 v[230:233], v246 offset:45056\n"
"v_mfma_f32_16x16x32_bf16 v[158:161], v[102:105], v[198:201], v[158:161]\n"
"v_mfma_f32_16x16x32_bf16 v[174:177], v[106:109], v[198:201], v[174:177]\n"
"s_waitcnt lgkmcnt(3)\n"
"ds_read_b128 v[190:193], v1 offset:47104\n"
"ds_read_b128 v[226:229], v246 offset:49152\n"
"v_mfma_f32_16x16x32_bf16 v[162:165], v[102:105], v[202:205], v[162:165]\n"
"v_mfma_f32_16x16x32_bf16 v[178:181], v[106:109], v[202:205], v[178:181]\n"
"s_waitcnt lgkmcnt(2)\n"
"v_mfma_f32_16x16x32_bf16 v[242:245], v[230:233], a[252:255], v[242:245]\n"
"ds_read_b128 v[194:197], v1 offset:48128\n"
"ds_read_b128 v[230:233], v246 offset:53248\n"
"v_mfma_f32_16x16x32_bf16 v[150:153], v[182:185], v[110:113], v[150:153]\n"
"v_mfma_f32_16x16x32_bf16 v[166:169], v[182:185], v[114:117], v[166:169]\n"
"s_waitcnt lgkmcnt(2)\n"
"v_mfma_f32_16x16x32_bf16 v[242:245], v[226:229], a[252:255], v[242:245]\n"
"ds_read_b128 v[198:201], v1 offset:49152\n"
"ds_read_b128 v[226:229], v246 offset:57344\n"
"v_mfma_f32_16x16x32_bf16 v[154:157], v[186:189], v[110:113], v[154:157]\n"
"v_mfma_f32_16x16x32_bf16 v[170:173], v[186:189], v[114:117], v[170:173]\n"
"s_waitcnt lgkmcnt(2)\n"
"v_mfma_f32_16x16x32_bf16 v[242:245], v[230:233], a[252:255], v[242:245]\n"
"ds_read_b128 v[202:205], v1 offset:50176\n"
"ds_read_b128 v[230:233], v246 offset:61440\n"
"v_mfma_f32_16x16x32_bf16 v[158:161], v[110:113], v[190:193], v[158:161]\n"
"v_mfma_f32_16x16x32_bf16 v[174:177], v[114:117], v[190:193], v[174:177]\n"
"s_waitcnt lgkmcnt(2)\n"
"v_mfma_f32_16x16x32_bf16 v[242:245], v[226:229], a[252:255], v[242:245]\n"
"ds_read_b128 v[182:185], v1 offset:51200\n"
"v_mfma_f32_16x16x32_bf16 v[162:165], v[110:113], v[194:197], v[162:165]\n"
"v_mfma_f32_16x16x32_bf16 v[178:181], v[114:117], v[194:197], v[178:181]\n"
"s_waitcnt lgkmcnt(1)\n"
"v_mfma_f32_16x16x32_bf16 v[242:245], v[230:233], a[252:255], v[242:245]\n"
"s_waitcnt vmcnt(14)\n"
"ds_read_b128 v[186:189], v1 offset:52224\n"
"v_mfma_f32_16x16x32_bf16 v[150:153], v[198:201], v[118:121], v[150:153]\n"
"s_waitcnt vmcnt(13)\n"
"v_mfma_f32_16x16x32_bf16 v[166:169], v[198:201], v[122:125], v[166:169]\n"
"ds_read_b128 v[190:193], v1 offset:53248\n"
"v_mfma_f32_16x16x32_bf16 v[154:157], v[202:205], v[118:121], v[154:157]\n"
"v_mfma_f32_16x16x32_bf16 v[170:173], v[202:205], v[122:125], v[170:173]\n"
"s_waitcnt lgkmcnt(2)\n"
"ds_read_b128 v[194:197], v1 offset:54272\n"
"ds_write_b128 v247, v[242:245] offset:128\n"
"v_mfma_f32_16x16x32_bf16 v[158:161], v[118:121], v[182:185], v[158:161]\n"
"v_mfma_f32_16x16x32_bf16 v[174:177], v[122:125], v[182:185], v[174:177]\n"
"ds_read_b128 v[198:201], v1 offset:55296\n"
"s_waitcnt lgkmcnt(1)\n"
"s_barrier\n"
"ds_read_b128 v[226:229], v4 offset:128\n"
"ds_read_b128 v[230:233], v4 offset:192\n"
"ds_read_b32 v250, v5 offset:256\n"
"ds_read_b32 v251, v5 offset:320\n"
"v_mfma_f32_16x16x32_bf16 v[162:165], v[118:121], v[186:189], v[162:165]\n"
"v_mfma_f32_16x16x32_bf16 v[178:181], v[122:125], v[186:189], v[178:181]\n"
"s_waitcnt vmcnt(12)\n"
"ds_read_b128 v[202:205], v1 offset:56320\n"
"v_mfma_f32_16x16x32_bf16 v[150:153], v[190:193], v[126:129], v[150:153]\n"
"s_waitcnt vmcnt(11)\n"
"v_mfma_f32_16x16x32_bf16 v[166:169], v[190:193], v[130:133], v[166:169]\n"
"ds_read_b128 v[182:185], v1 offset:57344\n"
"v_mfma_f32_16x16x32_bf16 v[154:157], v[194:197], v[126:129], v[154:157]\n"
"v_mfma_f32_16x16x32_bf16 v[170:173], v[194:197], v[130:133], v[170:173]\n"
"s_waitcnt lgkmcnt(5)\n"
"ds_read_b128 v[186:189], v1 offset:58368\n"
"v_mul_f32 v206, v226, v214\n"
"v_mul_f32 v207, v227, v214\n"
"v_mul_f32 v208, v228, v214\n"
"v_mfma_f32_16x16x32_bf16 v[158:161], v[126:129], v[198:201], v[158:161]\n"
"v_mfma_f32_16x16x32_bf16 v[174:177], v[130:133], v[198:201], v[174:177]\n"
"s_waitcnt lgkmcnt(2)\n"
"ds_read_b128 v[190:193], v1 offset:59392\n"
"v_mul_f32 v209, v229, v214\n"
"v_mul_f32 v210, v230, v214\n"
"v_mul_f32 v211, v231, v214\n"
"v_mfma_f32_16x16x32_bf16 v[162:165], v[126:129], v[202:205], v[162:165]\n"
"v_mfma_f32_16x16x32_bf16 v[178:181], v[130:133], v[202:205], v[178:181]\n"
"s_waitcnt vmcnt(10) lgkmcnt(2)\n"
"ds_read_b128 v[194:197], v1 offset:60416\n"
"v_mul_f32 v212, v232, v214\n"
"v_mul_f32 v213, v233, v214\n"
"v_mul_f32 v226, v226, v215\n"
"v_mfma_f32_16x16x32_bf16 v[150:153], v[182:185], v[134:137], v[150:153]\n"
"s_waitcnt vmcnt(9)\n"
"v_mfma_f32_16x16x32_bf16 v[166:169], v[182:185], v[138:141], v[166:169]\n"
"s_waitcnt lgkmcnt(2)\n"
"ds_read_b128 v[198:201], v1 offset:61440\n"
"v_mul_f32 v227, v227, v215\n"
"v_mul_f32 v228, v228, v215\n"
"v_mul_f32 v229, v229, v215\n"
"v_mfma_f32_16x16x32_bf16 v[154:157], v[186:189], v[134:137], v[154:157]\n"
"v_mfma_f32_16x16x32_bf16 v[170:173], v[186:189], v[138:141], v[170:173]\n"
"s_waitcnt lgkmcnt(2)\n"
"ds_read_b128 v[202:205], v1 offset:62464\n"
"v_mul_f32 v230, v230, v215\n"
"v_mul_f32 v231, v231, v215\n"
"v_mul_f32 v232, v232, v215\n"
"v_mfma_f32_16x16x32_bf16 v[158:161], v[134:137], v[190:193], v[158:161]\n"
"v_mfma_f32_16x16x32_bf16 v[174:177], v[138:141], v[190:193], v[174:177]\n"
"s_waitcnt lgkmcnt(2)\n"
"ds_read_b128 v[182:185], v1 offset:63488\n"
"v_mul_f32 v233, v233, v215\n"
"v_mul_f32 v242, v234, v250\n"
"v_mul_f32 v243, v235, v250\n"
"v_mfma_f32_16x16x32_bf16 v[162:165], v[134:137], v[194:197], v[162:165]\n"
"v_mfma_f32_16x16x32_bf16 v[178:181], v[138:141], v[194:197], v[178:181]\n"
"s_waitcnt vmcnt(8) lgkmcnt(2)\n"
"ds_read_b128 v[186:189], v1 offset:64512\n"
"v_mul_f32 v244, v236, v250\n"
"v_mul_f32 v245, v237, v250\n"
"v_mul_f32 v246, v238, v250\n"
"v_mfma_f32_16x16x32_bf16 v[150:153], v[198:201], v[142:145], v[150:153]\n"
"s_waitcnt vmcnt(7)\n"
"v_mfma_f32_16x16x32_bf16 v[166:169], v[198:201], v[146:149], v[166:169]\n"
"v_mul_f32 v247, v239, v250\n"
"v_mul_f32 v248, v240, v250\n"
"v_mul_f32 v249, v241, v250\n"
"s_mov_b32 s46, 0xc91000\n"
"s_mul_i32 s51, s34, 0x40000\n"
"s_add_u32 s46, s46, s51\n"
"s_mul_i32 s51, s97, 0x4000\n"
"s_add_u32 s46, s46, s51\n"
"s_mul_i32 s51, s96, 0x1000\n"
"s_add_u32 s46, s46, s51\n"
"s_waitcnt lgkmcnt(2)\n"
"v_mfma_f32_16x16x32_bf16 v[154:157], v[202:205], v[142:145], v[154:157]\n"
"v_mfma_f32_16x16x32_bf16 v[170:173], v[202:205], v[146:149], v[170:173]\n"
"s_waitcnt lgkmcnt(1)\n"
"v_mul_f32 v234, v234, v251\n"
"v_mul_f32 v235, v235, v251\n"
"v_mul_f32 v236, v236, v251\n"
"v_mfma_f32_16x16x32_bf16 v[158:161], v[142:145], v[182:185], v[158:161]\n"
"v_mfma_f32_16x16x32_bf16 v[174:177], v[146:149], v[182:185], v[174:177]\n"
"s_waitcnt lgkmcnt(0)\n"
"v_mul_f32 v237, v237, v251\n"
"v_mul_f32 v238, v238, v251\n"
"v_mul_f32 v239, v239, v251\n"
"v_mfma_f32_16x16x32_bf16 v[162:165], v[142:145], v[186:189], v[162:165]\n"
"v_mfma_f32_16x16x32_bf16 v[178:181], v[146:149], v[186:189], v[178:181]\n"
"v_mul_f32 v240, v240, v251\n"
"v_mul_f32 v241, v241, v251\n"
"v_pk_fma_f32 v[150:151], v[150:151], v[216:217], v[206:207] op_sel_hi:[1,0,1]\n"
"v_pk_fma_f32 v[152:153], v[152:153], v[216:217], v[208:209] op_sel_hi:[1,0,1]\n"
"v_pk_fma_f32 v[154:155], v[154:155], v[216:217], v[210:211] op_sel_hi:[1,0,1]\n"
"v_pk_fma_f32 v[156:157], v[156:157], v[216:217], v[212:213] op_sel_hi:[1,0,1]\n"
"v_cvt_pk_bf16_f32 v190, v150, v151\n"
"v_cvt_pk_bf16_f32 v191, v152, v153\n"
"v_cvt_pk_bf16_f32 v192, v154, v155\n"
"v_cvt_pk_bf16_f32 v193, v156, v157\n"
"buffer_store_dwordx4 v[190:193], v0, s[20:23], s46 offen sc1\n"
"v_pk_fma_f32 v[166:167], v[166:167], v[216:217], v[226:227] op_sel:[0,1,0]\n"
"v_pk_fma_f32 v[168:169], v[168:169], v[216:217], v[228:229] op_sel:[0,1,0]\n"
"v_pk_fma_f32 v[170:171], v[170:171], v[216:217], v[230:231] op_sel:[0,1,0]\n"
"v_pk_fma_f32 v[172:173], v[172:173], v[216:217], v[232:233] op_sel:[0,1,0]\n"
"v_cvt_pk_bf16_f32 v194, v166, v167\n"
"v_cvt_pk_bf16_f32 v195, v168, v169\n"
"v_cvt_pk_bf16_f32 v196, v170, v171\n"
"v_cvt_pk_bf16_f32 v197, v172, v173\n"
"buffer_store_dwordx4 v[194:197], v0, s[20:23], s46 offen offset:1024 sc1\n"
"v_pk_fma_f32 v[158:159], v[158:159], v[218:219], v[242:243]\n"
"v_pk_fma_f32 v[160:161], v[160:161], v[220:221], v[244:245]\n"
"v_pk_fma_f32 v[174:175], v[174:175], v[222:223], v[246:247]\n"
"v_pk_fma_f32 v[176:177], v[176:177], v[224:225], v[248:249]\n"
"v_cvt_pk_bf16_f32 v186, v158, v159\n"
"v_cvt_pk_bf16_f32 v187, v160, v161\n"
"v_cvt_pk_bf16_f32 v188, v174, v175\n"
"v_cvt_pk_bf16_f32 v189, v176, v177\n"
"buffer_store_dwordx4 v[186:189], v0, s[20:23], s46 offen offset:2048 sc1\n"
"v_pk_fma_f32 v[162:163], v[162:163], v[218:219], v[234:235]\n"
"v_pk_fma_f32 v[164:165], v[164:165], v[220:221], v[236:237]\n"
"v_pk_fma_f32 v[178:179], v[178:179], v[222:223], v[238:239]\n"
"v_pk_fma_f32 v[180:181], v[180:181], v[224:225], v[240:241]\n"
"v_cvt_pk_bf16_f32 v182, v162, v163\n"
"v_cvt_pk_bf16_f32 v183, v164, v165\n"
"v_cvt_pk_bf16_f32 v184, v178, v179\n"
"v_cvt_pk_bf16_f32 v185, v180, v181\n"
"s_mov_b64 exec, s[42:43]\n"
"v_mov_b32 v182, 0x3f803f80\n"
"v_mov_b32 v183, 0x3f803f80\n"
"v_mov_b32 v184, 0x3f803f80\n"
"v_mov_b32 v185, 0x3f803f80\n"
"s_mov_b64 exec, -1\n"
"buffer_store_dwordx4 v[182:185], v0, s[20:23], s46 offen offset:3072 sc1\n"
"s_lshl_b32 s48, s96, 12\n"
"v_add_u32 v252, s48, v0\n"
"ds_write_b128 v252, v[190:193] offset:32768\n"
"ds_write_b128 v252, v[194:197] offset:33792\n"
"ds_write_b128 v252, v[186:189] offset:34816\n"
"ds_write_b128 v252, v[182:185] offset:35840\n"
"v_mul_f32 v250, 0x3e8b97f6, v216\n"
"v_mul_f32 v251, 0x3e8b97f6, v217\n"
"v_mul_f32 v242, 0x3e8b97f6, v214\n"
"v_mul_f32 v243, 0x3e8b97f6, v215\n"
"s_add_u32 s47, s53, 0x4180\n"
"v_mul_u32_u24 v254, 0x3f8, v0\n"
"s_sub_u32 s50, 15, s97\n"
"s_min_u32 s50, s50, 2\n"
"s_bfm_b64 s[56:57], s50, 0\n"
"s_lshl_b32 s48, s97, 2\n"
"s_lshl_b32 s49, s96, 1\n"
"s_add_u32 s48, s48, s49\n"
"s_lshl_b32 s49, s34, 6\n"
"s_add_u32 s49, s49, s48\n"
"s_lshl_b32 s49, s49, 12\n"
"s_add_u32 s44, s49, 0xc89000\n"
"s_cmp_ge_u32 s48, 8\n"
"s_cselect_b32 s44, s44, 0\n"
"s_cselect_b32 s49, 0, 0x7ff00000\n"
"s_cselect_b32 s46, 1, 0\n"
"v_add_u32 v253, s49, v0\n"
"s_add_u32 s45, s44, 0x1000\n"
"v_add_u32 v245, s67, v2\n"
"s_lshl_b32 s51, s96, 6\n"
"v_add_u32 v248, s51, v4\n"
"s_waitcnt vmcnt(4)\n"
"ds_read_b128 v[174:177], v245\n"
"ds_read_b128 v[178:181], v245 offset:4096\n"
"s_waitcnt lgkmcnt(1)\n"
"v_mfma_f32_16x16x32_bf16 v[170:173], v[174:177], a[252:255], 0\n"
"s_waitcnt lgkmcnt(0)\n"
"ds_read_b128 v[174:177], v245 offset:8192\n"
"v_mfma_f32_16x16x32_bf16 v[170:173], v[178:181], a[252:255], v[170:173]\n"
"s_waitcnt lgkmcnt(0)\n"
"ds_read_b128 v[178:181], v245 offset:12288\n"
"v_mfma_f32_16x16x32_bf16 v[170:173], v[174:177], a[252:255], v[170:173]\n"
"s_waitcnt lgkmcnt(0)\n"
"ds_read_b128 v[174:177], v245 offset:16384\n"
"v_mfma_f32_16x16x32_bf16 v[170:173], v[178:181], a[252:255], v[170:173]\n"
"s_waitcnt lgkmcnt(0)\n"
"ds_read_b128 v[178:181], v245 offset:20480\n"
"v_mfma_f32_16x16x32_bf16 v[170:173], v[174:177], a[252:255], v[170:173]\n"
"s_waitcnt lgkmcnt(0)\n"
"ds_read_b128 v[174:177], v245 offset:24576\n"
"v_mfma_f32_16x16x32_bf16 v[170:173], v[178:181], a[252:255], v[170:173]\n"
"s_waitcnt lgkmcnt(0)\n"
"v_mfma_f32_16x16x32_bf16 v[170:173], v[174:177], a[252:255], v[170:173]\n"
"s_nop 7\n"
"ds_write_b128 v248, v[170:173] offset:2048\n"
"s_mul_i32 s51, s67, 14\n"
"v_add_u32 v245, s51, v1\n"
"ds_read_b128 a[196:199], v245 offset:8192\n"
"ds_read_b128 a[200:203], v245 offset:9216\n"
"ds_read_b128 a[204:207], v245 offset:10240\n"
"ds_read_b128 a[208:211], v245 offset:11264\n"
"ds_read_b128 a[212:215], v245 offset:12288\n"
"ds_read_b128 a[216:219], v245 offset:13312\n"
"ds_read_b128 a[220:223], v245 offset:14336\n"
"ds_read_b128 a[224:227], v245 offset:15360\n"
"ds_read_b128 a[228:231], v245 offset:16384\n"
"ds_read_b128 a[232:235], v245 offset:17408\n"
"ds_read_b128 a[236:239], v245 offset:18432\n"
"ds_read_b128 a[240:243], v245 offset:19456\n"
"ds_read_b128 a[244:247], v245 offset:20480\n"
"ds_read_b128 a[248:251], v245 offset:21504\n"
"v_lshrrev_b32 v244, 1, v0\n"
"v_and_b32 v244, 0xff, v244\n"
"buffer_load_dwordx2 v[246:247], v244, s[20:23], s53 offen offset:768 sc1\n"
"s_mov_b32 s55, 0\n"
"s_waitcnt vmcnt(0)\n"
".Lktpoll_26:\n"
"v_cmp_ne_u64 vcc, s[70:71], v[246:247]\n"
"s_add_u32 s55, s55, 1\n"
"s_nop 0\n"
"s_cbranch_vccz .Lktdone_27\n"
"buffer_load_dwordx2 v[246:247], v244, s[20:23], s53 offen offset:768 sc1\n"
"s_cmp_lt_u32 s55, 0x40000\n"
"s_waitcnt vmcnt(0)\n"
"s_cbranch_scc1 .Lktpoll_26\n"
"s_mov_b32 s48, 0x40000\n"
"buffer_store_dword v10, off, s[20:23], s48 sc1\n"
".Lktdone_27:\n"
"s_add_u32 s62, s54, 0\n"
"buffer_load_dwordx4 a[0:3], v0, s[20:23], s62 offen sc1\n"
"buffer_load_dwordx4 a[4:7], v0, s[20:23], s62 offen offset:1024 sc1\n"
"buffer_load_dwordx4 a[8:11], v0, s[20:23], s62 offen offset:2048 sc1\n"
"buffer_load_dwordx4 a[12:15], v0, s[20:23], s62 offen offset:3072 sc1\n"
"s_add_u32 s62, s54, 0x1000\n"
"buffer_load_dwordx4 a[16:19], v0, s[20:23], s62 offen sc1\n"
"buffer_load_dwordx4 a[20:23], v0, s[20:23], s62 offen offset:1024 sc1\n"
"s_waitcnt lgkmcnt(0)\n"
"s_barrier\n"
"s_cmp_lg_u32 s96, 0\n"
"s_cbranch_scc1 .Lkw0_29\n"
"s_cmp_eq_u32 s50, 0\n"
"s_cbranch_scc1 .Lknosig_28\n"
"s_mov_b64 exec, s[56:57]\n"
"buffer_atomic_add v10, v254, s[20:23], s47 offen\n"
"s_mov_b64 exec, -1\n"
".Lknosig_28:\n"
".Lkw0_29:\n"
"ds_read_b128 v[150:153], v2\n"
"ds_read_b128 v[154:157], v2 offset:1024\n"
"ds_read_b128 v[158:161], v2 offset:2048\n"
"ds_read_b128 v[162:165], v2 offset:3072\n"
"s_waitcnt lgkmcnt(3)\n"
"v_mfma_f32_16x16x32_bf16 v[218:221], v[150:153], v[38:41], 0\n"
"v_mfma_f32_16x16x32_bf16 v[234:237], v[150:153], v[42:45], 0\n"
"s_waitcnt lgkmcnt(2)\n"
"ds_read_b128 v[166:169], v2 offset:4096\n"
"s_mul_i32 s63, s67, 14\n"
"s_add_u32 m0, s63, 0x12000\n"
"s_add_u32 s60, s63, s66\n"
"buffer_load_dwordx4 v0, s[20:23], s60 offen sc1 lds\n"
"ds_read_b128 v[226:229], v4 offset:2048\n"
"ds_read_b128 v[230:233], v4 offset:2112\n"
"v_mfma_f32_16x16x32_bf16 v[222:225], v[154:157], v[38:41], 0\n"
"v_mfma_f32_16x16x32_bf16 v[238:241], v[154:157], v[42:45], 0\n"
"s_waitcnt lgkmcnt(4)\n"
"ds_read_b128 v[170:173], v2 offset:5120\n"
"ds_read_b128 v[206:209], v4 offset:2176\n"
"ds_read_b128 v[210:213], v4 offset:2240\n"
"v_mfma_f32_16x16x32_bf16 v[218:221], v[158:161], v[46:49], v[218:221]\n"
"v_mfma_f32_16x16x32_bf16 v[234:237], v[158:161], v[50:53], v[234:237]\n"
"s_waitcnt lgkmcnt(6)\n"
"ds_read_b128 v[150:153], v2 offset:6144\n"
"buffer_load_dwordx4 v0, s[20:23], s60 offen offset:1024 sc1 lds\n"
"v_mfma_f32_16x16x32_bf16 v[222:225], v[162:165], v[46:49], v[222:225]\n"
"v_mfma_f32_16x16x32_bf16 v[238:241], v[162:165], v[50:53], v[238:241]\n"
"s_waitcnt lgkmcnt(6)\n"
"ds_read_b128 v[154:157], v2 offset:7168\n"
"v_mfma_f32_16x16x32_bf16 v[218:221], v[166:169], v[54:57], v[218:221]\n"
"v_mfma_f32_16x16x32_bf16 v[234:237], v[166:169], v[58:61], v[234:237]\n"
"s_waitcnt lgkmcnt(3)\n"
"ds_read_b128 v[158:161], v2 offset:8192\n"
"buffer_load_dwordx4 a[24:27], v0, s[20:23], s62 offen offset:2048 sc1\n"
"v_add_f32 v226, v226, v206\n"
"v_add_f32 v227, v227, v207\n"
"v_mfma_f32_16x16x32_bf16 v[222:225], v[170:173], v[54:57], v[222:225]\n"
"v_mfma_f32_16x16x32_bf16 v[238:241], v[170:173], v[58:61], v[238:241]\n"
"s_waitcnt lgkmcnt(2)\n"
"ds_read_b128 v[162:165], v2 offset:9216\n"
"v_add_f32 v228, v228, v208\n"
"v_add_f32 v229, v229, v209\n"
"v_mfma_f32_16x16x32_bf16 v[218:221], v[150:153], v[62:65], v[218:221]\n"
"v_mfma_f32_16x16x32_bf16 v[234:237], v[150:153], v[66:69], v[234:237]\n"
"s_waitcnt lgkmcnt(2)\n"
"ds_read_b128 v[166:169], v2 offset:10240\n"
"buffer_load_dwordx4 a[28:31], v0, s[20:23], s62 offen offset:3072 sc1\n"
"v_add_f32 v230, v230, v210\n"
"v_add_f32 v231, v231, v211\n"
"v_mfma_f32_16x16x32_bf16 v[222:225], v[154:157], v[62:65], v[222:225]\n"
"v_mfma_f32_16x16x32_bf16 v[238:241], v[154:157], v[66:69], v[238:241]\n"
"s_waitcnt lgkmcnt(2)\n"
"ds_read_b128 v[170:173], v2 offset:11264\n"
"v_add_f32 v232, v232, v212\n"
"v_add_f32 v233, v233, v213\n"
"v_mfma_f32_16x16x32_bf16 v[218:221], v[158:161], v[70:73], v[218:221]\n"
"v_mfma_f32_16x16x32_bf16 v[234:237], v[158:161], v[74:77], v[234:237]\n"
"s_waitcnt lgkmcnt(2)\n"
"ds_read_b128 v[150:153], v2 offset:12288\n"
"s_add_u32 s62, s54, 0x2000\n"
"buffer_load_dwordx4 a[32:35], v0, s[20:23], s62 offen sc1\n"
"ds_write_b128 v4, v[226:229]\n"
"ds_write_b128 v4, v[230:233] offset:64\n"
"v_mfma_f32_16x16x32_bf16 v[222:225], v[162:165], v[70:73], v[222:225]\n"
"v_mfma_f32_16x16x32_bf16 v[238:241], v[162:165], v[74:77], v[238:241]\n"
"s_waitcnt lgkmcnt(4)\n"
"ds_read_b128 v[154:157], v2 offset:13312\n"
"v_mul_f32 v206, v226, v242\n"
"v_mul_f32 v207, v227, v242\n"
"v_mfma_f32_16x16x32_bf16 v[218:221], v[166:169], v[78:81], v[218:221]\n"
"v_mfma_f32_16x16x32_bf16 v[234:237], v[166:169], v[82:85], v[234:237]\n"
"s_waitcnt lgkmcnt(4)\n"
"ds_read_b128 v[158:161], v2 offset:14336\n"
"buffer_load_dwordx4 a[36:39], v0, s[20:23], s62 offen offset:1024 sc1\n"
"v_mul_f32 v208, v228, v242\n"
"v_mul_f32 v209, v229, v242\n"
"v_mfma_f32_16x16x32_bf16 v[222:225], v[170:173], v[78:81], v[222:225]\n"
"v_mfma_f32_16x16x32_bf16 v[238:241], v[170:173], v[82:85], v[238:241]\n"
"s_waitcnt lgkmcnt(4)\n"
"ds_read_b128 v[162:165], v2 offset:15360\n"
"v_mul_f32 v210, v230, v242\n"
"v_mul_f32 v211, v231, v242\n"
"v_mfma_f32_16x16x32_bf16 v[218:221], v[150:153], v[86:89], v[218:221]\n"
"v_mfma_f32_16x16x32_bf16 v[234:237], v[150:153], v[90:93], v[234:237]\n"
"s_waitcnt lgkmcnt(2)\n"
"ds_read_b128 v[166:169], v2 offset:16384\n"
"buffer_load_dwordx4 a[40:43], v0, s[20:23], s62 offen offset:2048 sc1\n"
"v_mul_f32 v212, v232, v242\n"
"v_mul_f32 v213, v233, v242\n"
"v_mfma_f32_16x16x32_bf16 v[222:225], v[154:157], v[86:89], v[222:225]\n"
"v_mfma_f32_16x16x32_bf16 v[238:241], v[154:157], v[90:93], v[238:241]\n"
"s_waitcnt lgkmcnt(2)\n"
"ds_read_b128 v[170:173], v2 offset:17408\n"
"v_mul_f32 v226, v226, v243\n"
"v_mul_f32 v227, v227, v243\n"
"v_mfma_f32_16x16x32_bf16 v[218:221], v[158:161], v[94:97], v[218:221]\n"
"v_mfma_f32_16x16x32_bf16 v[234:237], v[158:161], v[98:101], v[234:237]\n"
"s_waitcnt lgkmcnt(2)\n"
"ds_read_b128 v[150:153], v2 offset:18432\n"
"buffer_load_dwordx4 a[44:47], v0, s[20:23], s62 offen offset:3072 sc1\n"
"v_mul_f32 v228, v228, v243\n"
"v_mul_f32 v229, v229, v243\n"
"v_mfma_f32_16x16x32_bf16 v[222:225], v[162:165], v[94:97], v[222:225]\n"
"v_mfma_f32_16x16x32_bf16 v[238:241], v[162:165], v[98:101], v[238:241]\n"
"s_waitcnt lgkmcnt(2)\n"
"ds_read_b128 v[154:157], v2 offset:19456\n"
"v_mul_f32 v230, v230, v243\n"
"v_mul_f32 v231, v231, v243\n"
"v_mfma_f32_16x16x32_bf16 v[218:221], v[166:169], v[102:105], v[218:221]\n"
"v_mfma_f32_16x16x32_bf16 v[234:237], v[166:169], v[106:109], v[234:237]\n"
"s_waitcnt lgkmcnt(2)\n"
"ds_read_b128 v[158:161], v2 offset:20480\n"
"v_mul_f32 v232, v232, v243\n"
"v_mul_f32 v233, v233, v243\n"
"v_mfma_f32_16x16x32_bf16 v[222:225], v[170:173], v[102:105], v[222:225]\n"
"v_mfma_f32_16x16x32_bf16 v[238:241], v[170:173], v[106:109], v[238:241]\n"
"s_waitcnt lgkmcnt(2)\n"
"ds_read_b128 v[162:165], v2 offset:21504\n"
"v_mfma_f32_16x16x32_bf16 v[218:221], v[150:153], v[110:113], v[218:221]\n"
"v_mfma_f32_16x16x32_bf16 v[234:237], v[150:153], v[114:117], v[234:237]\n"
"s_waitcnt lgkmcnt(2)\n"
"ds_read_b128 v[166:169], v2 offset:22528\n"
"v_mfma_f32_16x16x32_bf16 v[222:225], v[154:157], v[110:113], v[222:225]\n"
"v_mfma_f32_16x16x32_bf16 v[238:241], v[154:157], v[114:117], v[238:241]\n"
"s_waitcnt lgkmcnt(2)\n"
"ds_read_b128 v[170:173], v2 offset:23552\n"
"v_mfma_f32_16x16x32_bf16 v[218:221], v[158:161], v[118:121], v[218:221]\n"
"v_mfma_f32_16x16x32_bf16 v[234:237], v[158:161], v[122:125], v[234:237]\n"
"s_waitcnt lgkmcnt(2)\n"
"ds_read_b128 v[150:153], v2 offset:24576\n"
"buffer_load_dword v244, off, s[20:23], s68 offset:256 sc1\n"
"v_mfma_f32_16x16x32_bf16 v[222:225], v[162:165], v[118:121], v[222:225]\n"
"v_mfma_f32_16x16x32_bf16 v[238:241], v[162:165], v[122:125], v[238:241]\n"
"s_waitcnt lgkmcnt(2)\n"
"ds_read_b128 v[154:157], v2 offset:25600\n"
"v_mfma_f32_16x16x32_bf16 v[218:221], v[166:169], v[126:129], v[218:221]\n"
"v_mfma_f32_16x16x32_bf16 v[234:237], v[166:169], v[130:133], v[234:237]\n"
"s_waitcnt lgkmcnt(2)\n"
"ds_read_b128 v[158:161], v2 offset:26624\n"
"v_mfma_f32_16x16x32_bf16 v[222:225], v[170:173], v[126:129], v[222:225]\n"
"v_mfma_f32_16x16x32_bf16 v[238:241], v[170:173], v[130:133], v[238:241]\n"
"s_waitcnt lgkmcnt(2)\n"
"ds_read_b128 v[162:165], v2 offset:27648\n"
"v_mfma_f32_16x16x32_bf16 v[218:221], v[150:153], v[134:137], v[218:221]\n"
"v_mfma_f32_16x16x32_bf16 v[234:237], v[150:153], v[138:141], v[234:237]\n"
"s_waitcnt lgkmcnt(2)\n"
"v_mfma_f32_16x16x32_bf16 v[222:225], v[154:157], v[134:137], v[222:225]\n"
"v_mfma_f32_16x16x32_bf16 v[238:241], v[154:157], v[138:141], v[238:241]\n"
"s_waitcnt lgkmcnt(1)\n"
"v_mfma_f32_16x16x32_bf16 v[218:221], v[158:161], v[142:145], v[218:221]\n"
"v_mfma_f32_16x16x32_bf16 v[234:237], v[158:161], v[146:149], v[234:237]\n"
"s_waitcnt lgkmcnt(0)\n"
"v_mfma_f32_16x16x32_bf16 v[222:225], v[162:165], v[142:145], v[222:225]\n"
"v_mfma_f32_16x16x32_bf16 v[238:241], v[162:165], v[146:149], v[238:241]\n"
"s_nop 3\n"
"v_pk_fma_f32 v[218:219], v[218:219], v[250:251], v[206:207] op_sel_hi:[1,0,1]\n"
"v_pk_fma_f32 v[220:221], v[220:221], v[250:251], v[208:209] op_sel_hi:[1,0,1]\n"
"s_nop 0\n"
"v_pk_fma_f32 v[222:223], v[222:223], v[250:251], v[210:211] op_sel_hi:[1,0,1]\n"
"v_pk_fma_f32 v[224:225], v[224:225], v[250:251], v[212:213] op_sel_hi:[1,0,1]\n"
"v_cvt_pk_bf16_f32 v38, v218, v219\n"
"v_cvt_pk_bf16_f32 v39, v220, v221\n"
"v_cvt_pk_bf16_f32 v40, v222, v223\n"
"v_cvt_pk_bf16_f32 v41, v224, v225\n"
"v_pk_fma_f32 v[234:235], v[234:235], v[250:251], v[226:227] op_sel:[0,1,0]\n"
"v_pk_fma_f32 v[236:237], v[236:237], v[250:251], v[228:229] op_sel:[0,1,0]\n"
"v_pk_fma_f32 v[238:239], v[238:239], v[250:251], v[230:231] op_sel:[0,1,0]\n"
"v_pk_fma_f32 v[240:241], v[240:241], v[250:251], v[232:233] op_sel:[0,1,0]\n"
"v_cvt_pk_bf16_f32 v42, v234, v235\n"
"v_cvt_pk_bf16_f32 v43, v236, v237\n"
"v_cvt_pk_bf16_f32 v44, v238, v239\n"
"v_cvt_pk_bf16_f32 v45, v240, v241\n"
"; A2 layer 0\n"
"s_lshl_b32 s48, s96, 12\n"
"v_add_u32 v79, s48, v0\n"
"s_mov_b32 s63, m0\n"
"s_lshl_b32 m0, s96, 13\n"
"s_mov_b32 s55, 0\n"
"s_waitcnt vmcnt(0)\n"
".Lkpoll_30:\n"
"v_readfirstlane_b32 s48, v244\n"
"s_add_u32 s55, s55, 1\n"
"s_cmp_ge_u32 s48, s46\n"
"s_cbranch_scc1 .Lkdone_31\n"
"buffer_load_dword v244, off, s[20:23], s68 offset:256 sc1\n"
"s_cmp_lt_u32 s55, 0x40000\n"
"s_waitcnt vmcnt(0)\n"
"s_cbranch_scc1 .Lkpoll_30\n"
"s_mov_b32 s48, 0x40000\n"
"buffer_store_dword v10, off, s[20:23], s48 sc1\n"
".Lkdone_31:\n"
"buffer_load_dwordx4 v253, s[20:23], s44 offen sc1 lds\n"
"buffer_load_dwordx4 v253, s[20:23], s44 offen offset:1024 sc1 lds\n"
"buffer_load_dwordx4 v253, s[20:23], s44 offen offset:2048 sc1 lds\n"
"buffer_load_dwordx4 v253, s[20:23], s44 offen offset:3072 sc1 lds\n"
"s_add_u32 m0, m0, 0x1000\n"
"s_nop 0\n"
"buffer_load_dwordx4 v253, s[20:23], s45 offen sc1 lds\n"
"buffer_load_dwordx4 v253, s[20:23], s45 offen offset:1024 sc1 lds\n"
"buffer_load_dwordx4 v253, s[20:23], s45 offen offset:2048 sc1 lds\n"
"buffer_load_dwordx4 v253, s[20:23], s45 offen offset:3072 sc1 lds\n"
"s_mov_b32 m0, s63\n"
"s_waitcnt vmcnt(0)\n"
"s_waitcnt lgkmcnt(0)\n"
"s_barrier\n"
"ds_read_b128 v[80:83], v79\n"
"ds_read_b128 v[84:87], v79 offset:1024\n"
"ds_read_b128 v[88:91], v79 offset:4096\n"
"ds_read_b128 v[92:95], v79 offset:5120\n"
"ds_read_b128 v[96:99], v79 offset:2048\n"
"ds_read_b128 v[100:103], v79 offset:3072\n"
"v_mov_b32 v148, 0\n"
"v_mov_b32 v149, 0\n"
"s_waitcnt lgkmcnt(5)\n"
"v_mfma_f32_16x16x32_bf16 v[112:115], v[80:83], v[38:41], v[16:19]\n"
"s_waitcnt lgkmcnt(4)\n"
"v_mfma_f32_16x16x32_bf16 v[116:119], v[84:87], v[38:41], 0\n"
"v_mfma_f32_16x16x32_bf16 v[124:127], v[84:87], v[42:45], v[16:19]\n"
"s_waitcnt lgkmcnt(3)\n"
"v_mfma_f32_16x16x32_bf16 v[128:131], v[88:91], v[38:41], 0\n"
"ds_read_b128 v[80:83], v79 offset:8192\n"
"s_nop 1\n"
"v_exp_f32 v112, v112\n"
"v_exp_f32 v113, v113\n"
"ds_read_b128 v[84:87], v79 offset:9216\n"
"buffer_load_dwordx4 v0, s[20:23], s60 offen offset:2048 sc1 lds\n"
"v_exp_f32 v114, v114\n"
"v_exp_f32 v115, v115\n"
"ds_read_b128 v[104:107], v79 offset:6144\n"
"v_exp_f32 v116, v116\n"
"v_exp_f32 v117, v117\n"
"s_waitcnt lgkmcnt(5)\n"
"v_mfma_f32_16x16x32_bf16 v[132:135], v[92:95], v[38:41], 0\n"
"ds_read_b128 v[108:111], v79 offset:7168\n"
"buffer_load_dwordx4 v0, s[20:23], s60 offen offset:3072 sc1 lds\n"
"v_exp_f32 v118, v118\n"
"v_exp_f32 v119, v119\n"
"v_cvt_pk_bf16_f32 v144, v112, v113\n"
"v_cvt_pk_bf16_f32 v145, v114, v115\n"
"v_mfma_f32_16x16x32_bf16 v[136:139], v[88:91], v[42:45], 0\n"
"v_cvt_pk_bf16_f32 v146, v116, v117\n"
"v_cvt_pk_bf16_f32 v147, v118, v119\n"
"v_mfma_f32_16x16x32_bf16 v[140:143], v[92:95], v[42:45], 0\n"
"s_waitcnt lgkmcnt(5)\n"
"v_mfma_f32_16x16x32_bf16 v[152:155], v[96:99], v[144:147], 0\n"
"s_waitcnt lgkmcnt(4)\n"
"s_add_u32 m0, m0, 0x1000\n"
"s_add_u32 s60, s60, 0x1000\n"
"buffer_load_dwordx4 v0, s[20:23], s60 offen sc1 lds\n"
"v_exp_f32 v124, v124\n"
"v_exp_f32 v125, v125\n"
"v_mfma_f32_16x16x32_bf16 v[156:159], v[100:103], v[144:147], 0\n"
"s_waitcnt lgkmcnt(3)\n"
"v_exp_f32 v126, v126\n"
"v_exp_f32 v127, v127\n"
"v_cvt_pk_bf16_f32 v150, v124, v125\n"
"v_cvt_pk_bf16_f32 v151, v126, v127\n"
"v_mfma_f32_16x16x32_bf16 v[112:115], v[80:83], v[38:41], 0\n"
"ds_read_b128 v[88:91], v79 offset:12288\n"
"buffer_load_dwordx4 v0, s[20:23], s60 offen offset:1024 sc1 lds\n"
"v_exp_f32 v128, v128\n"
"v_exp_f32 v129, v129\n"
"v_mfma_f32_16x16x32_bf16 v[160:163], v[96:99], v[148:151], 0\n"
"ds_read_b128 v[92:95], v79 offset:13312\n"
"v_exp_f32 v130, v130\n"
"v_exp_f32 v131, v131\n"
"v_mfma_f32_16x16x32_bf16 v[164:167], v[100:103], v[148:151], 0\n"
"s_waitcnt lgkmcnt(4)\n"
"ds_read_b128 v[96:99], v79 offset:10240\n"
"v_exp_f32 v132, v132\n"
"v_exp_f32 v133, v133\n"
"v_mfma_f32_16x16x32_bf16 v[116:119], v[84:87], v[38:41], 0\n"
"ds_read_b128 v[100:103], v79 offset:11264\n"
"buffer_load_dwordx4 v0, s[20:23], s60 offen offset:2048 sc1 lds\n"
"v_exp_f32 v134, v134\n"
"v_exp_f32 v135, v135\n"
"v_cvt_pk_bf16_f32 v144, v128, v129\n"
"v_cvt_pk_bf16_f32 v145, v130, v131\n"
"v_mfma_f32_16x16x32_bf16 v[120:123], v[80:83], v[42:45], 0\n"
"v_cvt_pk_bf16_f32 v146, v132, v133\n"
"v_cvt_pk_bf16_f32 v147, v134, v135\n"
"v_exp_f32 v136, v136\n"
"v_exp_f32 v137, v137\n"
"v_mfma_f32_16x16x32_bf16 v[124:127], v[84:87], v[42:45], 0\n"
"s_waitcnt lgkmcnt(5)\n"
"v_exp_f32 v138, v138\n"
"v_exp_f32 v139, v139\n"
"v_mfma_f32_16x16x32_bf16 v[152:155], v[104:107], v[144:147], v[152:155]\n"
"s_waitcnt lgkmcnt(4)\n"
"buffer_load_dwordx4 v0, s[20:23], s60 offen offset:3072 sc1 lds\n"
"v_exp_f32 v140, v140\n"
"v_exp_f32 v141, v141\n"
"v_cvt_pk_bf16_f32 v148, v136, v137\n"
"v_cvt_pk_bf16_f32 v149, v138, v139\n"
"v_mfma_f32_16x16x32_bf16 v[156:159], v[108:111], v[144:147], v[156:159]\n"
"s_waitcnt lgkmcnt(3)\n"
"v_exp_f32 v142, v142\n"
"v_exp_f32 v143, v143\n"
"v_cvt_pk_bf16_f32 v150, v140, v141\n"
"v_cvt_pk_bf16_f32 v151, v142, v143\n"
"v_mfma_f32_16x16x32_bf16 v[128:131], v[88:91], v[38:41], 0\n"
"ds_read_b128 v[80:83], v79 offset:16384\n"
"s_add_u32 m0, m0, 0x1000\n"
"s_add_u32 s60, s60, 0x1000\n"
"buffer_load_dwordx4 v0, s[20:23], s60 offen sc1 lds\n"
"v_exp_f32 v112, v112\n"
"v_exp_f32 v113, v113\n"
"v_mfma_f32_16x16x32_bf16 v[160:163], v[104:107], v[148:151], v[160:163]\n"
"ds_read_b128 v[84:87], v79 offset:17408\n"
"v_exp_f32 v114, v114\n"
"v_exp_f32 v115, v115\n"
"v_mfma_f32_16x16x32_bf16 v[164:167], v[108:111], v[148:151], v[164:167]\n"
"s_waitcnt lgkmcnt(4)\n"
"ds_read_b128 v[104:107], v79 offset:14336\n"
"v_exp_f32 v116, v116\n"
"v_exp_f32 v117, v117\n"
"v_mfma_f32_16x16x32_bf16 v[132:135], v[92:95], v[38:41], 0\n"
"ds_read_b128 v[108:111], v79 offset:15360\n"
"buffer_load_dwordx4 v0, s[20:23], s60 offen offset:1024 sc1 lds\n"
"v_exp_f32 v118, v118\n"
"v_exp_f32 v119, v119\n"
"v_cvt_pk_bf16_f32 v144, v112, v113\n"
"v_cvt_pk_bf16_f32 v145, v114, v115\n"
"v_mfma_f32_16x16x32_bf16 v[136:139], v[88:91], v[42:45], 0\n"
"v_cvt_pk_bf16_f32 v146, v116, v117\n"
"v_cvt_pk_bf16_f32 v147, v118, v119\n"
"v_exp_f32 v120, v120\n"
"v_exp_f32 v121, v121\n"
"v_mfma_f32_16x16x32_bf16 v[140:143], v[92:95], v[42:45], 0\n"
"s_waitcnt lgkmcnt(5)\n"
"buffer_load_dwordx4 v0, s[20:23], s60 offen offset:2048 sc1 lds\n"
"v_exp_f32 v122, v122\n"
"v_exp_f32 v123, v123\n"
"v_mfma_f32_16x16x32_bf16 v[152:155], v[96:99], v[144:147], v[152:155]\n"
"s_waitcnt lgkmcnt(4)\n"
"v_exp_f32 v124, v124\n"
"v_exp_f32 v125, v125\n"
"v_cvt_pk_bf16_f32 v148, v120, v121\n"
"v_cvt_pk_bf16_f32 v149, v122, v123\n"
"v_mfma_f32_16x16x32_bf16 v[156:159], v[100:103], v[144:147], v[156:159]\n"
"s_waitcnt lgkmcnt(3)\n"
"v_exp_f32 v126, v126\n"
"v_exp_f32 v127, v127\n"
"v_cvt_pk_bf16_f32 v150, v124, v125\n"
"v_cvt_pk_bf16_f32 v151, v126, v127\n"
"v_mfma_f32_16x16x32_bf16 v[112:115], v[80:83], v[38:41], 0\n"
"ds_read_b128 v[88:91], v79 offset:20480\n"
"buffer_load_dwordx4 v0, s[20:23], s60 offen offset:3072 sc1 lds\n"
"v_exp_f32 v128, v128\n"
"v_exp_f32 v129, v129\n"
"v_mfma_f32_16x16x32_bf16 v[160:163], v[96:99], v[148:151], v[160:163]\n"
"ds_read_b128 v[92:95], v79 offset:21504\n"
"v_exp_f32 v130, v130\n"
"v_exp_f32 v131, v131\n"
"v_mfma_f32_16x16x32_bf16 v[164:167], v[100:103], v[148:151], v[164:167]\n"
"s_waitcnt lgkmcnt(4)\n"
"ds_read_b128 v[96:99], v79 offset:18432\n"
"v_exp_f32 v132, v132\n"
"v_exp_f32 v133, v133\n"
"v_mfma_f32_16x16x32_bf16 v[116:119], v[84:87], v[38:41], 0\n"
"ds_read_b128 v[100:103], v79 offset:19456\n"
"s_add_u32 m0, m0, 0x1000\n"
"s_add_u32 s60, s60, 0x1000\n"
"buffer_load_dwordx4 v0, s[20:23], s60 offen sc1 lds\n"
"v_exp_f32 v134, v134\n"
"v_exp_f32 v135, v135\n"
"v_cvt_pk_bf16_f32 v144, v128, v129\n"
"v_cvt_pk_bf16_f32 v145, v130, v131\n"
"v_mfma_f32_16x16x32_bf16 v[120:123], v[80:83], v[42:45], 0\n"
"v_cvt_pk_bf16_f32 v146, v132, v133\n"
"v_cvt_pk_bf16_f32 v147, v134, v135\n"
"v_exp_f32 v136, v136\n"
"v_exp_f32 v137, v137\n"
"v_mfma_f32_16x16x32_bf16 v[124:127], v[84:87], v[42:45], 0\n"
"s_waitcnt lgkmcnt(5)\n"
"buffer_load_dwordx4 v0, s[20:23], s60 offen offset:1024 sc1 lds\n"
"v_exp_f32 v138, v138\n"
"v_exp_f32 v139, v139\n"
"v_mfma_f32_16x16x32_bf16 v[152:155], v[104:107], v[144:147], v[152:155]\n"
"s_waitcnt lgkmcnt(4)\n"
"v_exp_f32 v140, v140\n"
"v_exp_f32 v141, v141\n"
"v_cvt_pk_bf16_f32 v148, v136, v137\n"
"v_cvt_pk_bf16_f32 v149, v138, v139\n"
"v_mfma_f32_16x16x32_bf16 v[156:159], v[108:111], v[144:147], v[156:159]\n"
"s_waitcnt lgkmcnt(3)\n"
"v_exp_f32 v142, v142\n"
"v_exp_f32 v143, v143\n"
"v_cvt_pk_bf16_f32 v150, v140, v141\n"
"v_cvt_pk_bf16_f32 v151, v142, v143\n"
"v_mfma_f32_16x16x32_bf16 v[128:131], v[88:91], v[38:41], 0\n"
"ds_read_b128 v[80:83], v79 offset:24576\n"
"s_mul_i32 s63, s67, 6\n"
"s_add_u32 m0, s63, 0xc000\n"
"s_add_u32 s60, s63, s65\n"
"s_add_u32 s60, s60, 0xc000\n"
"buffer_load_dwordx4 v0, s[20:23], s60 offen sc1 lds\n"
"v_exp_f32 v112, v112\n"
"v_exp_f32 v113, v113\n"
"v_mfma_f32_16x16x32_bf16 v[160:163], v[104:107], v[148:151], v[160:163]\n"
"ds_read_b128 v[84:87], v79 offset:25600\n"
"v_exp_f32 v114, v114\n"
"v_exp_f32 v115, v115\n"
"v_mfma_f32_16x16x32_bf16 v[164:167], v[108:111], v[148:151], v[164:167]\n"
"s_waitcnt lgkmcnt(4)\n"
"ds_read_b128 v[104:107], v79 offset:22528\n"
"buffer_load_dwordx4 v0, s[20:23], s60 offen offset:1024 sc1 lds\n"
"v_exp_f32 v116, v116\n"
"v_exp_f32 v117, v117\n"
"v_mfma_f32_16x16x32_bf16 v[132:135], v[92:95], v[38:41], 0\n"
"ds_read_b128 v[108:111], v79 offset:23552\n"
"v_exp_f32 v118, v118\n"
"v_exp_f32 v119, v119\n"
"v_cvt_pk_bf16_f32 v144, v112, v113\n"
"v_cvt_pk_bf16_f32 v145, v114, v115\n"
"v_mfma_f32_16x16x32_bf16 v[136:139], v[88:91], v[42:45], 0\n"
"v_cvt_pk_bf16_f32 v146, v116, v117\n"
"v_cvt_pk_bf16_f32 v147, v118, v119\n"
"v_exp_f32 v120, v120\n"
"v_exp_f32 v121, v121\n"
"v_mfma_f32_16x16x32_bf16 v[140:143], v[92:95], v[42:45], 0\n"
"s_waitcnt lgkmcnt(5)\n"
"buffer_load_dwordx4 v0, s[20:23], s60 offen offset:2048 sc1 lds\n"
"v_exp_f32 v122, v122\n"
"v_exp_f32 v123, v123\n"
"v_mfma_f32_16x16x32_bf16 v[152:155], v[96:99], v[144:147], v[152:155]\n"
"s_waitcnt lgkmcnt(4)\n"
"v_exp_f32 v124, v124\n"
"v_exp_f32 v125, v125\n"
"v_cvt_pk_bf16_f32 v148, v120, v121\n"
"v_cvt_pk_bf16_f32 v149, v122, v123\n"
"v_mfma_f32_16x16x32_bf16 v[156:159], v[100:103], v[144:147], v[156:159]\n"
"s_waitcnt lgkmcnt(3)\n"
"v_exp_f32 v126, v126\n"
"v_exp_f32 v127, v127\n"
"v_cvt_pk_bf16_f32 v150, v124, v125\n"
"v_cvt_pk_bf16_f32 v151, v126, v127\n"
"v_mfma_f32_16x16x32_bf16 v[112:115], v[80:83], v[38:41], 0\n"
"ds_read_b128 v[88:91], v79 offset:28672\n"
"buffer_load_dwordx4 v0, s[20:23], s60 offen offset:3072 sc1 lds\n"
"v_exp_f32 v128, v128\n"
"v_exp_f32 v129, v129\n"
"v_mfma_f32_16x16x32_bf16 v[160:163], v[96:99], v[148:151], v[160:163]\n"
"ds_read_b128 v[92:95], v79 offset:29696\n"
"v_exp_f32 v130, v130\n"
"v_exp_f32 v131, v131\n"
"v_mfma_f32_16x16x32_bf16 v[164:167], v[100:103], v[148:151], v[164:167]\n"
"s_waitcnt lgkmcnt(4)\n"
"ds_read_b128 v[96:99], v79 offset:26624\n"
"s_add_u32 m0, m0, 0x1000\n"
"s_add_u32 s60, s60, 0x1000\n"
"buffer_load_dwordx4 v0, s[20:23], s60 offen sc1 lds\n"
"v_exp_f32 v132, v132\n"
"v_exp_f32 v133, v133\n"
"v_mfma_f32_16x16x32_bf16 v[116:119], v[84:87], v[38:41], 0\n"
"ds_read_b128 v[100:103], v79 offset:27648\n"
"v_exp_f32 v134, v134\n"
"v_exp_f32 v135, v135\n"
"v_cvt_pk_bf16_f32 v144, v128, v129\n"
"v_cvt_pk_bf16_f32 v145, v130, v131\n"
"v_mfma_f32_16x16x32_bf16 v[120:123], v[80:83], v[42:45], 0\n"
"v_cvt_pk_bf16_f32 v146, v132, v133\n"
"v_cvt_pk_bf16_f32 v147, v134, v135\n"
"v_exp_f32 v136, v136\n"
"v_exp_f32 v137, v137\n"
"v_mfma_f32_16x16x32_bf16 v[124:127], v[84:87], v[42:45], 0\n"
"s_waitcnt lgkmcnt(5)\n"
"buffer_load_dwordx4 v0, s[20:23], s60 offen offset:1024 sc1 lds\n"
"v_exp_f32 v138, v138\n"
"v_exp_f32 v139, v139\n"
"v_mfma_f32_16x16x32_bf16 v[152:155], v[104:107], v[144:147], v[152:155]\n"
"s_waitcnt lgkmcnt(4)\n"
"v_exp_f32 v140, v140\n"
"v_exp_f32 v141, v141\n"
"v_cvt_pk_bf16_f32 v148, v136, v137\n"
"v_cvt_pk_bf16_f32 v149, v138, v139\n"
"v_mfma_f32_16x16x32_bf16 v[156:159], v[108:111], v[144:147], v[156:159]\n"
"s_mul_i32 s63, s67, 7\n"
"s_min_u32 s63, s63, 0x4c00\n"
"s_add_u32 m0, s63, 0x20000\n"
"s_add_u32 s60, s63, s65\n"
"s_add_u32 s60, s60, 0x12000\n"
"buffer_load_dwordx4 v0, s[20:23], s60 offen sc1 lds\n"
"v_exp_f32 v142, v142\n"
"v_exp_f32 v143, v143\n"
"v_cvt_pk_bf16_f32 v150, v140, v141\n"
"v_cvt_pk_bf16_f32 v151, v142, v143\n"
"s_waitcnt lgkmcnt(3)\n"
"v_mfma_f32_16x16x32_bf16 v[128:131], v[88:91], v[38:41], 0\n"
"ds_read_b128 v[80:83], v79 offset:32768\n"
"v_exp_f32 v112, v112\n"
"v_exp_f32 v113, v113\n"
"v_mfma_f32_16x16x32_bf16 v[160:163], v[104:107], v[148:151], v[160:163]\n"
"ds_read_b128 v[84:87], v79 offset:33792\n"
"v_exp_f32 v114, v114\n"
"v_exp_f32 v115, v115\n"
"v_mfma_f32_16x16x32_bf16 v[164:167], v[108:111], v[148:151], v[164:167]\n"
"s_waitcnt lgkmcnt(4)\n"
"ds_read_b128 v[104:107], v79 offset:30720\n"
"buffer_load_dwordx4 v0, s[20:23], s60 offen offset:1024 sc1 lds\n"
"v_exp_f32 v116, v116\n"
"v_exp_f32 v117, v117\n"
"v_mfma_f32_16x16x32_bf16 v[132:135], v[92:95], v[38:41], 0\n"
"ds_read_b128 v[108:111], v79 offset:31744\n"
"v_exp_f32 v118, v118\n"
"v_exp_f32 v119, v119\n"
"v_cvt_pk_bf16_f32 v144, v112, v113\n"
"v_cvt_pk_bf16_f32 v145, v114, v115\n"
"v_mfma_f32_16x16x32_bf16 v[136:139], v[88:91], v[42:45], 0\n"
"v_cvt_pk_bf16_f32 v146, v116, v117\n"
"v_cvt_pk_bf16_f32 v147, v118, v119\n"
"v_exp_f32 v120, v120\n"
"v_exp_f32 v121, v121\n"
"v_mfma_f32_16x16x32_bf16 v[140:143], v[92:95], v[42:45], 0\n"
"s_waitcnt lgkmcnt(5)\n"
"buffer_load_dwordx4 v0, s[20:23], s60 offen offset:2048 sc1 lds\n"
"v_exp_f32 v122, v122\n"
"v_exp_f32 v123, v123\n"
"v_mfma_f32_16x16x32_bf16 v[152:155], v[96:99], v[144:147], v[152:155]\n"
"s_waitcnt lgkmcnt(4)\n"
"v_exp_f32 v124, v124\n"
"v_exp_f32 v125, v125\n"
"v_cvt_pk_bf16_f32 v148, v120, v121\n"
"v_cvt_pk_bf16_f32 v149, v122, v123\n"
"v_mfma_f32_16x16x32_bf16 v[156:159], v[100:103], v[144:147], v[156:159]\n"
"s_waitcnt lgkmcnt(3)\n"
"buffer_load_dwordx4 v0, s[20:23], s60 offen offset:3072 sc1 lds\n"
"v_exp_f32 v126, v126\n"
"v_exp_f32 v127, v127\n"
"v_cvt_pk_bf16_f32 v150, v124, v125\n"
"v_cvt_pk_bf16_f32 v151, v126, v127\n"
"v_mfma_f32_16x16x32_bf16 v[112:115], v[80:83], v[38:41], v[12:15]\n"
"v_exp_f32 v128, v128\n"
"v_exp_f32 v129, v129\n"
"v_mfma_f32_16x16x32_bf16 v[160:163], v[96:99], v[148:151], v[160:163]\n"
"v_exp_f32 v130, v130\n"
"v_exp_f32 v131, v131\n"
"v_mfma_f32_16x16x32_bf16 v[164:167], v[100:103], v[148:151], v[164:167]\n"
"ds_read_b128 v[96:99], v79 offset:34816\n"
"s_add_u32 m0, m0, 0x1000\n"
"s_add_u32 s60, s60, 0x1000\n"
"buffer_load_dwordx4 v0, s[20:23], s60 offen sc1 lds\n"
"v_exp_f32 v132, v132\n"
"v_exp_f32 v133, v133\n"
"ds_read_b128 v[100:103], v79 offset:35840\n"
"v_exp_f32 v134, v134\n"
"v_exp_f32 v135, v135\n"
"v_cvt_pk_bf16_f32 v144, v128, v129\n"
"v_cvt_pk_bf16_f32 v145, v130, v131\n"
"v_mfma_f32_16x16x32_bf16 v[120:123], v[80:83], v[42:45], 0\n"
"s_waitcnt lgkmcnt(4)\n"
"buffer_load_dwordx4 v0, s[20:23], s60 offen offset:1024 sc1 lds\n"
"v_cvt_pk_bf16_f32 v146, v132, v133\n"
"v_cvt_pk_bf16_f32 v147, v134, v135\n"
"v_exp_f32 v136, v136\n"
"v_exp_f32 v137, v137\n"
"v_mfma_f32_16x16x32_bf16 v[124:127], v[84:87], v[42:45], v[12:15]\n"
"s_waitcnt lgkmcnt(3)\n"
"v_exp_f32 v138, v138\n"
"v_exp_f32 v139, v139\n"
"v_mfma_f32_16x16x32_bf16 v[152:155], v[104:107], v[144:147], v[152:155]\n"
"s_waitcnt lgkmcnt(2)\n"
"v_exp_f32 v140, v140\n"
"v_exp_f32 v141, v141\n"
"v_cvt_pk_bf16_f32 v148, v136, v137\n"
"v_cvt_pk_bf16_f32 v149, v138, v139\n"
"v_mfma_f32_16x16x32_bf16 v[156:159], v[108:111], v[144:147], v[156:159]\n"
"buffer_load_dwordx4 v0, s[20:23], s60 offen offset:2048 sc1 lds\n"
"v_exp_f32 v142, v142\n"
"v_exp_f32 v143, v143\n"
"v_cvt_pk_bf16_f32 v150, v140, v141\n"
"v_cvt_pk_bf16_f32 v151, v142, v143\n"
"v_exp_f32 v112, v112\n"
"v_exp_f32 v113, v113\n"
"v_mfma_f32_16x16x32_bf16 v[160:163], v[104:107], v[148:151], v[160:163]\n"
"v_exp_f32 v114, v114\n"
"v_exp_f32 v115, v115\n"
"v_mfma_f32_16x16x32_bf16 v[164:167], v[108:111], v[148:151], v[164:167]\n"
"s_mov_b32 s44, 0x1491000\n"
"s_mul_i32 s51, s97, 0x20000\n"
"s_add_u32 s44, s44, s51\n"
"s_mul_i32 s51, s96, 0x8000\n"
"s_add_u32 s44, s44, s51\n"
"s_mul_i32 s51, s34, 0x400\n"
"s_add_u32 s44, s44, s51\n"
"s_add_u32 s45, s44, 0x4000\n"
"v_cvt_pk_bf16_f32 v144, v112, v113\n"
"v_cvt_pk_bf16_f32 v145, v114, v115\n"
"v_mov_b32 v146, 0\n"
"v_mov_b32 v147, 0\n"
"v_exp_f32 v120, v120\n"
"v_exp_f32 v121, v121\n"
"v_exp_f32 v122, v122\n"
"v_exp_f32 v123, v123\n"
"s_waitcnt lgkmcnt(1)\n"
"v_mfma_f32_16x16x32_bf16 v[152:155], v[96:99], v[144:147], v[152:155]\n"
"s_waitcnt lgkmcnt(0)\n"
"v_exp_f32 v124, v124\n"
"v_exp_f32 v125, v125\n"
"v_cvt_pk_bf16_f32 v148, v120, v121\n"
"v_cvt_pk_bf16_f32 v149, v122, v123\n"
"v_mfma_f32_16x16x32_bf16 v[156:159], v[100:103], v[144:147], v[156:159]\n"
"v_exp_f32 v126, v126\n"
"v_exp_f32 v127, v127\n"
"v_cvt_pk_bf16_f32 v150, v124, v125\n"
"v_cvt_pk_bf16_f32 v151, v126, v127\n"
"s_nop 1\n"
"v_mfma_f32_16x16x32_bf16 v[160:163], v[96:99], v[148:151], v[160:163]\n"
"v_mfma_f32_16x16x32_bf16 v[164:167], v[100:103], v[148:151], v[164:167]\n"
"v_mov_b32 v114, v156\n"
"s_nop 6\n"
"v_mov_b32 v115, v164\n"
"s_nop 1\n"
"v_permlane16_swap_b32 v114, v115\n"
"v_mov_b32 v112, v115\n"
"s_nop 1\n"
"v_permlane32_swap_b32 v112, v115\n"
"v_add_u32 v114, 0xde800000, v115\n"
"v_cmp_lt_u32 vcc, 0x3c000000, v114\n"
"s_nop 0\n"
"s_cbranch_vccnz .Lkfallback_33\n"
".Lknorm_32:\n"
"v_rcp_f32 v112, v115\n"
"s_nop 0\n"
"v_mov_b32 v113, v112\n"
"s_nop 1\n"
"v_permlane16_swap_b32 v112, v113\n"
"v_pk_mul_f32 v[152:153], v[152:153], v[112:113] op_sel_hi:[1,0]\n"
"v_pk_mul_f32 v[154:155], v[154:155], v[112:113] op_sel_hi:[1,0]\n"
"v_pk_mul_f32 v[156:157], v[156:157], v[112:113] op_sel_hi:[1,0]\n"
"v_pk_mul_f32 v[158:159], v[158:159], v[112:113] op_sel_hi:[1,0]\n"
"v_cvt_pk_bf16_f32 v144, v152, v153\n"
"v_cvt_pk_bf16_f32 v145, v154, v155\n"
"v_cvt_pk_bf16_f32 v146, v156, v157\n"
"v_cvt_pk_bf16_f32 v147, v158, v159\n"
"buffer_store_dwordx4 v[144:147], v0, s[20:23], s44 offen sc1\n"
"v_pk_mul_f32 v[160:161], v[160:161], v[112:113] op_sel:[0,1]\n"
"v_pk_mul_f32 v[162:163], v[162:163], v[112:113] op_sel:[0,1]\n"
"v_pk_mul_f32 v[164:165], v[164:165], v[112:113] op_sel:[0,1]\n"
"v_pk_mul_f32 v[166:167], v[166:167], v[112:113] op_sel:[0,1]\n"
"v_cvt_pk_bf16_f32 v148, v160, v161\n"
"v_cvt_pk_bf16_f32 v149, v162, v163\n"
"v_cvt_pk_bf16_f32 v150, v164, v165\n"
"v_cvt_pk_bf16_f32 v151, v166, v167\n"
"buffer_store_dwordx4 v[148:151], v0, s[20:23], s45 offen sc1\n"
"s_waitcnt lgkmcnt(0)\n"
"s_barrier\n"
"s_mul_i32 s63, s67, 12\n"
"s_add_u32 m0, s63, 0\n"
"s_add_u32 s60, s63, s65\n"
"buffer_load_dwordx4 v0, s[20:23], s60 offen sc1 lds\n"
"buffer_load_dwordx4 v0, s[20:23], s60 offen offset:1024 sc1 lds\n"
"buffer_load_dwordx4 v0, s[20:23], s60 offen offset:2048 sc1 lds\n"
"buffer_load_dwordx4 v0, s[20:23], s60 offen offset:3072 sc1 lds\n"
"s_add_u32 m0, m0, 0x1000\n"
"s_add_u32 s60, s60, 0x1000\n"
"buffer_load_dwordx4 v0, s[20:23], s60 offen sc1 lds\n"
"buffer_load_dwordx4 v0, s[20:23], s60 offen offset:1024 sc1 lds\n"
"s_mov_b32 s44, 0x1491000\n"
"s_mul_i32 s51, s37, 0x4000\n"
"s_add_u32 s44, s44, s51\n"
"s_waitcnt vmcnt(6)\n"
"s_waitcnt lgkmcnt(0)\n"
"s_barrier\n"
"s_cmp_lg_u32 s96, 0\n"
"s_cbranch_scc1 .Lkw0_38\n"
"s_mov_b64 exec, 0xffff\n"
"buffer_atomic_add v10, v11, s[20:23], s52 offen\n"
"s_mov_b64 exec, -1\n"
".Lkw0_38:\n"
"ds_read_b128 v[102:105], v1 offset:8192\n"
"ds_read_b128 v[106:109], v1 offset:9216\n"
"ds_read_b128 v[110:113], v1 offset:10240\n"
"ds_read_b128 v[114:117], v1 offset:57344\n"
"ds_read_b128 v[118:121], v1 offset:11264\n"
"ds_read_b128 v[122:125], v1 offset:12288\n"
"ds_read_b128 v[126:129], v1 offset:13312\n"
"ds_read_b128 v[130:133], v1 offset:14336\n"
"; B layer 0\n"
"buffer_load_dword v190, off, s[20:23], s53 offset:0 sc1\n"
"s_mov_b32 s55, 0\n"
"s_waitcnt vmcnt(0)\n"
".Lkpoll_39:\n"
"v_readfirstlane_b32 s48, v190\n"
"s_add_u32 s55, s55, 1\n"
"s_cmp_ge_u32 s48, 16\n"
"s_cbranch_scc1 .Lkdone_40\n"
"buffer_load_dword v190, off, s[20:23], s53 offset:0 sc1\n"
"s_cmp_lt_u32 s55, 0x40000\n"
"s_waitcnt vmcnt(0)\n"
"s_cbranch_scc1 .Lkpoll_39\n"
"s_mov_b32 s48, 0x40000\n"
"buffer_store_dword v10, off, s[20:23], s48 sc1\n"
".Lkdone_40:\n"
"buffer_load_dwordx4 v[38:41], v0, s[20:23], s44 offen sc1\n"
"buffer_load_dwordx4 v[42:45], v0, s[20:23], s44 offen offset:1024 sc1\n"
"buffer_load_dwordx4 v[46:49], v0, s[20:23], s44 offen offset:2048 sc1\n"
"buffer_load_dwordx4 v[50:53], v0, s[20:23], s44 offen offset:3072 sc1\n"
"s_add_u32 s44, s44, 0x1000\n"
"buffer_load_dwordx4 v[54:57], v0, s[20:23], s44 offen sc1\n"
"buffer_load_dwordx4 v[58:61], v0, s[20:23], s44 offen offset:1024 sc1\n"
"buffer_load_dwordx4 v[62:65], v0, s[20:23], s44 offen offset:2048 sc1\n"
"buffer_load_dwordx4 v[66:69], v0, s[20:23], s44 offen offset:3072 sc1\n"
"s_waitcnt vmcnt(7) lgkmcnt(7)\n"
"v_mfma_f32_16x16x32_bf16 v[142:145], v[102:105], v[38:41], 0\n"
"s_waitcnt lgkmcnt(6)\n"
"ds_read_b128 v[134:137], v1 offset:15360\n"
"s_add_u32 s44, s44, 0x1000\n"
"buffer_load_dwordx4 v[70:73], v0, s[20:23], s44 offen sc1\n"
"v_mfma_f32_16x16x32_bf16 v[146:149], v[106:109], v[38:41], 0\n"
"s_waitcnt lgkmcnt(6)\n"
"ds_read_b128 v[138:141], v1 offset:16384\n"
"v_mfma_f32_16x16x32_bf16 v[150:153], v[110:113], v[38:41], 0\n"
"s_waitcnt lgkmcnt(6)\n"
"ds_read_b128 v[102:105], v1 offset:58368\n"
"v_mfma_f32_16x16x32_bf16 v[154:157], v[114:117], v[38:41], 0\n"
"s_waitcnt vmcnt(7) lgkmcnt(6)\n"
"ds_read_b128 v[106:109], v1 offset:17408\n"
"buffer_load_dwordx4 v[74:77], v0, s[20:23], s44 offen offset:1024 sc1\n"
"v_mfma_f32_16x16x32_bf16 v[142:145], v[118:121], v[42:45], v[142:145]\n"
"v_mfma_f32_16x16x32_bf16 v[158:161], v[114:117], v[42:45], 0\n"
"s_waitcnt lgkmcnt(6)\n"
"ds_read_b128 v[110:113], v1 offset:18432\n"
"v_mfma_f32_16x16x32_bf16 v[146:149], v[122:125], v[42:45], v[146:149]\n"
"s_waitcnt lgkmcnt(6)\n"
"ds_read_b128 v[114:117], v1 offset:19456\n"
"v_mfma_f32_16x16x32_bf16 v[150:153], v[126:129], v[42:45], v[150:153]\n"
"s_waitcnt vmcnt(7) lgkmcnt(6)\n"
"ds_read_b128 v[118:121], v1 offset:20480\n"
"v_mfma_f32_16x16x32_bf16 v[142:145], v[130:133], v[46:49], v[142:145]\n"
"s_waitcnt lgkmcnt(6)\n"
"ds_read_b128 v[122:125], v1 offset:21504\n"
"buffer_load_dwordx4 v[78:81], v0, s[20:23], s44 offen offset:2048 sc1\n"
"v_mfma_f32_16x16x32_bf16 v[146:149], v[134:137], v[46:49], v[146:149]\n"
"s_waitcnt lgkmcnt(6)\n"
"ds_read_b128 v[126:129], v1 offset:22528\n"
"v_mfma_f32_16x16x32_bf16 v[150:153], v[138:141], v[46:49], v[150:153]\n"
"s_waitcnt lgkmcnt(6)\n"
"ds_read_b128 v[130:133], v1 offset:59392\n"
"v_mfma_f32_16x16x32_bf16 v[154:157], v[102:105], v[46:49], v[154:157]\n"
"s_waitcnt vmcnt(7) lgkmcnt(6)\n"
"ds_read_b128 v[134:137], v1 offset:23552\n"
"buffer_load_dwordx4 v[82:85], v0, s[20:23], s44 offen offset:3072 sc1\n"
"v_mfma_f32_16x16x32_bf16 v[142:145], v[106:109], v[50:53], v[142:145]\n"
"v_mfma_f32_16x16x32_bf16 v[158:161], v[102:105], v[50:53], v[158:161]\n"
"s_waitcnt lgkmcnt(6)\n"
"ds_read_b128 v[138:141], v1 offset:24576\n"
"v_mfma_f32_16x16x32_bf16 v[146:149], v[110:113], v[50:53], v[146:149]\n"
"s_waitcnt lgkmcnt(6)\n"
"ds_read_b128 v[102:105], v1 offset:25600\n"
"v_mfma_f32_16x16x32_bf16 v[150:153], v[114:117], v[50:53], v[150:153]\n"
"s_waitcnt vmcnt(7) lgkmcnt(6)\n"
"ds_read_b128 v[106:109], v1 offset:26624\n"
"v_mfma_f32_16x16x32_bf16 v[142:145], v[118:121], v[54:57], v[142:145]\n"
"s_waitcnt lgkmcnt(6)\n"
"ds_read_b128 v[110:113], v1 offset:27648\n"
"s_add_u32 s44, s44, 0x1000\n"
"buffer_load_dwordx4 v[86:89], v0, s[20:23], s44 offen sc1\n"
"v_mfma_f32_16x16x32_bf16 v[146:149], v[122:125], v[54:57], v[146:149]\n"
"s_waitcnt lgkmcnt(6)\n"
"ds_read_b128 v[114:117], v1 offset:28672\n"
"v_mfma_f32_16x16x32_bf16 v[150:153], v[126:129], v[54:57], v[150:153]\n"
"s_waitcnt lgkmcnt(6)\n"
"ds_read_b128 v[118:121], v1 offset:60416\n"
"v_mfma_f32_16x16x32_bf16 v[154:157], v[130:133], v[54:57], v[154:157]\n"
"s_waitcnt vmcnt(7) lgkmcnt(6)\n"
"ds_read_b128 v[122:125], v1 offset:29696\n"
"buffer_load_dwordx4 v[90:93], v0, s[20:23], s44 offen offset:1024 sc1\n"
"v_mfma_f32_16x16x32_bf16 v[142:145], v[134:137], v[58:61], v[142:145]\n"
"v_mfma_f32_16x16x32_bf16 v[158:161], v[130:133], v[58:61], v[158:161]\n"
"s_waitcnt lgkmcnt(6)\n"
"ds_read_b128 v[126:129], v1 offset:30720\n"
"v_mfma_f32_16x16x32_bf16 v[146:149], v[138:141], v[58:61], v[146:149]\n"
"s_waitcnt lgkmcnt(6)\n"
"ds_read_b128 v[130:133], v1 offset:31744\n"
"v_mfma_f32_16x16x32_bf16 v[150:153], v[102:105], v[58:61], v[150:153]\n"
"s_waitcnt vmcnt(7) lgkmcnt(6)\n"
"ds_read_b128 v[134:137], v1 offset:32768\n"
"v_mfma_f32_16x16x32_bf16 v[142:145], v[106:109], v[62:65], v[142:145]\n"
"s_waitcnt lgkmcnt(6)\n"
"ds_read_b128 v[138:141], v1 offset:33792\n"
"buffer_load_dwordx4 v[94:97], v0, s[20:23], s44 offen offset:2048 sc1\n"
"v_mfma_f32_16x16x32_bf16 v[146:149], v[110:113], v[62:65], v[146:149]\n"
"s_waitcnt lgkmcnt(6)\n"
"ds_read_b128 v[102:105], v1 offset:34816\n"
"v_mfma_f32_16x16x32_bf16 v[150:153], v[114:117], v[62:65], v[150:153]\n"
"s_waitcnt lgkmcnt(6)\n"
"ds_read_b128 v[106:109], v1 offset:61440\n"
"v_mfma_f32_16x16x32_bf16 v[154:157], v[118:121], v[62:65], v[154:157]\n"
"s_waitcnt vmcnt(7) lgkmcnt(6)\n"
"ds_read_b128 v[110:113], v1 offset:35840\n"
"buffer_load_dwordx4 v[98:101], v0, s[20:23], s44 offen offset:3072 sc1\n"
"v_mfma_f32_16x16x32_bf16 v[142:145], v[122:125], v[66:69], v[142:145]\n"
"v_mfma_f32_16x16x32_bf16 v[158:161], v[118:121], v[66:69], v[158:161]\n"
"s_waitcnt lgkmcnt(6)\n"
"ds_read_b128 v[114:117], v1 offset:36864\n"
"v_mfma_f32_16x16x32_bf16 v[146:149], v[126:129], v[66:69], v[146:149]\n"
"s_waitcnt lgkmcnt(6)\n"
"ds_read_b128 v[118:121], v1 offset:37888\n"
"v_mfma_f32_16x16x32_bf16 v[150:153], v[130:133], v[66:69], v[150:153]\n"
"s_waitcnt vmcnt(7) lgkmcnt(6)\n"
"ds_read_b128 v[122:125], v1 offset:38912\n"
"v_mfma_f32_16x16x32_bf16 v[142:145], v[134:137], v[70:73], v[142:145]\n"
"s_waitcnt lgkmcnt(6)\n"
"ds_read_b128 v[126:129], v1 offset:39936\n"
"v_mfma_f32_16x16x32_bf16 v[146:149], v[138:141], v[70:73], v[146:149]\n"
"s_waitcnt lgkmcnt(6)\n"
"ds_read_b128 v[130:133], v1 offset:40960\n"
"v_mfma_f32_16x16x32_bf16 v[150:153], v[102:105], v[70:73], v[150:153]\n"
"s_waitcnt lgkmcnt(6)\n"
"ds_read_b128 v[134:137], v1 offset:62464\n"
"v_mfma_f32_16x16x32_bf16 v[154:157], v[106:109], v[70:73], v[154:157]\n"
"s_waitcnt vmcnt(6) lgkmcnt(6)\n"
"ds_read_b128 v[138:141], v1 offset:41984\n"
"v_mfma_f32_16x16x32_bf16 v[142:145], v[110:113], v[74:77], v[142:145]\n"
"v_mfma_f32_16x16x32_bf16 v[158:161], v[106:109], v[74:77], v[158:161]\n"
"s_waitcnt lgkmcnt(6)\n"
"ds_read_b128 v[102:105], v1 offset:43008\n"
"v_mfma_f32_16x16x32_bf16 v[146:149], v[114:117], v[74:77], v[146:149]\n"
"s_waitcnt lgkmcnt(6)\n"
"ds_read_b128 v[106:109], v1 offset:44032\n"
"v_mfma_f32_16x16x32_bf16 v[150:153], v[118:121], v[74:77], v[150:153]\n"
"s_waitcnt vmcnt(5) lgkmcnt(6)\n"
"ds_read_b128 v[110:113], v1 offset:45056\n"
"v_mfma_f32_16x16x32_bf16 v[142:145], v[122:125], v[78:81], v[142:145]\n"
"s_waitcnt lgkmcnt(6)\n"
"ds_read_b128 v[114:117], v1 offset:46080\n"
"v_mfma_f32_16x16x32_bf16 v[146:149], v[126:129], v[78:81], v[146:149]\n"
"s_waitcnt lgkmcnt(6)\n"
"ds_read_b128 v[118:121], v1 offset:47104\n"
"v_mfma_f32_16x16x32_bf16 v[150:153], v[130:133], v[78:81], v[150:153]\n"
"s_waitcnt lgkmcnt(6)\n"
"ds_read_b128 v[122:125], v1 offset:63488\n"
"v_mfma_f32_16x16x32_bf16 v[154:157], v[134:137], v[78:81], v[154:157]\n"
"s_waitcnt vmcnt(4) lgkmcnt(6)\n"
"ds_read_b128 v[126:129], v1 offset:48128\n"
"v_mfma_f32_16x16x32_bf16 v[142:145], v[138:141], v[82:85], v[142:145]\n"
"v_mfma_f32_16x16x32_bf16 v[158:161], v[134:137], v[82:85], v[158:161]\n"
"s_waitcnt lgkmcnt(6)\n"
"ds_read_b128 v[130:133], v1 offset:49152\n"
"v_mfma_f32_16x16x32_bf16 v[146:149], v[102:105], v[82:85], v[146:149]\n"
"s_waitcnt lgkmcnt(6)\n"
"ds_read_b128 v[134:137], v1 offset:50176\n"
"v_mfma_f32_16x16x32_bf16 v[150:153], v[106:109], v[82:85], v[150:153]\n"
"s_waitcnt vmcnt(3) lgkmcnt(6)\n"
"ds_read_b128 v[138:141], v1 offset:51200\n"
"v_mfma_f32_16x16x32_bf16 v[142:145], v[110:113], v[86:89], v[142:145]\n"
"s_waitcnt lgkmcnt(6)\n"
"ds_read_b128 v[102:105], v1 offset:52224\n"
"v_mfma_f32_16x16x32_bf16 v[146:149], v[114:117], v[86:89], v[146:149]\n"
"s_waitcnt lgkmcnt(6)\n"
"ds_read_b128 v[106:109], v1 offset:53248\n"
"v_mfma_f32_16x16x32_bf16 v[150:153], v[118:121], v[86:89], v[150:153]\n"
"s_waitcnt lgkmcnt(6)\n"
"ds_read_b128 v[110:113], v1 offset:64512\n"
"v_mfma_f32_16x16x32_bf16 v[154:157], v[122:125], v[86:89], v[154:157]\n"
"s_waitcnt vmcnt(2) lgkmcnt(6)\n"
"ds_read_b128 v[114:117], v1 offset:54272\n"
"v_mfma_f32_16x16x32_bf16 v[142:145], v[126:129], v[90:93], v[142:145]\n"
"v_mfma_f32_16x16x32_bf16 v[158:161], v[122:125], v[90:93], v[158:161]\n"
"s_waitcnt lgkmcnt(6)\n"
"ds_read_b128 v[118:121], v1 offset:55296\n"
"v_mfma_f32_16x16x32_bf16 v[146:149], v[130:133], v[90:93], v[146:149]\n"
"s_waitcnt lgkmcnt(6)\n"
"ds_read_b128 v[122:125], v1 offset:56320\n"
"v_mfma_f32_16x16x32_bf16 v[150:153], v[134:137], v[90:93], v[150:153]\n"
"s_waitcnt vmcnt(1) lgkmcnt(6)\n"
"v_mfma_f32_16x16x32_bf16 v[142:145], v[138:141], v[94:97], v[142:145]\n"
"s_waitcnt lgkmcnt(5)\n"
"v_mfma_f32_16x16x32_bf16 v[146:149], v[102:105], v[94:97], v[146:149]\n"
"s_waitcnt lgkmcnt(4)\n"
"s_mov_b32 s45, 0x6f1000\n"
"s_mul_i32 s51, s37, 0x3c00\n"
"s_add_u32 s45, s45, s51\n"
"v_mfma_f32_16x16x32_bf16 v[150:153], v[106:109], v[94:97], v[150:153]\n"
"s_waitcnt lgkmcnt(3)\n"
"s_add_u32 s46, s45, s69\n"
"v_mfma_f32_16x16x32_bf16 v[154:157], v[110:113], v[94:97], v[154:157]\n"
"s_waitcnt vmcnt(0) lgkmcnt(2)\n"
"s_mul_i32 s48, s35, 0x700\n"
"s_add_u32 s45, s45, s48\n"
"v_mfma_f32_16x16x32_bf16 v[142:145], v[114:117], v[98:101], v[142:145]\n"
"v_mfma_f32_16x16x32_bf16 v[158:161], v[110:113], v[98:101], v[158:161]\n"
"s_waitcnt lgkmcnt(1)\n"
"v_mfma_f32_16x16x32_bf16 v[146:149], v[118:121], v[98:101], v[146:149]\n"
"s_or_b32 s48, s53, s96\n"
"s_cbranch_scc1 .Lknoepoch_41\n"
"v_mov_b32 v192, s70\n"
"v_mov_b32 v193, s71\n"
"s_mov_b32 s48, 0x40100\n"
"s_mov_b64 exec, 1\n"
"buffer_store_dwordx2 v[192:193], off, s[20:23], s48 sc1\n"
"s_mov_b64 exec, -1\n"
".Lknoepoch_41:\n"
"s_waitcnt lgkmcnt(0)\n"
"v_mfma_f32_16x16x32_bf16 v[150:153], v[122:125], v[98:101], v[150:153]\n"
"v_cndmask_b32_e64 v154, v158, v154, s[38:39]\n"
"v_cndmask_b32_e64 v155, v159, v155, s[38:39]\n"
"v_cndmask_b32_e64 v156, v160, v156, s[38:39]\n"
"v_cndmask_b32_e64 v157, v161, v157, s[38:39]\n"
"v_pk_add_f32 v[20:21], v[20:21], v[142:143]\n"
"v_pk_add_f32 v[22:23], v[22:23], v[144:145]\n"
"v_pk_add_f32 v[24:25], v[24:25], v[146:147]\n"
"v_pk_add_f32 v[26:27], v[26:27], v[148:149]\n"
"v_pk_add_f32 v[28:29], v[28:29], v[150:151]\n"
"v_pk_add_f32 v[30:31], v[30:31], v[152:153]\n"
"v_mov_b32 v158, v154\n"
"v_mov_b32 v159, v155\n"
"v_mov_b32 v160, v156\n"
"v_mov_b32 v161, v157\n"
"v_permlane32_swap_b32 v154, v158\n"
"v_permlane32_swap_b32 v155, v159\n"
"v_permlane32_swap_b32 v156, v160\n"
"v_permlane32_swap_b32 v157, v161\n"
"v_pk_add_f32 v[32:33], v[32:33], v[154:155]\n"
"v_pk_add_f32 v[34:35], v[34:35], v[156:157]\n"
"v_pk_add_f32 v[32:33], v[32:33], v[158:159]\n"
"v_pk_add_f32 v[34:35], v[34:35], v[160:161]\n"
"v_cvt_pk_bf16_f32 v38, v20, v21\n"
"v_cvt_pk_bf16_f32 v39, v22, v23\n"
"v_cvt_pk_bf16_f32 v40, v24, v25\n"
"v_cvt_pk_bf16_f32 v41, v26, v27\n"
"buffer_store_dwordx4 v[38:41], v0, s[20:23], s45 offen sc1\n"
"v_cvt_pk_bf16_f32 v42, v28, v29\n"
"v_cvt_pk_bf16_f32 v43, v30, v31\n"
"v_cvt_pk_bf16_f32 v44, v32, v33\n"
"v_cvt_pk_bf16_f32 v45, v34, v35\n"
"v_mov_b32 v47, v42\n"
"v_mov_b32 v49, v43\n"
"s_nop 0\n"
"v_permlane16_swap_b32 v47, v46\n"
"v_permlane16_swap_b32 v49, v48\n"
"v_cndmask_b32_e64 v44, v46, v44, s[38:39]\n"
"v_cndmask_b32_e64 v45, v48, v45, s[38:39]\n"
"s_mov_b64 exec, s[40:41]\n"
"buffer_store_dwordx4 v[42:45], v0, s[20:23], s45 offen offset:1024 sc1\n"
"s_mov_b64 exec, -1\n"
"v_pk_add_f32 v[50:51], v[20:21], v[22:23]\n"
"v_pk_mul_f32 v[52:53], v[20:21], v[20:21]\n"
"v_pk_mul_f32 v[54:55], v[32:33], 0.5 op_sel_hi:[1,0]\n"
"v_pk_mul_f32 v[56:57], v[34:35], 0.5 op_sel_hi:[1,0]\n"
"v_pk_fma_f32 v[52:53], v[22:23], v[22:23], v[52:53]\n"
"v_pk_add_f32 v[50:51], v[50:51], v[24:25]\n"
"v_pk_fma_f32 v[52:53], v[24:25], v[24:25], v[52:53]\n"
"v_pk_add_f32 v[50:51], v[50:51], v[26:27]\n"
"v_pk_fma_f32 v[52:53], v[26:27], v[26:27], v[52:53]\n"
"v_pk_add_f32 v[50:51], v[50:51], v[28:29]\n"
"v_pk_fma_f32 v[52:53], v[28:29], v[28:29], v[52:53]\n"
"v_pk_add_f32 v[50:51], v[50:51], v[30:31]\n"
"v_pk_fma_f32 v[52:53], v[30:31], v[30:31], v[52:53]\n"
"v_pk_add_f32 v[50:51], v[50:51], v[54:55]\n"
"v_pk_fma_f32 v[52:53], v[54:55], v[32:33], v[52:53]\n"
"v_pk_add_f32 v[50:51], v[50:51], v[56:57]\n"
"v_pk_fma_f32 v[52:53], v[56:57], v[34:35], v[52:53]\n"
"v_add_f32 v50, v50, v51\n"
"v_add_f32 v51, v52, v53\n"
"s_nop 1\n"
"v_permlane16_swap_b32 v50, v51\n"
"v_add_f32 v50, v50, v51\n"
"v_mov_b32 v51, v50\n"
"s_nop 1\n"
"v_permlane32_swap_b32 v50, v51\n"
"v_add_f32 v50, v50, v51\n"
"s_mov_b64 exec, s[38:39]\n"
"buffer_store_dword v50, v36, s[20:23], s46 offen sc1\n"
"s_mov_b64 exec, -1\n"
"s_barrier\n"
"buffer_load_dwordx4 v0, s[20:23], s60 offen offset:2048 sc1 lds\n"
"buffer_load_dwordx4 v0, s[20:23], s60 offen offset:3072 sc1 lds\n"
"s_add_u32 m0, m0, 0x1000\n"
"s_add_u32 s60, s60, 0x1000\n"
"buffer_load_dwordx4 v0, s[20:23], s60 offen sc1 lds\n"
"buffer_load_dwordx4 v0, s[20:23], s60 offen offset:1024 sc1 lds\n"
"buffer_load_dwordx4 v0, s[20:23], s60 offen offset:2048 sc1 lds\n"
"buffer_load_dwordx4 v0, s[20:23], s60 offen offset:3072 sc1 lds\n"
"s_mov_b32 s44, 0x6f1000\n"
"s_mul_i32 s51, s97, 0x1e000\n"
"s_add_u32 s44, s44, s51\n"
"s_mul_i32 s51, s96, 0x7800\n"
"s_add_u32 s44, s44, s51\n"
"s_add_u32 s45, s44, 0x3c00\n"
"s_add_u32 s49, s44, 0x3800\n"
"s_add_u32 s50, s45, 0x3800\n"
"s_mov_b32 s46, 0x6f4800\n"
"s_mul_i32 s51, s37, 0x3c00\n"
"s_add_u32 s46, s46, s51\n"
"s_waitcnt vmcnt(6)\n"
"s_waitcnt lgkmcnt(0)\n"
"s_barrier\n"
"s_cmp_lg_u32 s96, 0\n"
"s_cbranch_scc1 .Lkw0_42\n"
"s_mov_b64 exec, 0xffff\n"
"buffer_atomic_add v10, v11, s[20:23], s52 offen\n"
"s_mov_b64 exec, -1\n"
".Lkw0_42:\n"
"s_add_u32 s62, s54, 0x3000\n"
"buffer_load_dwordx4 a[48:51], v0, s[20:23], s62 offen sc1\n"
"buffer_load_dwordx4 a[52:55], v0, s[20:23], s62 offen offset:1024 sc1\n"
"buffer_load_dwordx4 a[56:59], v0, s[20:23], s62 offen offset:2048 sc1\n"
"buffer_load_dwordx4 a[60:63], v0, s[20:23], s62 offen offset:3072 sc1\n"
"s_add_u32 s62, s54, 0x4000\n"
"buffer_load_dwordx4 a[64:67], v0, s[20:23], s62 offen sc1\n"
"buffer_load_dwordx4 a[68:71], v0, s[20:23], s62 offen offset:1024 sc1\n"
"s_waitcnt vmcnt(6)\n"
"s_waitcnt lgkmcnt(0)\n"
"s_barrier\n"
"s_add_u32 s48, s96, 4\n"
"s_min_u32 s48, s48, 6\n"
"s_lshl_b32 s51, s48, 10\n"
"v_add_u32 v246, s67, v0\n"
"v_add_u32 v247, s51, v0\n"
"v_add_u32 v248, 0x1f800, v246\n"
"v_add_u32 v249, 0x1f800, v247\n"
"s_lshl_b32 s51, s96, 6\n"
"v_add_u32 v251, s51, v4\n"
"s_lshl_b32 s51, s48, 6\n"
"v_add_u32 v252, s51, v4\n"
"s_cmp_lt_u32 s96, 2\n"
"s_cselect_b32 s51, 0xe000, 0\n"
"v_subrev_u32 v250, s51, v248\n"
"ds_read_b128 v[174:177], v246\n"
"ds_read_b128 v[194:197], v247\n"
"ds_read_b128 v[198:201], v246 offset:7168\n"
"ds_read_b128 v[206:209], v0\n"
"ds_read_b128 v[210:213], v0 offset:1024\n"
"ds_read_b128 v[214:217], v0 offset:2048\n"
"ds_read_b128 v[218:221], v0 offset:3072\n"
"; C layer 0\n"
"buffer_load_dword v230, off, s[20:23], s53 offset:0 sc1\n"
"s_mov_b32 s55, 0\n"
"s_waitcnt vmcnt(0)\n"
".Lkpoll_43:\n"
"v_readfirstlane_b32 s48, v230\n"
"s_add_u32 s55, s55, 1\n"
"s_cmp_ge_u32 s48, 32\n"
"s_cbranch_scc1 .Lkdone_44\n"
"buffer_load_dword v230, off, s[20:23], s53 offset:0 sc1\n"
"s_cmp_lt_u32 s55, 0x40000\n"
"s_waitcnt vmcnt(0)\n"
"s_cbranch_scc1 .Lkpoll_43\n"
"s_mov_b32 s48, 0x40000\n"
"buffer_store_dword v10, off, s[20:23], s48 sc1\n"
".Lkdone_44:\n"
"buffer_load_dwordx4 v[38:41], v0, s[20:23], s44 offen sc1\n"
"buffer_load_dwordx4 v[42:45], v0, s[20:23], s45 offen sc1\n"
"buffer_load_dwordx4 v[230:233], v0, s[20:23], s49 offen sc1\n"
"buffer_load_dwordx4 v[234:237], v0, s[20:23], s50 offen sc1\n"
"buffer_load_dwordx4 v[46:49], v0, s[20:23], s44 offen offset:1024 sc1\n"
"buffer_load_dwordx4 v[50:53], v0, s[20:23], s45 offen offset:1024 sc1\n"
"buffer_load_dwordx4 v[54:57], v0, s[20:23], s44 offen offset:2048 sc1\n"
"buffer_load_dwordx4 v[58:61], v0, s[20:23], s45 offen offset:2048 sc1\n"
"buffer_load_dwordx4 v[62:65], v0, s[20:23], s44 offen offset:3072 sc1\n"
"buffer_load_dwordx4 v[66:69], v0, s[20:23], s45 offen offset:3072 sc1\n"
"s_add_u32 s44, s44, 0x1000\n"
"buffer_load_dwordx4 v[70:73], v0, s[20:23], s44 offen sc1\n"
"s_add_u32 s45, s45, 0x1000\n"
"buffer_load_dwordx4 v[74:77], v0, s[20:23], s45 offen sc1\n"
"buffer_load_dwordx4 v[78:81], v0, s[20:23], s44 offen offset:1024 sc1\n"
"buffer_load_dwordx4 v[82:85], v0, s[20:23], s45 offen offset:1024 sc1\n"
"s_waitcnt vmcnt(13) lgkmcnt(3)\n"
"v_mfma_f32_16x16x32_bf16 v[150:153], v[206:209], v[38:41], 0\n"
"s_waitcnt vmcnt(12)\n"
"v_mfma_f32_16x16x32_bf16 v[178:181], v[206:209], v[42:45], 0\n"
"ds_read_b128 v[222:225], v0 offset:7168\n"
"v_mfma_f32_16x16x32_bf16 v[166:169], v[174:177], a[252:255], 0\n"
"s_waitcnt lgkmcnt(3)\n"
"ds_read_b128 v[202:205], v247 offset:7168\n"
"v_mfma_f32_16x16x32_bf16 v[154:157], v[210:213], v[38:41], 0\n"
"v_mfma_f32_16x16x32_bf16 v[182:185], v[210:213], v[42:45], 0\n"
"ds_read_b128 v[226:229], v0 offset:8192\n"
"v_mfma_f32_16x16x32_bf16 v[170:173], v[194:197], a[252:255], 0\n"
"s_waitcnt lgkmcnt(4)\n"
"ds_read_b128 v[174:177], v246 offset:14336\n"
"buffer_load_dwordx4 v[86:89], v0, s[20:23], s44 offen offset:2048 sc1\n"
"v_mfma_f32_16x16x32_bf16 v[158:161], v[214:217], v[38:41], 0\n"
"v_mfma_f32_16x16x32_bf16 v[186:189], v[214:217], v[42:45], 0\n"
"ds_read_b128 v[206:209], v0 offset:9216\n"
"v_mfma_f32_16x16x32_bf16 v[166:169], v[198:201], a[252:255], v[166:169]\n"
"s_waitcnt lgkmcnt(5)\n"
"ds_read_b128 v[194:197], v247 offset:14336\n"
"v_mfma_f32_16x16x32_bf16 v[162:165], v[218:221], v[38:41], 0\n"
"v_mfma_f32_16x16x32_bf16 v[190:193], v[218:221], v[42:45], 0\n"
"s_waitcnt lgkmcnt(4)\n"
"ds_read_b128 v[210:213], v0 offset:10240\n"
"v_mfma_f32_16x16x32_bf16 v[170:173], v[202:205], a[252:255], v[170:173]\n"
"s_waitcnt vmcnt(10)\n"
"ds_read_b128 v[198:201], v246 offset:21504\n"
"buffer_load_dwordx4 v[90:93], v0, s[20:23], s45 offen offset:2048 sc1\n"
"v_mfma_f32_16x16x32_bf16 v[150:153], v[222:225], v[46:49], v[150:153]\n"
"s_waitcnt vmcnt(10)\n"
"v_mfma_f32_16x16x32_bf16 v[178:181], v[222:225], v[50:53], v[178:181]\n"
"s_waitcnt lgkmcnt(4)\n"
"ds_read_b128 v[214:217], v0 offset:14336\n"
"v_mfma_f32_16x16x32_bf16 v[166:169], v[174:177], a[252:255], v[166:169]\n"
"ds_read_b128 v[202:205], v247 offset:21504\n"
"v_add_f32 v230, v230, v232\n"
"v_mfma_f32_16x16x32_bf16 v[154:157], v[226:229], v[46:49], v[154:157]\n"
"v_mfma_f32_16x16x32_bf16 v[182:185], v[226:229], v[50:53], v[182:185]\n"
"s_waitcnt lgkmcnt(4)\n"
"ds_read_b128 v[218:221], v0 offset:15360\n"
"v_mfma_f32_16x16x32_bf16 v[170:173], v[194:197], a[252:255], v[170:173]\n"
"ds_read_b128 v[174:177], v246 offset:28672\n"
"buffer_load_dwordx4 v[94:97], v0, s[20:23], s44 offen offset:3072 sc1\n"
"v_add_f32 v234, v234, v236\n"
"v_mfma_f32_16x16x32_bf16 v[158:161], v[206:209], v[46:49], v[158:161]\n"
"v_mfma_f32_16x16x32_bf16 v[186:189], v[206:209], v[50:53], v[186:189]\n"
"s_waitcnt lgkmcnt(4)\n"
"ds_read_b128 v[222:225], v0 offset:16384\n"
"v_mfma_f32_16x16x32_bf16 v[166:169], v[198:201], a[252:255], v[166:169]\n"
"ds_read_b128 v[194:197], v247 offset:28672\n"
"v_permlane16_swap_b32 v230, v234\n"
"v_mfma_f32_16x16x32_bf16 v[162:165], v[210:213], v[46:49], v[162:165]\n"
"v_mfma_f32_16x16x32_bf16 v[190:193], v[210:213], v[50:53], v[190:193]\n"
"s_waitcnt lgkmcnt(4)\n"
"ds_read_b128 v[226:229], v0 offset:17408\n"
"v_mfma_f32_16x16x32_bf16 v[170:173], v[202:205], a[252:255], v[170:173]\n"
"s_waitcnt vmcnt(10)\n"
"ds_read_b128 v[198:201], v246 offset:35840\n"
"buffer_load_dwordx4 v[98:101], v0, s[20:23], s45 offen offset:3072 sc1\n"
"v_add_f32 v230, v230, v234\n"
"v_mfma_f32_16x16x32_bf16 v[150:153], v[214:217], v[54:57], v[150:153]\n"
"s_waitcnt vmcnt(10)\n"
"v_mfma_f32_16x16x32_bf16 v[178:181], v[214:217], v[58:61], v[178:181]\n"
"s_waitcnt lgkmcnt(4)\n"
"ds_read_b128 v[206:209], v0 offset:21504\n"
"v_mfma_f32_16x16x32_bf16 v[166:169], v[174:177], a[252:255], v[166:169]\n"
"ds_read_b128 v[202:205], v247 offset:35840\n"
"v_mov_b32 v234, v230\n"
"v_mfma_f32_16x16x32_bf16 v[154:157], v[218:221], v[54:57], v[154:157]\n"
"v_mfma_f32_16x16x32_bf16 v[182:185], v[218:221], v[58:61], v[182:185]\n"
"s_waitcnt lgkmcnt(4)\n"
"ds_read_b128 v[210:213], v0 offset:22528\n"
"v_mfma_f32_16x16x32_bf16 v[170:173], v[194:197], a[252:255], v[170:173]\n"
"ds_read_b128 v[174:177], v246 offset:43008\n"
"s_add_u32 s44, s44, 0x1000\n"
"buffer_load_dwordx4 v[102:105], v0, s[20:23], s44 offen sc1\n"
"v_permlane32_swap_b32 v230, v234\n"
"v_mfma_f32_16x16x32_bf16 v[158:161], v[222:225], v[54:57], v[158:161]\n"
"v_mfma_f32_16x16x32_bf16 v[186:189], v[222:225], v[58:61], v[186:189]\n"
"s_waitcnt lgkmcnt(4)\n"
"ds_read_b128 v[214:217], v0 offset:23552\n"
"v_mfma_f32_16x16x32_bf16 v[166:169], v[198:201], a[252:255], v[166:169]\n"
"ds_read_b128 v[194:197], v247 offset:43008\n"
"v_add_f32 v230, v230, v234\n"
"v_mfma_f32_16x16x32_bf16 v[162:165], v[226:229], v[54:57], v[162:165]\n"
"v_mfma_f32_16x16x32_bf16 v[190:193], v[226:229], v[58:61], v[190:193]\n"
"s_waitcnt lgkmcnt(4)\n"
"ds_read_b128 v[218:221], v0 offset:24576\n"
"v_mfma_f32_16x16x32_bf16 v[170:173], v[202:205], a[252:255], v[170:173]\n"
"s_waitcnt vmcnt(10)\n"
"ds_read_b128 v[198:201], v246 offset:50176\n"
"s_add_u32 s45, s45, 0x1000\n"
"buffer_load_dwordx4 v[106:109], v0, s[20:23], s45 offen sc1\n"
"v_mul_f32 v242, 0xbb124925, v230\n"
"v_mfma_f32_16x16x32_bf16 v[150:153], v[206:209], v[62:65], v[150:153]\n"
"s_waitcnt vmcnt(10)\n"
"v_mfma_f32_16x16x32_bf16 v[178:181], v[206:209], v[66:69], v[178:181]\n"
"s_waitcnt lgkmcnt(4)\n"
"ds_read_b128 v[222:225], v0 offset:28672\n"
"v_mfma_f32_16x16x32_bf16 v[166:169], v[174:177], a[252:255], v[166:169]\n"
"ds_read_b128 v[202:205], v247 offset:50176\n"
"v_mov_b32 v243, v242\n"
"v_mfma_f32_16x16x32_bf16 v[154:157], v[210:213], v[62:65], v[154:157]\n"
"v_mfma_f32_16x16x32_bf16 v[182:185], v[210:213], v[66:69], v[182:185]\n"
"s_waitcnt lgkmcnt(4)\n"
"ds_read_b128 v[226:229], v0 offset:29696\n"
"v_mfma_f32_16x16x32_bf16 v[170:173], v[194:197], a[252:255], v[170:173]\n"
"ds_read_b128 v[174:177], v246 offset:57344\n"
"buffer_load_dwordx4 v[110:113], v0, s[20:23], s44 offen offset:1024 sc1\n"
"v_permlane16_swap_b32 v242, v243\n"
"v_mfma_f32_16x16x32_bf16 v[158:161], v[214:217], v[62:65], v[158:161]\n"
"v_mfma_f32_16x16x32_bf16 v[186:189], v[214:217], v[66:69], v[186:189]\n"
"s_waitcnt lgkmcnt(4)\n"
"ds_read_b128 v[206:209], v0 offset:30720\n"
"v_mfma_f32_16x16x32_bf16 v[166:169], v[198:201], a[252:255], v[166:169]\n"
"ds_read_b128 v[194:197], v247 offset:57344\n"
"v_mfma_f32_16x16x32_bf16 v[162:165], v[218:221], v[62:65], v[162:165]\n"
"v_mfma_f32_16x16x32_bf16 v[190:193], v[218:221], v[66:69], v[190:193]\n"
"s_waitcnt lgkmcnt(4)\n"
"ds_read_b128 v[210:213], v0 offset:31744\n"
"v_mfma_f32_16x16x32_bf16 v[170:173], v[202:205], a[252:255], v[170:173]\n"
"s_waitcnt vmcnt(10)\n"
"ds_read_b128 v[198:201], v246 offset:64512\n"
"buffer_load_dwordx4 v[114:117], v0, s[20:23], s45 offen offset:1024 sc1\n"
"v_mfma_f32_16x16x32_bf16 v[150:153], v[222:225], v[70:73], v[150:153]\n"
"s_waitcnt vmcnt(10)\n"
"v_mfma_f32_16x16x32_bf16 v[178:181], v[222:225], v[74:77], v[178:181]\n"
"s_waitcnt lgkmcnt(4)\n"
"ds_read_b128 v[214:217], v0 offset:35840\n"
"v_mfma_f32_16x16x32_bf16 v[166:169], v[174:177], a[252:255], v[166:169]\n"
"ds_read_b128 v[202:205], v247 offset:64512\n"
"v_mfma_f32_16x16x32_bf16 v[154:157], v[226:229], v[70:73], v[154:157]\n"
"v_mfma_f32_16x16x32_bf16 v[182:185], v[226:229], v[74:77], v[182:185]\n"
"s_waitcnt lgkmcnt(4)\n"
"ds_read_b128 v[218:221], v0 offset:36864\n"
"v_mfma_f32_16x16x32_bf16 v[170:173], v[194:197], a[252:255], v[170:173]\n"
"ds_read_b128 v[174:177], v250\n"
"buffer_load_dwordx4 v[118:121], v0, s[20:23], s44 offen offset:2048 sc1\n"
"v_mfma_f32_16x16x32_bf16 v[158:161], v[206:209], v[70:73], v[158:161]\n"
"v_mfma_f32_16x16x32_bf16 v[186:189], v[206:209], v[74:77], v[186:189]\n"
"s_waitcnt lgkmcnt(4)\n"
"ds_read_b128 v[222:225], v0 offset:37888\n"
"v_mfma_f32_16x16x32_bf16 v[166:169], v[198:201], a[252:255], v[166:169]\n"
"ds_read_b128 v[194:197], v249\n"
"v_mfma_f32_16x16x32_bf16 v[162:165], v[210:213], v[70:73], v[162:165]\n"
"v_mfma_f32_16x16x32_bf16 v[190:193], v[210:213], v[74:77], v[190:193]\n"
"s_waitcnt lgkmcnt(4)\n"
"ds_read_b128 v[226:229], v0 offset:38912\n"
"v_mfma_f32_16x16x32_bf16 v[170:173], v[202:205], a[252:255], v[170:173]\n"
"s_waitcnt vmcnt(10)\n"
"ds_read_b128 v[198:201], v248 offset:7168\n"
"buffer_load_dwordx4 v[122:125], v0, s[20:23], s45 offen offset:2048 sc1\n"
"v_mfma_f32_16x16x32_bf16 v[150:153], v[214:217], v[78:81], v[150:153]\n"
"s_waitcnt vmcnt(10)\n"
"v_mfma_f32_16x16x32_bf16 v[178:181], v[214:217], v[82:85], v[178:181]\n"
"s_waitcnt lgkmcnt(4)\n"
"ds_read_b128 v[206:209], v0 offset:43008\n"
"v_mfma_f32_16x16x32_bf16 v[166:169], v[174:177], a[252:255], v[166:169]\n"
"ds_read_b128 v[202:205], v249 offset:7168\n"
"v_mfma_f32_16x16x32_bf16 v[154:157], v[218:221], v[78:81], v[154:157]\n"
"v_mfma_f32_16x16x32_bf16 v[182:185], v[218:221], v[82:85], v[182:185]\n"
"s_waitcnt lgkmcnt(4)\n"
"ds_read_b128 v[210:213], v0 offset:44032\n"
"v_mfma_f32_16x16x32_bf16 v[170:173], v[194:197], a[252:255], v[170:173]\n"
"ds_read_b128 v[174:177], v248 offset:14336\n"
"buffer_load_dwordx4 v[126:129], v0, s[20:23], s44 offen offset:3072 sc1\n"
"v_mfma_f32_16x16x32_bf16 v[158:161], v[222:225], v[78:81], v[158:161]\n"
"v_mfma_f32_16x16x32_bf16 v[186:189], v[222:225], v[82:85], v[186:189]\n"
"s_waitcnt lgkmcnt(4)\n"
"ds_read_b128 v[214:217], v0 offset:45056\n"
"v_mfma_f32_16x16x32_bf16 v[166:169], v[198:201], a[252:255], v[166:169]\n"
"ds_read_b128 v[194:197], v249 offset:14336\n"
"v_mfma_f32_16x16x32_bf16 v[162:165], v[226:229], v[78:81], v[162:165]\n"
"v_mfma_f32_16x16x32_bf16 v[190:193], v[226:229], v[82:85], v[190:193]\n"
"s_waitcnt lgkmcnt(4)\n"
"ds_read_b128 v[218:221], v0 offset:46080\n"
"v_mfma_f32_16x16x32_bf16 v[170:173], v[202:205], a[252:255], v[170:173]\n"
"s_waitcnt vmcnt(10)\n"
"ds_read_b128 v[198:201], v248 offset:21504\n"
"buffer_load_dwordx4 v[130:133], v0, s[20:23], s45 offen offset:3072 sc1\n"
"v_mfma_f32_16x16x32_bf16 v[150:153], v[206:209], v[86:89], v[150:153]\n"
"s_waitcnt vmcnt(10)\n"
"v_mfma_f32_16x16x32_bf16 v[178:181], v[206:209], v[90:93], v[178:181]\n"
"s_waitcnt lgkmcnt(4)\n"
"ds_read_b128 v[222:225], v0 offset:50176\n"
"v_mfma_f32_16x16x32_bf16 v[166:169], v[174:177], a[252:255], v[166:169]\n"
"ds_read_b128 v[202:205], v249 offset:21504\n"
"v_mfma_f32_16x16x32_bf16 v[154:157], v[210:213], v[86:89], v[154:157]\n"
"v_mfma_f32_16x16x32_bf16 v[182:185], v[210:213], v[90:93], v[182:185]\n"
"s_waitcnt lgkmcnt(4)\n"
"ds_read_b128 v[226:229], v0 offset:51200\n"
"v_mfma_f32_16x16x32_bf16 v[170:173], v[194:197], a[252:255], v[170:173]\n"
"s_add_u32 s44, s44, 0x1000\n"
"buffer_load_dwordx4 v[134:137], v0, s[20:23], s44 offen sc1\n"
"v_mfma_f32_16x16x32_bf16 v[158:161], v[214:217], v[86:89], v[158:161]\n"
"v_mfma_f32_16x16x32_bf16 v[186:189], v[214:217], v[90:93], v[186:189]\n"
"s_waitcnt lgkmcnt(3)\n"
"ds_read_b128 v[206:209], v0 offset:52224\n"
"v_mfma_f32_16x16x32_bf16 v[166:169], v[198:201], a[252:255], v[166:169]\n"
"v_mfma_f32_16x16x32_bf16 v[162:165], v[218:221], v[86:89], v[162:165]\n"
"v_mfma_f32_16x16x32_bf16 v[190:193], v[218:221], v[90:93], v[190:193]\n"
"s_waitcnt lgkmcnt(2)\n"
"ds_read_b128 v[210:213], v0 offset:53248\n"
"v_mfma_f32_16x16x32_bf16 v[170:173], v[202:205], a[252:255], v[170:173]\n"
"s_waitcnt vmcnt(10)\n"
"s_add_u32 s45, s45, 0x1000\n"
"buffer_load_dwordx4 v[138:141], v0, s[20:23], s45 offen sc1\n"
"v_mfma_f32_16x16x32_bf16 v[150:153], v[222:225], v[94:97], v[150:153]\n"
"s_waitcnt vmcnt(10)\n"
"v_mfma_f32_16x16x32_bf16 v[178:181], v[222:225], v[98:101], v[178:181]\n"
"s_waitcnt lgkmcnt(2)\n"
"ds_read_b128 v[214:217], v0 offset:57344\n"
"v_mfma_f32_16x16x32_bf16 v[154:157], v[226:229], v[94:97], v[154:157]\n"
"v_mfma_f32_16x16x32_bf16 v[182:185], v[226:229], v[98:101], v[182:185]\n"
"s_waitcnt lgkmcnt(2)\n"
"ds_read_b128 v[218:221], v0 offset:58368\n"
"ds_write_b128 v251, v[166:169] offset:512\n"
"ds_write_b128 v252, v[170:173] offset:512\n"
"buffer_load_dwordx4 v[142:145], v0, s[20:23], s44 offen offset:1024 sc1\n"
"v_mfma_f32_16x16x32_bf16 v[158:161], v[206:209], v[94:97], v[158:161]\n"
"v_mfma_f32_16x16x32_bf16 v[186:189], v[206:209], v[98:101], v[186:189]\n"
"ds_read_b128 v[222:225], v0 offset:59392\n"
"s_waitcnt lgkmcnt(1)\n"
"s_barrier\n"
"ds_read_b128 v[194:197], v4 offset:768\n"
"ds_read_b128 v[198:201], v4 offset:832\n"
"ds_read_b128 v[202:205], v4 offset:896\n"
"v_mfma_f32_16x16x32_bf16 v[162:165], v[210:213], v[94:97], v[162:165]\n"
"v_mfma_f32_16x16x32_bf16 v[190:193], v[210:213], v[98:101], v[190:193]\n"
"s_waitcnt vmcnt(10)\n"
"ds_read_b128 v[226:229], v0 offset:60416\n"
"buffer_load_dwordx4 v[146:149], v0, s[20:23], s45 offen offset:1024 sc1\n"
"v_mfma_f32_16x16x32_bf16 v[150:153], v[214:217], v[102:105], v[150:153]\n"
"s_waitcnt vmcnt(10)\n"
"v_mfma_f32_16x16x32_bf16 v[178:181], v[214:217], v[106:109], v[178:181]\n"
"ds_read_b128 v[206:209], v0 offset:64512\n"
"v_mfma_f32_16x16x32_bf16 v[154:157], v[218:221], v[102:105], v[154:157]\n"
"v_mfma_f32_16x16x32_bf16 v[182:185], v[218:221], v[106:109], v[182:185]\n"
"s_waitcnt lgkmcnt(5)\n"
"ds_read_b128 v[210:213], v1\n"
"buffer_load_dwordx4 v[238:241], v0, s[20:23], s46 offen sc1\n"
"v_mfma_f32_16x16x32_bf16 v[158:161], v[222:225], v[102:105], v[158:161]\n"
"v_mfma_f32_16x16x32_bf16 v[186:189], v[222:225], v[106:109], v[186:189]\n"
"s_waitcnt lgkmcnt(2)\n"
"ds_read_b128 v[214:217], v1 offset:1024\n"
"v_mfma_f32_16x16x32_bf16 v[162:165], v[226:229], v[102:105], v[162:165]\n"
"v_mfma_f32_16x16x32_bf16 v[190:193], v[226:229], v[106:109], v[190:193]\n"
"s_waitcnt vmcnt(10) lgkmcnt(2)\n"
"ds_read_b128 v[218:221], v1 offset:2048\n"
"buffer_load_dwordx4 a[72:75], v0, s[20:23], s62 offen offset:2048 sc1\n"
"v_mfma_f32_16x16x32_bf16 v[150:153], v[206:209], v[110:113], v[150:153]\n"
"s_waitcnt vmcnt(10)\n"
"v_mfma_f32_16x16x32_bf16 v[178:181], v[206:209], v[114:117], v[178:181]\n"
"s_waitcnt lgkmcnt(2)\n"
"ds_read_b128 v[222:225], v1 offset:6144\n"
"v_mul_f32 v166, v194, v242\n"
"v_mul_f32 v167, v195, v242\n"
"v_mfma_f32_16x16x32_bf16 v[154:157], v[210:213], v[110:113], v[154:157]\n"
"v_mfma_f32_16x16x32_bf16 v[182:185], v[210:213], v[114:117], v[182:185]\n"
"s_waitcnt lgkmcnt(2)\n"
"ds_read_b128 v[226:229], v1 offset:7168\n"
"buffer_load_dwordx4 a[76:79], v0, s[20:23], s62 offen offset:3072 sc1\n"
"v_mul_f32 v168, v196, v242\n"
"v_mul_f32 v169, v197, v242\n"
"v_mfma_f32_16x16x32_bf16 v[158:161], v[214:217], v[110:113], v[158:161]\n"
"v_mfma_f32_16x16x32_bf16 v[186:189], v[214:217], v[114:117], v[186:189]\n"
"s_waitcnt lgkmcnt(2)\n"
"ds_read_b128 v[206:209], v2\n"
"v_mul_f32 v194, v194, v243\n"
"v_mul_f32 v195, v195, v243\n"
"v_mfma_f32_16x16x32_bf16 v[162:165], v[218:221], v[110:113], v[162:165]\n"
"v_mfma_f32_16x16x32_bf16 v[190:193], v[218:221], v[114:117], v[190:193]\n"
"s_waitcnt vmcnt(10) lgkmcnt(2)\n"
"ds_read_b128 v[210:213], v2 offset:1024\n"
"s_add_u32 s62, s54, 0x5000\n"
"buffer_load_dwordx4 a[80:83], v0, s[20:23], s62 offen sc1\n"
"v_mul_f32 v196, v196, v243\n"
"v_mul_f32 v197, v197, v243\n"
"v_mfma_f32_16x16x32_bf16 v[150:153], v[222:225], v[118:121], v[150:153]\n"
"s_waitcnt vmcnt(10)\n"
"v_mfma_f32_16x16x32_bf16 v[178:181], v[222:225], v[122:125], v[178:181]\n"
"s_waitcnt lgkmcnt(2)\n"
"ds_read_b128 v[214:217], v2 offset:5120\n"
"v_mul_f32 v170, v198, v242\n"
"v_mul_f32 v171, v199, v242\n"
"v_mfma_f32_16x16x32_bf16 v[154:157], v[226:229], v[118:121], v[154:157]\n"
"v_mfma_f32_16x16x32_bf16 v[182:185], v[226:229], v[122:125], v[182:185]\n"
"s_waitcnt lgkmcnt(2)\n"
"ds_read_b128 v[218:221], v2 offset:6144\n"
"buffer_load_dwordx4 a[84:87], v0, s[20:23], s62 offen offset:1024 sc1\n"
"v_mul_f32 v172, v200, v242\n"
"v_mul_f32 v173, v201, v242\n"
"v_mfma_f32_16x16x32_bf16 v[158:161], v[206:209], v[118:121], v[158:161]\n"
"v_mfma_f32_16x16x32_bf16 v[186:189], v[206:209], v[122:125], v[186:189]\n"
"s_waitcnt lgkmcnt(2)\n"
"ds_read_b128 v[222:225], v2 offset:7168\n"
"v_mul_f32 v198, v198, v243\n"
"v_mul_f32 v199, v199, v243\n"
"v_mfma_f32_16x16x32_bf16 v[162:165], v[210:213], v[118:121], v[162:165]\n"
"v_mfma_f32_16x16x32_bf16 v[190:193], v[210:213], v[122:125], v[190:193]\n"
"s_waitcnt vmcnt(10) lgkmcnt(2)\n"
"ds_read_b128 v[226:229], v2 offset:8192\n"
"buffer_load_dwordx4 a[88:91], v0, s[20:23], s62 offen offset:2048 sc1\n"
"v_mul_f32 v200, v200, v243\n"
"v_mul_f32 v201, v201, v243\n"
"v_mfma_f32_16x16x32_bf16 v[150:153], v[214:217], v[126:129], v[150:153]\n"
"s_waitcnt vmcnt(10)\n"
"v_mfma_f32_16x16x32_bf16 v[178:181], v[214:217], v[130:133], v[178:181]\n"
"s_waitcnt lgkmcnt(2)\n"
"ds_read_b128 v[206:209], v2 offset:12288\n"
"v_mul_f32 v174, v202, v242\n"
"v_mul_f32 v175, v203, v242\n"
"v_mfma_f32_16x16x32_bf16 v[154:157], v[218:221], v[126:129], v[154:157]\n"
"v_mfma_f32_16x16x32_bf16 v[182:185], v[218:221], v[130:133], v[182:185]\n"
"s_waitcnt lgkmcnt(2)\n"
"ds_read_b128 v[210:213], v2 offset:13312\n"
"buffer_load_dwordx4 a[92:95], v0, s[20:23], s62 offen offset:3072 sc1\n"
"v_mul_f32 v176, v204, v242\n"
"v_mul_f32 v177, v205, v242\n"
"v_mfma_f32_16x16x32_bf16 v[158:161], v[222:225], v[126:129], v[158:161]\n"
"v_mfma_f32_16x16x32_bf16 v[186:189], v[222:225], v[130:133], v[186:189]\n"
"s_waitcnt lgkmcnt(2)\n"
"ds_read_b128 v[214:217], v2 offset:14336\n"
"v_mul_f32 v202, v202, v243\n"
"v_mul_f32 v203, v203, v243\n"
"v_mfma_f32_16x16x32_bf16 v[162:165], v[226:229], v[126:129], v[162:165]\n"
"v_mfma_f32_16x16x32_bf16 v[190:193], v[226:229], v[130:133], v[190:193]\n"
"s_waitcnt vmcnt(10) lgkmcnt(2)\n"
"ds_read_b128 v[218:221], v2 offset:15360\n"
"s_add_u32 s62, s54, 0x6000\n"
"buffer_load_dwordx4 a[96:99], v0, s[20:23], s62 offen sc1\n"
"v_mul_f32 v204, v204, v243\n"
"v_mul_f32 v205, v205, v243\n"
"v_mfma_f32_16x16x32_bf16 v[150:153], v[206:209], v[134:137], v[150:153]\n"
"s_waitcnt vmcnt(10)\n"
"v_mfma_f32_16x16x32_bf16 v[178:181], v[206:209], v[138:141], v[178:181]\n"
"s_waitcnt lgkmcnt(2)\n"
"ds_read_b128 v[222:225], v2 offset:19456\n"
"v_mfma_f32_16x16x32_bf16 v[154:157], v[210:213], v[134:137], v[154:157]\n"
"v_mfma_f32_16x16x32_bf16 v[182:185], v[210:213], v[138:141], v[182:185]\n"
"s_waitcnt lgkmcnt(2)\n"
"ds_read_b128 v[226:229], v2 offset:20480\n"
"buffer_load_dwordx4 a[100:103], v0, s[20:23], s62 offen offset:1024 sc1\n"
"v_mfma_f32_16x16x32_bf16 v[158:161], v[214:217], v[134:137], v[158:161]\n"
"v_mfma_f32_16x16x32_bf16 v[186:189], v[214:217], v[138:141], v[186:189]\n"
"s_waitcnt lgkmcnt(2)\n"
"ds_read_b128 v[206:209], v2 offset:21504\n"
"v_mfma_f32_16x16x32_bf16 v[162:165], v[218:221], v[134:137], v[162:165]\n"
"v_mfma_f32_16x16x32_bf16 v[190:193], v[218:221], v[138:141], v[190:193]\n"
"s_waitcnt vmcnt(10) lgkmcnt(2)\n"
"ds_read_b128 v[210:213], v2 offset:22528\n"
"buffer_load_dwordx4 a[104:107], v0, s[20:23], s62 offen offset:2048 sc1\n"
"v_mfma_f32_16x16x32_bf16 v[150:153], v[222:225], v[142:145], v[150:153]\n"
"s_waitcnt vmcnt(10)\n"
"v_mfma_f32_16x16x32_bf16 v[178:181], v[222:225], v[146:149], v[178:181]\n"
"ds_read_b128 v[214:217], v0 offset:4096\n"
"s_mov_b32 s44, 0x1891000\n"
"s_mul_i32 s51, s97, 0x70000\n"
"s_add_u32 s44, s44, s51\n"
"s_mul_i32 s51, s96, 0x1c000\n"
"s_add_u32 s44, s44, s51\n"
"s_mul_i32 s51, s34, 0xe00\n"
"s_add_u32 s44, s44, s51\n"
"s_waitcnt lgkmcnt(3)\n"
"v_mfma_f32_16x16x32_bf16 v[154:157], v[226:229], v[142:145], v[154:157]\n"
"v_mfma_f32_16x16x32_bf16 v[182:185], v[226:229], v[146:149], v[182:185]\n"
"s_waitcnt lgkmcnt(2)\n"
"ds_read_b128 v[218:221], v0 offset:5120\n"
"buffer_load_dwordx4 a[108:111], v0, s[20:23], s62 offen offset:3072 sc1\n"
"s_add_u32 s45, s44, 0xe000\n"
"v_mfma_f32_16x16x32_bf16 v[158:161], v[206:209], v[142:145], v[158:161]\n"
"v_mfma_f32_16x16x32_bf16 v[186:189], v[206:209], v[146:149], v[186:189]\n"
"s_waitcnt lgkmcnt(2)\n"
"ds_read_b128 v[222:225], v0 offset:6144\n"
"v_mfma_f32_16x16x32_bf16 v[162:165], v[210:213], v[142:145], v[162:165]\n"
"v_mfma_f32_16x16x32_bf16 v[190:193], v[210:213], v[146:149], v[190:193]\n"
"s_waitcnt lgkmcnt(2)\n"
"ds_read_b128 v[226:229], v0 offset:11264\n"
"v_mfma_f32_16x16x32_bf16 v[166:169], v[214:217], v[38:41], v[166:169]\n"
"v_mfma_f32_16x16x32_bf16 v[194:197], v[214:217], v[42:45], v[194:197]\n"
"s_waitcnt lgkmcnt(2)\n"
"ds_read_b128 v[206:209], v0 offset:12288\n"
"s_add_u32 s62, s54, 0x7000\n"
"buffer_load_dwordx4 a[112:115], v0, s[20:23], s62 offen sc1\n"
"v_mfma_f32_16x16x32_bf16 v[170:173], v[218:221], v[38:41], v[170:173]\n"
"v_mfma_f32_16x16x32_bf16 v[198:201], v[218:221], v[42:45], v[198:201]\n"
"s_waitcnt lgkmcnt(2)\n"
"ds_read_b128 v[210:213], v0 offset:13312\n"
"ds_read_b128 v[230:233], v4 offset:512\n"
"ds_read_b128 v[234:237], v4 offset:576\n"
"v_mfma_f32_16x16x32_bf16 v[174:177], v[222:225], v[38:41], v[174:177]\n"
"v_mfma_f32_16x16x32_bf16 v[202:205], v[222:225], v[42:45], v[202:205]\n"
"s_waitcnt lgkmcnt(4)\n"
"ds_read_b128 v[214:217], v0 offset:18432\n"
"v_mfma_f32_16x16x32_bf16 v[166:169], v[226:229], v[46:49], v[166:169]\n"
"v_mfma_f32_16x16x32_bf16 v[194:197], v[226:229], v[50:53], v[194:197]\n"
"s_waitcnt lgkmcnt(2)\n"
"ds_read_b128 v[218:221], v0 offset:19456\n"
"v_fmac_f32 v150, v230, v242\n"
"v_fmac_f32 v151, v231, v242\n"
"v_fmac_f32 v152, v232, v242\n"
"buffer_load_dwordx4 a[116:119], v0, s[20:23], s62 offen offset:1024 sc1\n"
"v_mfma_f32_16x16x32_bf16 v[170:173], v[206:209], v[46:49], v[170:173]\n"
"v_mfma_f32_16x16x32_bf16 v[198:201], v[206:209], v[50:53], v[198:201]\n"
"s_waitcnt lgkmcnt(2)\n"
"ds_read_b128 v[222:225], v0 offset:20480\n"
"v_fmac_f32 v153, v233, v242\n"
"v_fmac_f32 v154, v234, v242\n"
"v_fmac_f32 v155, v235, v242\n"
"v_mfma_f32_16x16x32_bf16 v[174:177], v[210:213], v[46:49], v[174:177]\n"
"v_mfma_f32_16x16x32_bf16 v[202:205], v[210:213], v[50:53], v[202:205]\n"
"s_waitcnt lgkmcnt(2)\n"
"ds_read_b128 v[226:229], v0 offset:25600\n"
"v_fmac_f32 v156, v236, v242\n"
"v_fmac_f32 v157, v237, v242\n"
"v_cvt_pk_bf16_f32 v246, v150, v151\n"
"v_mfma_f32_16x16x32_bf16 v[166:169], v[214:217], v[54:57], v[166:169]\n"
"v_mfma_f32_16x16x32_bf16 v[194:197], v[214:217], v[58:61], v[194:197]\n"
"s_waitcnt lgkmcnt(2)\n"
"ds_read_b128 v[206:209], v0 offset:26624\n"
"v_cvt_pk_bf16_f32 v247, v152, v153\n"
"v_cvt_pk_bf16_f32 v248, v154, v155\n"
"v_cvt_pk_bf16_f32 v249, v156, v157\n"
"buffer_load_dwordx4 a[120:123], v0, s[20:23], s62 offen offset:2048 sc1\n"
"v_mfma_f32_16x16x32_bf16 v[170:173], v[218:221], v[54:57], v[170:173]\n"
"v_mfma_f32_16x16x32_bf16 v[198:201], v[218:221], v[58:61], v[198:201]\n"
"s_waitcnt lgkmcnt(2)\n"
"ds_read_b128 v[210:213], v0 offset:27648\n"
"v_pk_max_i16 v246, v246, 0\n"
"v_pk_max_i16 v247, v247, 0\n"
"v_pk_max_i16 v248, v248, 0\n"
"v_mfma_f32_16x16x32_bf16 v[174:177], v[222:225], v[54:57], v[174:177]\n"
"v_mfma_f32_16x16x32_bf16 v[202:205], v[222:225], v[58:61], v[202:205]\n"
"s_waitcnt lgkmcnt(2)\n"
"ds_read_b128 v[214:217], v0 offset:32768\n"
"v_pk_max_i16 v249, v249, 0\n"
"buffer_store_dwordx4 v[246:249], v0, s[20:23], s44 offen sc1\n"
"v_mfma_f32_16x16x32_bf16 v[166:169], v[226:229], v[62:65], v[166:169]\n"
"v_mfma_f32_16x16x32_bf16 v[194:197], v[226:229], v[66:69], v[194:197]\n"
"s_waitcnt lgkmcnt(2)\n"
"ds_read_b128 v[218:221], v0 offset:33792\n"
"v_fmac_f32 v178, v230, v243\n"
"v_fmac_f32 v179, v231, v243\n"
"v_fmac_f32 v180, v232, v243\n"
"buffer_load_dwordx4 a[124:127], v0, s[20:23], s62 offen offset:3072 sc1\n"
"v_mfma_f32_16x16x32_bf16 v[170:173], v[206:209], v[62:65], v[170:173]\n"
"v_mfma_f32_16x16x32_bf16 v[198:201], v[206:209], v[66:69], v[198:201]\n"
"s_waitcnt lgkmcnt(2)\n"
"ds_read_b128 v[222:225], v0 offset:34816\n"
"v_fmac_f32 v181, v233, v243\n"
"v_fmac_f32 v182, v234, v243\n"
"v_fmac_f32 v183, v235, v243\n"
"v_mfma_f32_16x16x32_bf16 v[174:177], v[210:213], v[62:65], v[174:177]\n"
"v_mfma_f32_16x16x32_bf16 v[202:205], v[210:213], v[66:69], v[202:205]\n"
"s_waitcnt lgkmcnt(2)\n"
"ds_read_b128 v[226:229], v0 offset:39936\n"
"v_fmac_f32 v184, v236, v243\n"
"v_fmac_f32 v185, v237, v243\n"
"v_cvt_pk_bf16_f32 v250, v178, v179\n"
"v_mfma_f32_16x16x32_bf16 v[166:169], v[214:217], v[70:73], v[166:169]\n"
"v_mfma_f32_16x16x32_bf16 v[194:197], v[214:217], v[74:77], v[194:197]\n"
"s_waitcnt lgkmcnt(2)\n"
"ds_read_b128 v[206:209], v0 offset:40960\n"
"v_cvt_pk_bf16_f32 v251, v180, v181\n"
"v_cvt_pk_bf16_f32 v252, v182, v183\n"
"v_cvt_pk_bf16_f32 v253, v184, v185\n"
"s_add_u32 s62, s54, 0x8000\n"
"buffer_load_dwordx4 a[128:131], v0, s[20:23], s62 offen sc1\n"
"v_mfma_f32_16x16x32_bf16 v[170:173], v[218:221], v[70:73], v[170:173]\n"
"v_mfma_f32_16x16x32_bf16 v[198:201], v[218:221], v[74:77], v[198:201]\n"
"s_waitcnt lgkmcnt(2)\n"
"ds_read_b128 v[210:213], v0 offset:41984\n"
"v_pk_max_i16 v250, v250, 0\n"
"v_pk_max_i16 v251, v251, 0\n"
"v_pk_max_i16 v252, v252, 0\n"
"v_mfma_f32_16x16x32_bf16 v[174:177], v[222:225], v[70:73], v[174:177]\n"
"v_mfma_f32_16x16x32_bf16 v[202:205], v[222:225], v[74:77], v[202:205]\n"
"s_waitcnt lgkmcnt(2)\n"
"ds_read_b128 v[214:217], v0 offset:47104\n"
"v_pk_max_i16 v253, v253, 0\n"
"buffer_store_dwordx4 v[250:253], v0, s[20:23], s45 offen sc1\n"
"v_mfma_f32_16x16x32_bf16 v[166:169], v[226:229], v[78:81], v[166:169]\n"
"v_mfma_f32_16x16x32_bf16 v[194:197], v[226:229], v[82:85], v[194:197]\n"
"s_waitcnt lgkmcnt(2)\n"
"ds_read_b128 v[218:221], v0 offset:48128\n"
"ds_read_b128 v[230:233], v4 offset:640\n"
"ds_read_b128 v[234:237], v4 offset:704\n"
"buffer_load_dwordx4 a[132:135], v0, s[20:23], s62 offen offset:1024 sc1\n"
"v_mfma_f32_16x16x32_bf16 v[170:173], v[206:209], v[78:81], v[170:173]\n"
"v_mfma_f32_16x16x32_bf16 v[198:201], v[206:209], v[82:85], v[198:201]\n"
"s_waitcnt lgkmcnt(4)\n"
"ds_read_b128 v[222:225], v0 offset:49152\n"
"v_mfma_f32_16x16x32_bf16 v[174:177], v[210:213], v[78:81], v[174:177]\n"
"v_mfma_f32_16x16x32_bf16 v[202:205], v[210:213], v[82:85], v[202:205]\n"
"s_waitcnt lgkmcnt(2)\n"
"ds_read_b128 v[226:229], v0 offset:54272\n"
"v_fmac_f32 v158, v230, v242\n"
"v_fmac_f32 v159, v231, v242\n"
"v_fmac_f32 v160, v232, v242\n"
"v_mfma_f32_16x16x32_bf16 v[166:169], v[214:217], v[86:89], v[166:169]\n"
"v_mfma_f32_16x16x32_bf16 v[194:197], v[214:217], v[90:93], v[194:197]\n"
"s_waitcnt lgkmcnt(2)\n"
"ds_read_b128 v[206:209], v0 offset:55296\n"
"v_fmac_f32 v161, v233, v242\n"
"v_fmac_f32 v162, v234, v242\n"
"v_fmac_f32 v163, v235, v242\n"
"buffer_load_dwordx4 a[136:139], v0, s[20:23], s62 offen offset:2048 sc1\n"
"v_mfma_f32_16x16x32_bf16 v[170:173], v[218:221], v[86:89], v[170:173]\n"
"v_mfma_f32_16x16x32_bf16 v[198:201], v[218:221], v[90:93], v[198:201]\n"
"s_waitcnt lgkmcnt(2)\n"
"ds_read_b128 v[210:213], v0 offset:56320\n"
"v_fmac_f32 v164, v236, v242\n"
"v_fmac_f32 v165, v237, v242\n"
"v_cvt_pk_bf16_f32 v246, v158, v159\n"
"v_mfma_f32_16x16x32_bf16 v[174:177], v[222:225], v[86:89], v[174:177]\n"
"v_mfma_f32_16x16x32_bf16 v[202:205], v[222:225], v[90:93], v[202:205]\n"
"s_waitcnt lgkmcnt(2)\n"
"ds_read_b128 v[214:217], v0 offset:61440\n"
"v_cvt_pk_bf16_f32 v247, v160, v161\n"
"v_cvt_pk_bf16_f32 v248, v162, v163\n"
"v_cvt_pk_bf16_f32 v249, v164, v165\n"
"v_mfma_f32_16x16x32_bf16 v[166:169], v[226:229], v[94:97], v[166:169]\n"
"v_mfma_f32_16x16x32_bf16 v[194:197], v[226:229], v[98:101], v[194:197]\n"
"s_waitcnt lgkmcnt(2)\n"
"ds_read_b128 v[218:221], v0 offset:62464\n"
"v_pk_max_i16 v246, v246, 0\n"
"v_pk_max_i16 v247, v247, 0\n"
"v_pk_max_i16 v248, v248, 0\n"
"buffer_load_dwordx4 a[140:143], v0, s[20:23], s62 offen offset:3072 sc1\n"
"v_mfma_f32_16x16x32_bf16 v[170:173], v[206:209], v[94:97], v[170:173]\n"
"v_mfma_f32_16x16x32_bf16 v[198:201], v[206:209], v[98:101], v[198:201]\n"
"s_waitcnt lgkmcnt(2)\n"
"ds_read_b128 v[222:225], v0 offset:63488\n"
"v_pk_max_i16 v249, v249, 0\n"
"buffer_store_dwordx4 v[246:249], v0, s[20:23], s44 offen offset:1024 sc1\n"
"v_mfma_f32_16x16x32_bf16 v[174:177], v[210:213], v[94:97], v[174:177]\n"
"v_mfma_f32_16x16x32_bf16 v[202:205], v[210:213], v[98:101], v[202:205]\n"
"s_waitcnt lgkmcnt(2)\n"
"ds_read_b128 v[226:229], v1 offset:3072\n"
"v_fmac_f32 v186, v230, v243\n"
"v_fmac_f32 v187, v231, v243\n"
"v_fmac_f32 v188, v232, v243\n"
"v_mfma_f32_16x16x32_bf16 v[166:169], v[214:217], v[102:105], v[166:169]\n"
"v_mfma_f32_16x16x32_bf16 v[194:197], v[214:217], v[106:109], v[194:197]\n"
"s_waitcnt lgkmcnt(2)\n"
"ds_read_b128 v[206:209], v1 offset:4096\n"
"v_fmac_f32 v189, v233, v243\n"
"v_fmac_f32 v190, v234, v243\n"
"v_fmac_f32 v191, v235, v243\n"
"s_add_u32 s62, s54, 0x9000\n"
"buffer_load_dwordx4 a[144:147], v0, s[20:23], s62 offen sc1\n"
"v_mfma_f32_16x16x32_bf16 v[170:173], v[218:221], v[102:105], v[170:173]\n"
"v_mfma_f32_16x16x32_bf16 v[198:201], v[218:221], v[106:109], v[198:201]\n"
"s_waitcnt lgkmcnt(2)\n"
"ds_read_b128 v[210:213], v1 offset:5120\n"
"v_fmac_f32 v192, v236, v243\n"
"v_fmac_f32 v193, v237, v243\n"
"v_cvt_pk_bf16_f32 v250, v186, v187\n"
"v_mfma_f32_16x16x32_bf16 v[174:177], v[222:225], v[102:105], v[174:177]\n"
"v_mfma_f32_16x16x32_bf16 v[202:205], v[222:225], v[106:109], v[202:205]\n"
"s_waitcnt lgkmcnt(2)\n"
"ds_read_b128 v[214:217], v2 offset:2048\n"
"v_cvt_pk_bf16_f32 v251, v188, v189\n"
"v_cvt_pk_bf16_f32 v252, v190, v191\n"
"v_cvt_pk_bf16_f32 v253, v192, v193\n"
"v_mfma_f32_16x16x32_bf16 v[166:169], v[226:229], v[110:113], v[166:169]\n"
"v_mfma_f32_16x16x32_bf16 v[194:197], v[226:229], v[114:117], v[194:197]\n"
"s_waitcnt lgkmcnt(2)\n"
"ds_read_b128 v[218:221], v2 offset:3072\n"
"v_pk_max_i16 v250, v250, 0\n"
"v_pk_max_i16 v251, v251, 0\n"
"v_pk_max_i16 v252, v252, 0\n"
"buffer_load_dwordx4 a[148:151], v0, s[20:23], s62 offen offset:1024 sc1\n"
"v_mfma_f32_16x16x32_bf16 v[170:173], v[206:209], v[110:113], v[170:173]\n"
"v_mfma_f32_16x16x32_bf16 v[198:201], v[206:209], v[114:117], v[198:201]\n"
"s_waitcnt lgkmcnt(2)\n"
"ds_read_b128 v[222:225], v2 offset:4096\n"
"v_pk_max_i16 v253, v253, 0\n"
"buffer_store_dwordx4 v[250:253], v0, s[20:23], s45 offen offset:1024 sc1\n"
"v_mfma_f32_16x16x32_bf16 v[174:177], v[210:213], v[110:113], v[174:177]\n"
"v_mfma_f32_16x16x32_bf16 v[202:205], v[210:213], v[114:117], v[202:205]\n"
"s_waitcnt lgkmcnt(2)\n"
"ds_read_b128 v[226:229], v2 offset:9216\n"
"v_mfma_f32_16x16x32_bf16 v[166:169], v[214:217], v[118:121], v[166:169]\n"
"v_mfma_f32_16x16x32_bf16 v[194:197], v[214:217], v[122:125], v[194:197]\n"
"s_waitcnt vmcnt(24) lgkmcnt(2)\n"
"ds_read_b128 v[206:209], v2 offset:10240\n"
"v_add_f32 v238, v238, v240\n"
"buffer_load_dwordx4 a[152:155], v0, s[20:23], s62 offen offset:2048 sc1\n"
"v_mfma_f32_16x16x32_bf16 v[170:173], v[218:221], v[118:121], v[170:173]\n"
"v_mfma_f32_16x16x32_bf16 v[198:201], v[218:221], v[122:125], v[198:201]\n"
"s_waitcnt lgkmcnt(2)\n"
"ds_read_b128 v[210:213], v2 offset:11264\n"
"v_add_f32 v239, v239, v241\n"
"v_mfma_f32_16x16x32_bf16 v[174:177], v[222:225], v[118:121], v[174:177]\n"
"v_mfma_f32_16x16x32_bf16 v[202:205], v[222:225], v[122:125], v[202:205]\n"
"s_waitcnt lgkmcnt(2)\n"
"ds_read_b128 v[214:217], v2 offset:16384\n"
"v_permlane16_swap_b32 v238, v239\n"
"v_mfma_f32_16x16x32_bf16 v[166:169], v[226:229], v[126:129], v[166:169]\n"
"v_mfma_f32_16x16x32_bf16 v[194:197], v[226:229], v[130:133], v[194:197]\n"
"s_waitcnt lgkmcnt(2)\n"
"ds_read_b128 v[218:221], v2 offset:17408\n"
"v_add_f32 v238, v238, v239\n"
"buffer_load_dwordx4 a[156:159], v0, s[20:23], s62 offen offset:3072 sc1\n"
"v_mfma_f32_16x16x32_bf16 v[170:173], v[206:209], v[126:129], v[170:173]\n"
"v_mfma_f32_16x16x32_bf16 v[198:201], v[206:209], v[130:133], v[198:201]\n"
"s_waitcnt lgkmcnt(2)\n"
"ds_read_b128 v[222:225], v2 offset:18432\n"
"v_mov_b32 v239, v238\n"
"v_mfma_f32_16x16x32_bf16 v[174:177], v[210:213], v[126:129], v[174:177]\n"
"v_mfma_f32_16x16x32_bf16 v[202:205], v[210:213], v[130:133], v[202:205]\n"
"s_waitcnt lgkmcnt(2)\n"
"ds_read_b128 v[226:229], v2 offset:23552\n"
"v_permlane32_swap_b32 v238, v239\n"
"v_mfma_f32_16x16x32_bf16 v[166:169], v[214:217], v[134:137], v[166:169]\n"
"v_mfma_f32_16x16x32_bf16 v[194:197], v[214:217], v[138:141], v[194:197]\n"
"s_waitcnt lgkmcnt(2)\n"
"ds_read_b128 v[206:209], v2 offset:24576\n"
"v_add_f32 v238, v238, v239\n"
"s_add_u32 s62, s54, 0xa000\n"
"buffer_load_dwordx4 a[160:163], v0, s[20:23], s62 offen sc1\n"
"v_mfma_f32_16x16x32_bf16 v[170:173], v[218:221], v[134:137], v[170:173]\n"
"v_mfma_f32_16x16x32_bf16 v[198:201], v[218:221], v[138:141], v[198:201]\n"
"s_waitcnt lgkmcnt(2)\n"
"ds_read_b128 v[210:213], v2 offset:25600\n"
"v_mov_b32 v239, v238\n"
"v_mfma_f32_16x16x32_bf16 v[174:177], v[222:225], v[134:137], v[174:177]\n"
"v_mfma_f32_16x16x32_bf16 v[202:205], v[222:225], v[138:141], v[202:205]\n"
"s_waitcnt lgkmcnt(2)\n"
"v_permlane16_swap_b32 v238, v239\n"
"v_mfma_f32_16x16x32_bf16 v[166:169], v[226:229], v[142:145], v[166:169]\n"
"v_mfma_f32_16x16x32_bf16 v[194:197], v[226:229], v[146:149], v[194:197]\n"
"s_waitcnt lgkmcnt(1)\n"
"v_mul_f32 v238, 0x3b124925, v238\n"
"buffer_load_dwordx4 a[164:167], v0, s[20:23], s62 offen offset:1024 sc1\n"
"v_mfma_f32_16x16x32_bf16 v[170:173], v[206:209], v[142:145], v[170:173]\n"
"v_mfma_f32_16x16x32_bf16 v[198:201], v[206:209], v[146:149], v[198:201]\n"
"s_waitcnt lgkmcnt(0)\n"
"v_mul_f32 v238, v238, v238\n"
"v_mfma_f32_16x16x32_bf16 v[174:177], v[210:213], v[142:145], v[174:177]\n"
"v_mfma_f32_16x16x32_bf16 v[202:205], v[210:213], v[146:149], v[202:205]\n"
"v_fma_f32 v238, v239, v37, -v238\n"
"v_max_f32 v238, 0, v238\n"
"v_add_f32 v238, 0x3727c5ac, v238\n"
"v_rsq_f32 v244, v238\n"
"v_cvt_pk_bf16_f32 v246, v166, v167\n"
"v_cvt_pk_bf16_f32 v247, v168, v169\n"
"v_cvt_pk_bf16_f32 v248, v170, v171\n"
"v_cvt_pk_bf16_f32 v249, v172, v173\n"
"v_pk_max_i16 v246, v246, 0\n"
"v_pk_max_i16 v247, v247, 0\n"
"v_pk_max_i16 v248, v248, 0\n"
"v_pk_max_i16 v249, v249, 0\n"
"buffer_store_dwordx4 v[246:249], v0, s[20:23], s44 offen offset:2048 sc1\n"
"v_cvt_pk_bf16_f32 v250, v194, v195\n"
"v_cvt_pk_bf16_f32 v251, v196, v197\n"
"v_cvt_pk_bf16_f32 v252, v198, v199\n"
"v_cvt_pk_bf16_f32 v253, v200, v201\n"
"v_pk_max_i16 v250, v250, 0\n"
"v_pk_max_i16 v251, v251, 0\n"
"v_pk_max_i16 v252, v252, 0\n"
"v_pk_max_i16 v253, v253, 0\n"
"buffer_store_dwordx4 v[250:253], v0, s[20:23], s45 offen offset:2048 sc1\n"
"v_cvt_pk_bf16_f32 v206, v174, v175\n"
"v_cvt_pk_bf16_f32 v207, v176, v177\n"
"v_cvt_pk_bf16_f32 v208, v202, v203\n"
"v_cvt_pk_bf16_f32 v209, v204, v205\n"
"v_pk_max_i16 v206, v206, 0\n"
"v_pk_max_i16 v207, v207, 0\n"
"v_pk_max_i16 v208, v208, 0\n"
"v_pk_max_i16 v209, v209, 0\n"
"s_nop 0\n"
"v_permlane16_swap_b32 v206, v208\n"
"v_permlane16_swap_b32 v207, v209\n"
"buffer_store_dwordx4 v[206:209], v8, s[20:23], s44 offen offset:3072 sc1\n"
"s_barrier\n"
"buffer_load_dwordx4 a[168:171], v0, s[20:23], s62 offen offset:2048 sc1\n"
"buffer_load_dwordx4 a[172:175], v0, s[20:23], s62 offen offset:3072 sc1\n"
"s_add_u32 s62, s54, 0xb000\n"
"buffer_load_dwordx4 a[176:179], v0, s[20:23], s62 offen sc1\n"
"buffer_load_dwordx4 a[180:183], v0, s[20:23], s62 offen offset:1024 sc1\n"
"buffer_load_dwordx4 a[184:187], v0, s[20:23], s62 offen offset:2048 sc1\n"
"buffer_load_dwordx4 a[188:191], v0, s[20:23], s62 offen offset:3072 sc1\n"
"s_add_u32 s62, s54, 0xc000\n"
"buffer_load_dwordx4 a[192:195], v0, s[20:23], s62 offen sc1\n"
"s_mov_b32 s49, 0x1891000\n"
"s_mul_i32 s51, s97, 0x70000\n"
"s_add_u32 s49, s49, s51\n"
"s_mul_i32 s51, s36, 0x38000\n"
"s_add_u32 s49, s49, s51\n"
"s_mul_i32 s51, s96, 0x3800\n"
"s_add_u32 s49, s49, s51\n"
"s_add_u32 s48, s96, 0\n"
"s_and_b32 s48, s48, 3\n"
"s_mul_i32 s50, s48, 0xe000\n"
"s_add_u32 s44, s49, s50\n"
"s_mul_i32 s48, s48, 0x3000\n"
"v_add_u32 v184, s48, v0\n"
"s_add_u32 s48, s96, 1\n"
"s_and_b32 s48, s48, 3\n"
"s_mul_i32 s50, s48, 0xe000\n"
"s_add_u32 s45, s49, s50\n"
"s_mul_i32 s48, s48, 0x3000\n"
"v_add_u32 v185, s48, v0\n"
"s_add_u32 s48, s96, 2\n"
"s_and_b32 s48, s48, 3\n"
"s_mul_i32 s50, s48, 0xe000\n"
"s_add_u32 s46, s49, s50\n"
"s_mul_i32 s48, s48, 0x3000\n"
"s_add_u32 s48, s48, 0x1000\n"
"v_add_u32 v186, s48, v0\n"
"s_add_u32 s48, s96, 3\n"
"s_and_b32 s48, s48, 3\n"
"s_mul_i32 s50, s48, 0xe000\n"
"s_add_u32 s47, s49, s50\n"
"s_mul_i32 s48, s48, 0x3000\n"
"s_add_u32 s48, s48, 0x2000\n"
"v_add_u32 v187, s48, v0\n"
"v_mov_b32 v38, v244\n"
"s_waitcnt vmcnt(7)\n"
"s_waitcnt lgkmcnt(0)\n"
"s_barrier\n"
"s_cmp_lg_u32 s96, 0\n"
"s_cbranch_scc1 .Lkw0_45\n"
"s_mov_b64 exec, 0xffff\n"
"buffer_atomic_add v10, v11, s[20:23], s52 offen\n"
"s_mov_b64 exec, -1\n"
".Lkw0_45:\n"
"s_mul_i32 s51, s67, 14\n"
"v_add_u32 v124, s51, v1\n"
"ds_write_b128 v124, a[196:199] offset:8192\n"
"ds_write_b128 v124, a[200:203] offset:9216\n"
"ds_write_b128 v124, a[204:207] offset:10240\n"
"ds_write_b128 v124, a[208:211] offset:11264\n"
"ds_write_b128 v124, a[212:215] offset:12288\n"
"ds_write_b128 v124, a[216:219] offset:13312\n"
"ds_write_b128 v124, a[220:223] offset:14336\n"
"ds_write_b128 v124, a[224:227] offset:15360\n"
"ds_write_b128 v124, a[228:231] offset:16384\n"
"ds_write_b128 v124, a[232:235] offset:17408\n"
"ds_write_b128 v124, a[236:239] offset:18432\n"
"ds_write_b128 v124, a[240:243] offset:19456\n"
"ds_write_b128 v124, a[244:247] offset:20480\n"
"ds_write_b128 v124, a[248:251] offset:21504\n"
"; D layer 0\n"
"buffer_load_dword v120, off, s[20:23], s53 offset:0 sc1\n"
"s_mov_b32 s55, 0\n"
"s_waitcnt vmcnt(0)\n"
".Lkpoll_46:\n"
"v_readfirstlane_b32 s48, v120\n"
"s_add_u32 s55, s55, 1\n"
"s_cmp_ge_u32 s48, 48\n"
"s_cbranch_scc1 .Lkdone_47\n"
"buffer_load_dword v120, off, s[20:23], s53 offset:0 sc1\n"
"s_cmp_lt_u32 s55, 0x40000\n"
"s_waitcnt vmcnt(0)\n"
"s_cbranch_scc1 .Lkpoll_46\n"
"s_mov_b32 s48, 0x40000\n"
"buffer_store_dword v10, off, s[20:23], s48 sc1\n"
".Lkdone_47:\n"
"buffer_load_dwordx4 v[120:123], v0, s[20:23], s44 offen sc1\n"
"buffer_load_dwordx4 v[124:127], v0, s[20:23], s45 offen sc1\n"
"buffer_load_dwordx4 v[128:131], v0, s[20:23], s46 offen sc1\n"
"buffer_load_dwordx4 v[132:135], v0, s[20:23], s47 offen sc1\n"
"buffer_load_dwordx4 v[136:139], v0, s[20:23], s44 offen offset:1024 sc1\n"
"buffer_load_dwordx4 v[140:143], v0, s[20:23], s45 offen offset:1024 sc1\n"
"buffer_load_dwordx4 v[144:147], v0, s[20:23], s46 offen offset:1024 sc1\n"
"buffer_load_dwordx4 v[148:151], v0, s[20:23], s47 offen offset:1024 sc1\n"
"buffer_load_dwordx4 v[152:155], v0, s[20:23], s44 offen offset:2048 sc1\n"
"buffer_load_dwordx4 v[156:159], v0, s[20:23], s45 offen offset:2048 sc1\n"
"buffer_load_dwordx4 v[160:163], v0, s[20:23], s46 offen offset:2048 sc1\n"
"buffer_load_dwordx4 v[164:167], v0, s[20:23], s47 offen offset:2048 sc1\n"
"s_waitcnt vmcnt(11)\n"
"v_mfma_f32_16x16x32_bf16 v[40:43], a[0:3], v[120:123], 0\n"
"v_mfma_f32_16x16x32_bf16 v[44:47], a[4:7], v[120:123], 0\n"
"v_mfma_f32_16x16x32_bf16 v[48:51], a[8:11], v[120:123], 0\n"
"v_mfma_f32_16x16x32_bf16 v[52:55], a[168:171], v[120:123], 0\n"
"s_waitcnt vmcnt(10)\n"
"buffer_load_dwordx4 v[168:171], v0, s[20:23], s44 offen offset:3072 sc1\n"
"v_mfma_f32_16x16x32_bf16 v[60:63], a[0:3], v[124:127], 0\n"
"v_mfma_f32_16x16x32_bf16 v[64:67], a[4:7], v[124:127], 0\n"
"v_mfma_f32_16x16x32_bf16 v[68:71], a[8:11], v[124:127], 0\n"
"v_mfma_f32_16x16x32_bf16 v[72:75], a[168:171], v[124:127], 0\n"
"s_waitcnt vmcnt(10)\n"
"buffer_load_dwordx4 v[172:175], v0, s[20:23], s45 offen offset:3072 sc1\n"
"v_mfma_f32_16x16x32_bf16 v[80:83], a[0:3], v[128:131], 0\n"
"v_mfma_f32_16x16x32_bf16 v[84:87], a[4:7], v[128:131], 0\n"
"v_mfma_f32_16x16x32_bf16 v[88:91], a[8:11], v[128:131], 0\n"
"v_mfma_f32_16x16x32_bf16 v[92:95], a[168:171], v[128:131], 0\n"
"s_waitcnt vmcnt(10)\n"
"buffer_load_dwordx4 v[176:179], v0, s[20:23], s46 offen offset:3072 sc1\n"
"v_mfma_f32_16x16x32_bf16 v[100:103], a[0:3], v[132:135], 0\n"
"v_mfma_f32_16x16x32_bf16 v[104:107], a[4:7], v[132:135], 0\n"
"v_mfma_f32_16x16x32_bf16 v[108:111], a[8:11], v[132:135], 0\n"
"v_mfma_f32_16x16x32_bf16 v[112:115], a[168:171], v[132:135], 0\n"
"s_waitcnt vmcnt(10)\n"
"buffer_load_dwordx4 v[180:183], v0, s[20:23], s47 offen offset:3072 sc1\n"
"v_mfma_f32_16x16x32_bf16 v[40:43], a[12:15], v[136:139], v[40:43]\n"
"v_mfma_f32_16x16x32_bf16 v[44:47], a[16:19], v[136:139], v[44:47]\n"
"v_mfma_f32_16x16x32_bf16 v[48:51], a[20:23], v[136:139], v[48:51]\n"
"v_mfma_f32_16x16x32_bf16 v[56:59], a[168:171], v[136:139], 0\n"
"s_waitcnt vmcnt(10)\n"
"s_add_u32 s44, s44, 0x1000\n"
"buffer_load_dwordx4 v[120:123], v0, s[20:23], s44 offen sc1\n"
"v_mfma_f32_16x16x32_bf16 v[60:63], a[12:15], v[140:143], v[60:63]\n"
"v_mfma_f32_16x16x32_bf16 v[64:67], a[16:19], v[140:143], v[64:67]\n"
"v_mfma_f32_16x16x32_bf16 v[68:71], a[20:23], v[140:143], v[68:71]\n"
"v_mfma_f32_16x16x32_bf16 v[76:79], a[168:171], v[140:143], 0\n"
"s_waitcnt vmcnt(10)\n"
"s_add_u32 s45, s45, 0x1000\n"
"buffer_load_dwordx4 v[124:127], v0, s[20:23], s45 offen sc1\n"
"v_mfma_f32_16x16x32_bf16 v[80:83], a[12:15], v[144:147], v[80:83]\n"
"v_mfma_f32_16x16x32_bf16 v[84:87], a[16:19], v[144:147], v[84:87]\n"
"v_mfma_f32_16x16x32_bf16 v[88:91], a[20:23], v[144:147], v[88:91]\n"
"v_mfma_f32_16x16x32_bf16 v[96:99], a[168:171], v[144:147], 0\n"
"s_waitcnt vmcnt(10)\n"
"s_add_u32 s46, s46, 0x1000\n"
"buffer_load_dwordx4 v[128:131], v0, s[20:23], s46 offen sc1\n"
"v_mfma_f32_16x16x32_bf16 v[100:103], a[12:15], v[148:151], v[100:103]\n"
"v_mfma_f32_16x16x32_bf16 v[104:107], a[16:19], v[148:151], v[104:107]\n"
"v_mfma_f32_16x16x32_bf16 v[108:111], a[20:23], v[148:151], v[108:111]\n"
"v_mfma_f32_16x16x32_bf16 v[116:119], a[168:171], v[148:151], 0\n"
"s_waitcnt vmcnt(10)\n"
"s_add_u32 s47, s47, 0x1000\n"
"buffer_load_dwordx4 v[132:135], v0, s[20:23], s47 offen sc1\n"
"v_mfma_f32_16x16x32_bf16 v[40:43], a[24:27], v[152:155], v[40:43]\n"
"v_mfma_f32_16x16x32_bf16 v[44:47], a[28:31], v[152:155], v[44:47]\n"
"v_mfma_f32_16x16x32_bf16 v[48:51], a[32:35], v[152:155], v[48:51]\n"
"v_mfma_f32_16x16x32_bf16 v[52:55], a[172:175], v[152:155], v[52:55]\n"
"s_waitcnt vmcnt(10)\n"
"buffer_load_dwordx4 v[136:139], v0, s[20:23], s44 offen offset:1024 sc1\n"
"v_mfma_f32_16x16x32_bf16 v[60:63], a[24:27], v[156:159], v[60:63]\n"
"v_mfma_f32_16x16x32_bf16 v[64:67], a[28:31], v[156:159], v[64:67]\n"
"v_mfma_f32_16x16x32_bf16 v[68:71], a[32:35], v[156:159], v[68:71]\n"
"v_mfma_f32_16x16x32_bf16 v[72:75], a[172:175], v[156:159], v[72:75]\n"
"s_waitcnt vmcnt(10)\n"
"buffer_load_dwordx4 v[140:143], v0, s[20:23], s45 offen offset:1024 sc1\n"
"v_mfma_f32_16x16x32_bf16 v[80:83], a[24:27], v[160:163], v[80:83]\n"
"v_mfma_f32_16x16x32_bf16 v[84:87], a[28:31], v[160:163], v[84:87]\n"
"v_mfma_f32_16x16x32_bf16 v[88:91], a[32:35], v[160:163], v[88:91]\n"
"v_mfma_f32_16x16x32_bf16 v[92:95], a[172:175], v[160:163], v[92:95]\n"
"s_waitcnt vmcnt(10)\n"
"buffer_load_dwordx4 v[144:147], v0, s[20:23], s46 offen offset:1024 sc1\n"
"v_mfma_f32_16x16x32_bf16 v[100:103], a[24:27], v[164:167], v[100:103]\n"
"v_mfma_f32_16x16x32_bf16 v[104:107], a[28:31], v[164:167], v[104:107]\n"
"v_mfma_f32_16x16x32_bf16 v[108:111], a[32:35], v[164:167], v[108:111]\n"
"v_mfma_f32_16x16x32_bf16 v[112:115], a[172:175], v[164:167], v[112:115]\n"
"s_waitcnt vmcnt(10)\n"
"buffer_load_dwordx4 v[148:151], v0, s[20:23], s47 offen offset:1024 sc1\n"
"v_mfma_f32_16x16x32_bf16 v[40:43], a[36:39], v[168:171], v[40:43]\n"
"v_mfma_f32_16x16x32_bf16 v[44:47], a[40:43], v[168:171], v[44:47]\n"
"v_mfma_f32_16x16x32_bf16 v[48:51], a[44:47], v[168:171], v[48:51]\n"
"v_mfma_f32_16x16x32_bf16 v[56:59], a[172:175], v[168:171], v[56:59]\n"
"s_waitcnt vmcnt(10)\n"
"buffer_load_dwordx4 v[152:155], v0, s[20:23], s44 offen offset:2048 sc1\n"
"v_mfma_f32_16x16x32_bf16 v[60:63], a[36:39], v[172:175], v[60:63]\n"
"v_mfma_f32_16x16x32_bf16 v[64:67], a[40:43], v[172:175], v[64:67]\n"
"v_mfma_f32_16x16x32_bf16 v[68:71], a[44:47], v[172:175], v[68:71]\n"
"v_mfma_f32_16x16x32_bf16 v[76:79], a[172:175], v[172:175], v[76:79]\n"
"s_waitcnt vmcnt(10)\n"
"buffer_load_dwordx4 v[156:159], v0, s[20:23], s45 offen offset:2048 sc1\n"
"v_mfma_f32_16x16x32_bf16 v[80:83], a[36:39], v[176:179], v[80:83]\n"
"v_mfma_f32_16x16x32_bf16 v[84:87], a[40:43], v[176:179], v[84:87]\n"
"v_mfma_f32_16x16x32_bf16 v[88:91], a[44:47], v[176:179], v[88:91]\n"
"v_mfma_f32_16x16x32_bf16 v[96:99], a[172:175], v[176:179], v[96:99]\n"
"s_waitcnt vmcnt(10)\n"
"buffer_load_dwordx4 v[160:163], v0, s[20:23], s46 offen offset:2048 sc1\n"
"v_mfma_f32_16x16x32_bf16 v[100:103], a[36:39], v[180:183], v[100:103]\n"
"v_mfma_f32_16x16x32_bf16 v[104:107], a[40:43], v[180:183], v[104:107]\n"
"v_mfma_f32_16x16x32_bf16 v[108:111], a[44:47], v[180:183], v[108:111]\n"
"v_mfma_f32_16x16x32_bf16 v[116:119], a[172:175], v[180:183], v[116:119]\n"
"s_waitcnt vmcnt(10)\n"
"buffer_load_dwordx4 v[164:167], v0, s[20:23], s47 offen offset:2048 sc1\n"
"v_mfma_f32_16x16x32_bf16 v[40:43], a[48:51], v[120:123], v[40:43]\n"
"v_mfma_f32_16x16x32_bf16 v[44:47], a[52:55], v[120:123], v[44:47]\n"
"v_mfma_f32_16x16x32_bf16 v[48:51], a[56:59], v[120:123], v[48:51]\n"
"v_mfma_f32_16x16x32_bf16 v[52:55], a[176:179], v[120:123], v[52:55]\n"
"s_waitcnt vmcnt(10)\n"
"buffer_load_dwordx4 v[168:171], v0, s[20:23], s44 offen offset:3072 sc1\n"
"v_mfma_f32_16x16x32_bf16 v[60:63], a[48:51], v[124:127], v[60:63]\n"
"v_mfma_f32_16x16x32_bf16 v[64:67], a[52:55], v[124:127], v[64:67]\n"
"v_mfma_f32_16x16x32_bf16 v[68:71], a[56:59], v[124:127], v[68:71]\n"
"v_mfma_f32_16x16x32_bf16 v[72:75], a[176:179], v[124:127], v[72:75]\n"
"s_waitcnt vmcnt(10)\n"
"buffer_load_dwordx4 v[172:175], v0, s[20:23], s45 offen offset:3072 sc1\n"
"v_mfma_f32_16x16x32_bf16 v[80:83], a[48:51], v[128:131], v[80:83]\n"
"v_mfma_f32_16x16x32_bf16 v[84:87], a[52:55], v[128:131], v[84:87]\n"
"v_mfma_f32_16x16x32_bf16 v[88:91], a[56:59], v[128:131], v[88:91]\n"
"v_mfma_f32_16x16x32_bf16 v[92:95], a[176:179], v[128:131], v[92:95]\n"
"s_waitcnt vmcnt(10)\n"
"buffer_load_dwordx4 v[176:179], v0, s[20:23], s46 offen offset:3072 sc1\n"
"v_mfma_f32_16x16x32_bf16 v[100:103], a[48:51], v[132:135], v[100:103]\n"
"v_mfma_f32_16x16x32_bf16 v[104:107], a[52:55], v[132:135], v[104:107]\n"
"v_mfma_f32_16x16x32_bf16 v[108:111], a[56:59], v[132:135], v[108:111]\n"
"v_mfma_f32_16x16x32_bf16 v[112:115], a[176:179], v[132:135], v[112:115]\n"
"s_waitcnt vmcnt(10)\n"
"buffer_load_dwordx4 v[180:183], v0, s[20:23], s47 offen offset:3072 sc1\n"
"v_mfma_f32_16x16x32_bf16 v[40:43], a[60:63], v[136:139], v[40:43]\n"
"v_mfma_f32_16x16x32_bf16 v[44:47], a[64:67], v[136:139], v[44:47]\n"
"v_mfma_f32_16x16x32_bf16 v[48:51], a[68:71], v[136:139], v[48:51]\n"
"v_mfma_f32_16x16x32_bf16 v[56:59], a[176:179], v[136:139], v[56:59]\n"
"s_waitcnt vmcnt(10)\n"
"s_add_u32 s44, s44, 0x1000\n"
"buffer_load_dwordx4 v[120:123], v0, s[20:23], s44 offen sc1\n"
"v_mfma_f32_16x16x32_bf16 v[60:63], a[60:63], v[140:143], v[60:63]\n"
"v_mfma_f32_16x16x32_bf16 v[64:67], a[64:67], v[140:143], v[64:67]\n"
"v_mfma_f32_16x16x32_bf16 v[68:71], a[68:71], v[140:143], v[68:71]\n"
"v_mfma_f32_16x16x32_bf16 v[76:79], a[176:179], v[140:143], v[76:79]\n"
"s_waitcnt vmcnt(10)\n"
"s_add_u32 s45, s45, 0x1000\n"
"buffer_load_dwordx4 v[124:127], v0, s[20:23], s45 offen sc1\n"
"v_mfma_f32_16x16x32_bf16 v[80:83], a[60:63], v[144:147], v[80:83]\n"
"v_mfma_f32_16x16x32_bf16 v[84:87], a[64:67], v[144:147], v[84:87]\n"
"v_mfma_f32_16x16x32_bf16 v[88:91], a[68:71], v[144:147], v[88:91]\n"
"v_mfma_f32_16x16x32_bf16 v[96:99], a[176:179], v[144:147], v[96:99]\n"
"s_waitcnt vmcnt(10)\n"
"s_add_u32 s46, s46, 0x1000\n"
"buffer_load_dwordx4 v[128:131], v0, s[20:23], s46 offen sc1\n"
"v_mfma_f32_16x16x32_bf16 v[100:103], a[60:63], v[148:151], v[100:103]\n"
"v_mfma_f32_16x16x32_bf16 v[104:107], a[64:67], v[148:151], v[104:107]\n"
"v_mfma_f32_16x16x32_bf16 v[108:111], a[68:71], v[148:151], v[108:111]\n"
"v_mfma_f32_16x16x32_bf16 v[116:119], a[176:179], v[148:151], v[116:119]\n"
"s_waitcnt vmcnt(10)\n"
"s_add_u32 s47, s47, 0x1000\n"
"buffer_load_dwordx4 v[132:135], v0, s[20:23], s47 offen sc1\n"
"v_mfma_f32_16x16x32_bf16 v[40:43], a[72:75], v[152:155], v[40:43]\n"
"v_mfma_f32_16x16x32_bf16 v[44:47], a[76:79], v[152:155], v[44:47]\n"
"v_mfma_f32_16x16x32_bf16 v[48:51], a[80:83], v[152:155], v[48:51]\n"
"v_mfma_f32_16x16x32_bf16 v[52:55], a[180:183], v[152:155], v[52:55]\n"
"s_waitcnt vmcnt(10)\n"
"buffer_load_dwordx4 v[136:139], v0, s[20:23], s44 offen offset:1024 sc1\n"
"v_mfma_f32_16x16x32_bf16 v[60:63], a[72:75], v[156:159], v[60:63]\n"
"v_mfma_f32_16x16x32_bf16 v[64:67], a[76:79], v[156:159], v[64:67]\n"
"v_mfma_f32_16x16x32_bf16 v[68:71], a[80:83], v[156:159], v[68:71]\n"
"v_mfma_f32_16x16x32_bf16 v[72:75], a[180:183], v[156:159], v[72:75]\n"
"s_waitcnt vmcnt(10)\n"
"buffer_load_dwordx4 v[140:143], v0, s[20:23], s45 offen offset:1024 sc1\n"
"v_mfma_f32_16x16x32_bf16 v[80:83], a[72:75], v[160:163], v[80:83]\n"
"v_mfma_f32_16x16x32_bf16 v[84:87], a[76:79], v[160:163], v[84:87]\n"
"v_mfma_f32_16x16x32_bf16 v[88:91], a[80:83], v[160:163], v[88:91]\n"
"v_mfma_f32_16x16x32_bf16 v[92:95], a[180:183], v[160:163], v[92:95]\n"
"s_waitcnt vmcnt(10)\n"
"buffer_load_dwordx4 v[144:147], v0, s[20:23], s46 offen offset:1024 sc1\n"
"v_mfma_f32_16x16x32_bf16 v[100:103], a[72:75], v[164:167], v[100:103]\n"
"v_mfma_f32_16x16x32_bf16 v[104:107], a[76:79], v[164:167], v[104:107]\n"
"v_mfma_f32_16x16x32_bf16 v[108:111], a[80:83], v[164:167], v[108:111]\n"
"v_mfma_f32_16x16x32_bf16 v[112:115], a[180:183], v[164:167], v[112:115]\n"
"s_waitcnt vmcnt(10)\n"
"buffer_load_dwordx4 v[148:151], v0, s[20:23], s47 offen offset:1024 sc1\n"
"v_mfma_f32_16x16x32_bf16 v[40:43], a[84:87], v[168:171], v[40:43]\n"
"v_mfma_f32_16x16x32_bf16 v[44:47], a[88:91], v[168:171], v[44:47]\n"
"v_mfma_f32_16x16x32_bf16 v[48:51], a[92:95], v[168:171], v[48:51]\n"
"v_mfma_f32_16x16x32_bf16 v[56:59], a[180:183], v[168:171], v[56:59]\n"
"s_waitcnt vmcnt(10)\n"
"buffer_load_dwordx4 v[152:155], v0, s[20:23], s45 offen offset:2048 sc1\n"
"v_mfma_f32_16x16x32_bf16 v[60:63], a[84:87], v[172:175], v[60:63]\n"
"v_mfma_f32_16x16x32_bf16 v[64:67], a[88:91], v[172:175], v[64:67]\n"
"v_mfma_f32_16x16x32_bf16 v[68:71], a[92:95], v[172:175], v[68:71]\n"
"v_mfma_f32_16x16x32_bf16 v[76:79], a[180:183], v[172:175], v[76:79]\n"
"s_waitcnt vmcnt(10)\n"
"buffer_load_dwordx4 v[156:159], v0, s[20:23], s45 offen offset:3072 sc1\n"
"v_mfma_f32_16x16x32_bf16 v[80:83], a[84:87], v[176:179], v[80:83]\n"
"v_mfma_f32_16x16x32_bf16 v[84:87], a[88:91], v[176:179], v[84:87]\n"
"v_mfma_f32_16x16x32_bf16 v[88:91], a[92:95], v[176:179], v[88:91]\n"
"v_mfma_f32_16x16x32_bf16 v[96:99], a[180:183], v[176:179], v[96:99]\n"
"s_waitcnt vmcnt(10)\n"
"s_add_u32 s45, s45, 0x1000\n"
"buffer_load_dwordx4 v[160:163], v0, s[20:23], s45 offen sc1\n"
"v_mfma_f32_16x16x32_bf16 v[100:103], a[84:87], v[180:183], v[100:103]\n"
"v_mfma_f32_16x16x32_bf16 v[104:107], a[88:91], v[180:183], v[104:107]\n"
"v_mfma_f32_16x16x32_bf16 v[108:111], a[92:95], v[180:183], v[108:111]\n"
"v_mfma_f32_16x16x32_bf16 v[116:119], a[180:183], v[180:183], v[116:119]\n"
"s_waitcnt vmcnt(10)\n"
"buffer_load_dwordx4 v[164:167], v0, s[20:23], s45 offen offset:1024 sc1\n"
"v_mfma_f32_16x16x32_bf16 v[40:43], a[96:99], v[120:123], v[40:43]\n"
"v_mfma_f32_16x16x32_bf16 v[44:47], a[100:103], v[120:123], v[44:47]\n"
"v_mfma_f32_16x16x32_bf16 v[48:51], a[104:107], v[120:123], v[48:51]\n"
"v_mfma_f32_16x16x32_bf16 v[52:55], a[184:187], v[120:123], v[52:55]\n"
"s_waitcnt vmcnt(10)\n"
"buffer_load_dwordx4 v[168:171], v0, s[20:23], s46 offen offset:2048 sc1\n"
"v_mfma_f32_16x16x32_bf16 v[60:63], a[96:99], v[124:127], v[60:63]\n"
"v_mfma_f32_16x16x32_bf16 v[64:67], a[100:103], v[124:127], v[64:67]\n"
"v_mfma_f32_16x16x32_bf16 v[68:71], a[104:107], v[124:127], v[68:71]\n"
"v_mfma_f32_16x16x32_bf16 v[72:75], a[184:187], v[124:127], v[72:75]\n"
"s_waitcnt vmcnt(10)\n"
"buffer_load_dwordx4 v[172:175], v0, s[20:23], s46 offen offset:3072 sc1\n"
"v_mfma_f32_16x16x32_bf16 v[80:83], a[96:99], v[128:131], v[80:83]\n"
"v_mfma_f32_16x16x32_bf16 v[84:87], a[100:103], v[128:131], v[84:87]\n"
"v_mfma_f32_16x16x32_bf16 v[88:91], a[104:107], v[128:131], v[88:91]\n"
"v_mfma_f32_16x16x32_bf16 v[92:95], a[184:187], v[128:131], v[92:95]\n"
"s_waitcnt vmcnt(10)\n"
"s_add_u32 s46, s46, 0x1000\n"
"buffer_load_dwordx4 v[176:179], v0, s[20:23], s46 offen sc1\n"
"v_mfma_f32_16x16x32_bf16 v[100:103], a[96:99], v[132:135], v[100:103]\n"
"v_mfma_f32_16x16x32_bf16 v[104:107], a[100:103], v[132:135], v[104:107]\n"
"v_mfma_f32_16x16x32_bf16 v[108:111], a[104:107], v[132:135], v[108:111]\n"
"v_mfma_f32_16x16x32_bf16 v[112:115], a[184:187], v[132:135], v[112:115]\n"
"s_waitcnt vmcnt(10)\n"
"buffer_load_dwordx4 v[180:183], v0, s[20:23], s46 offen offset:1024 sc1\n"
"v_mfma_f32_16x16x32_bf16 v[40:43], a[108:111], v[136:139], v[40:43]\n"
"v_mfma_f32_16x16x32_bf16 v[44:47], a[112:115], v[136:139], v[44:47]\n"
"v_mfma_f32_16x16x32_bf16 v[48:51], a[116:119], v[136:139], v[48:51]\n"
"v_mfma_f32_16x16x32_bf16 v[56:59], a[184:187], v[136:139], v[56:59]\n"
"s_waitcnt vmcnt(10)\n"
"buffer_load_dwordx4 v[120:123], v0, s[20:23], s47 offen offset:2048 sc1\n"
"v_mfma_f32_16x16x32_bf16 v[60:63], a[108:111], v[140:143], v[60:63]\n"
"v_mfma_f32_16x16x32_bf16 v[64:67], a[112:115], v[140:143], v[64:67]\n"
"v_mfma_f32_16x16x32_bf16 v[68:71], a[116:119], v[140:143], v[68:71]\n"
"v_mfma_f32_16x16x32_bf16 v[76:79], a[184:187], v[140:143], v[76:79]\n"
"s_waitcnt vmcnt(10)\n"
"buffer_load_dwordx4 v[124:127], v0, s[20:23], s47 offen offset:3072 sc1\n"
"v_mfma_f32_16x16x32_bf16 v[80:83], a[108:111], v[144:147], v[80:83]\n"
"v_mfma_f32_16x16x32_bf16 v[84:87], a[112:115], v[144:147], v[84:87]\n"
"v_mfma_f32_16x16x32_bf16 v[88:91], a[116:119], v[144:147], v[88:91]\n"
"v_mfma_f32_16x16x32_bf16 v[96:99], a[184:187], v[144:147], v[96:99]\n"
"s_waitcnt vmcnt(10)\n"
"s_add_u32 s47, s47, 0x1000\n"
"buffer_load_dwordx4 v[128:131], v0, s[20:23], s47 offen sc1\n"
"v_mfma_f32_16x16x32_bf16 v[100:103], a[108:111], v[148:151], v[100:103]\n"
"v_mfma_f32_16x16x32_bf16 v[104:107], a[112:115], v[148:151], v[104:107]\n"
"v_mfma_f32_16x16x32_bf16 v[108:111], a[116:119], v[148:151], v[108:111]\n"
"v_mfma_f32_16x16x32_bf16 v[116:119], a[184:187], v[148:151], v[116:119]\n"
"s_waitcnt vmcnt(10)\n"
"buffer_load_dwordx4 v[132:135], v0, s[20:23], s47 offen offset:1024 sc1\n"
"v_mfma_f32_16x16x32_bf16 v[60:63], a[120:123], v[152:155], v[60:63]\n"
"v_mfma_f32_16x16x32_bf16 v[64:67], a[124:127], v[152:155], v[64:67]\n"
"v_mfma_f32_16x16x32_bf16 v[68:71], a[128:131], v[152:155], v[68:71]\n"
"v_mfma_f32_16x16x32_bf16 v[72:75], a[188:191], v[152:155], v[72:75]\n"
"s_waitcnt vmcnt(10)\n"
"buffer_load_dwordx4 v[136:139], v0, s[20:23], s44 offen offset:2048 sc1\n"
"v_mfma_f32_16x16x32_bf16 v[60:63], a[132:135], v[156:159], v[60:63]\n"
"v_mfma_f32_16x16x32_bf16 v[64:67], a[136:139], v[156:159], v[64:67]\n"
"v_mfma_f32_16x16x32_bf16 v[68:71], a[140:143], v[156:159], v[68:71]\n"
"v_mfma_f32_16x16x32_bf16 v[76:79], a[188:191], v[156:159], v[76:79]\n"
"s_waitcnt vmcnt(10)\n"
"buffer_load_dwordx4 v[140:143], v0, s[20:23], s44 offen offset:3072 sc1\n"
"v_mfma_f32_16x16x32_bf16 v[60:63], a[144:147], v[160:163], v[60:63]\n"
"v_mfma_f32_16x16x32_bf16 v[64:67], a[148:151], v[160:163], v[64:67]\n"
"v_mfma_f32_16x16x32_bf16 v[68:71], a[152:155], v[160:163], v[68:71]\n"
"v_mfma_f32_16x16x32_bf16 v[72:75], a[192:195], v[160:163], v[72:75]\n"
"s_waitcnt vmcnt(10)\n"
"s_add_u32 s44, s44, 0x1000\n"
"buffer_load_dwordx4 v[144:147], v0, s[20:23], s44 offen sc1\n"
"v_mfma_f32_16x16x32_bf16 v[60:63], a[156:159], v[164:167], v[60:63]\n"
"v_mfma_f32_16x16x32_bf16 v[64:67], a[160:163], v[164:167], v[64:67]\n"
"v_mfma_f32_16x16x32_bf16 v[68:71], a[164:167], v[164:167], v[68:71]\n"
"v_mfma_f32_16x16x32_bf16 v[76:79], a[192:195], v[164:167], v[76:79]\n"
"s_waitcnt vmcnt(10)\n"
"buffer_load_dwordx4 v[148:151], v0, s[20:23], s44 offen offset:1024 sc1\n"
"v_mfma_f32_16x16x32_bf16 v[80:83], a[120:123], v[168:171], v[80:83]\n"
"v_mfma_f32_16x16x32_bf16 v[84:87], a[124:127], v[168:171], v[84:87]\n"
"v_mfma_f32_16x16x32_bf16 v[88:91], a[128:131], v[168:171], v[88:91]\n"
"v_mfma_f32_16x16x32_bf16 v[92:95], a[188:191], v[168:171], v[92:95]\n"
"s_waitcnt vmcnt(10)\n"
"ds_write_b128 v185, v[60:63]\n"
"v_cndmask_b32_e64 v72, v76, v72, s[38:39]\n"
"v_cndmask_b32_e64 v73, v77, v73, s[38:39]\n"
"v_cndmask_b32_e64 v74, v78, v74, s[38:39]\n"
"v_cndmask_b32_e64 v75, v79, v75, s[38:39]\n"
"v_mfma_f32_16x16x32_bf16 v[80:83], a[132:135], v[172:175], v[80:83]\n"
"v_mfma_f32_16x16x32_bf16 v[84:87], a[136:139], v[172:175], v[84:87]\n"
"v_mfma_f32_16x16x32_bf16 v[88:91], a[140:143], v[172:175], v[88:91]\n"
"v_mfma_f32_16x16x32_bf16 v[96:99], a[188:191], v[172:175], v[96:99]\n"
"s_waitcnt vmcnt(9)\n"
"ds_write_b128 v185, v[64:67] offset:1024\n"
"v_mfma_f32_16x16x32_bf16 v[80:83], a[144:147], v[176:179], v[80:83]\n"
"v_mfma_f32_16x16x32_bf16 v[84:87], a[148:151], v[176:179], v[84:87]\n"
"v_mfma_f32_16x16x32_bf16 v[88:91], a[152:155], v[176:179], v[88:91]\n"
"v_mfma_f32_16x16x32_bf16 v[92:95], a[192:195], v[176:179], v[92:95]\n"
"s_waitcnt vmcnt(8)\n"
"ds_write_b128 v185, v[68:71] offset:2048\n"
"v_mfma_f32_16x16x32_bf16 v[80:83], a[156:159], v[180:183], v[80:83]\n"
"v_mfma_f32_16x16x32_bf16 v[84:87], a[160:163], v[180:183], v[84:87]\n"
"v_mfma_f32_16x16x32_bf16 v[88:91], a[164:167], v[180:183], v[88:91]\n"
"v_mfma_f32_16x16x32_bf16 v[96:99], a[192:195], v[180:183], v[96:99]\n"
"s_waitcnt vmcnt(7)\n"
"ds_write_b128 v185, v[72:75] offset:3072\n"
"v_mfma_f32_16x16x32_bf16 v[100:103], a[120:123], v[120:123], v[100:103]\n"
"v_mfma_f32_16x16x32_bf16 v[104:107], a[124:127], v[120:123], v[104:107]\n"
"v_mfma_f32_16x16x32_bf16 v[108:111], a[128:131], v[120:123], v[108:111]\n"
"v_mfma_f32_16x16x32_bf16 v[112:115], a[188:191], v[120:123], v[112:115]\n"
"s_waitcnt vmcnt(6)\n"
"ds_write_b128 v186, v[80:83]\n"
"v_cndmask_b32_e64 v92, v96, v92, s[38:39]\n"
"v_cndmask_b32_e64 v93, v97, v93, s[38:39]\n"
"v_cndmask_b32_e64 v94, v98, v94, s[38:39]\n"
"v_cndmask_b32_e64 v95, v99, v95, s[38:39]\n"
"v_mfma_f32_16x16x32_bf16 v[100:103], a[132:135], v[124:127], v[100:103]\n"
"v_mfma_f32_16x16x32_bf16 v[104:107], a[136:139], v[124:127], v[104:107]\n"
"v_mfma_f32_16x16x32_bf16 v[108:111], a[140:143], v[124:127], v[108:111]\n"
"v_mfma_f32_16x16x32_bf16 v[116:119], a[188:191], v[124:127], v[116:119]\n"
"s_waitcnt vmcnt(5)\n"
"ds_write_b128 v186, v[84:87] offset:1024\n"
"v_mfma_f32_16x16x32_bf16 v[100:103], a[144:147], v[128:131], v[100:103]\n"
"v_mfma_f32_16x16x32_bf16 v[104:107], a[148:151], v[128:131], v[104:107]\n"
"v_mfma_f32_16x16x32_bf16 v[108:111], a[152:155], v[128:131], v[108:111]\n"
"v_mfma_f32_16x16x32_bf16 v[112:115], a[192:195], v[128:131], v[112:115]\n"
"s_waitcnt vmcnt(4)\n"
"ds_write_b128 v186, v[88:91] offset:2048\n"
"v_mfma_f32_16x16x32_bf16 v[100:103], a[156:159], v[132:135], v[100:103]\n"
"v_mfma_f32_16x16x32_bf16 v[104:107], a[160:163], v[132:135], v[104:107]\n"
"v_mfma_f32_16x16x32_bf16 v[108:111], a[164:167], v[132:135], v[108:111]\n"
"v_mfma_f32_16x16x32_bf16 v[116:119], a[192:195], v[132:135], v[116:119]\n"
"s_waitcnt vmcnt(3)\n"
"ds_write_b128 v186, v[92:95] offset:3072\n"
"v_mfma_f32_16x16x32_bf16 v[40:43], a[120:123], v[136:139], v[40:43]\n"
"v_mfma_f32_16x16x32_bf16 v[44:47], a[124:127], v[136:139], v[44:47]\n"
"v_mfma_f32_16x16x32_bf16 v[48:51], a[128:131], v[136:139], v[48:51]\n"
"v_mfma_f32_16x16x32_bf16 v[52:55], a[188:191], v[136:139], v[52:55]\n"
"s_waitcnt vmcnt(2)\n"
"ds_write_b128 v187, v[100:103]\n"
"v_cndmask_b32_e64 v112, v116, v112, s[38:39]\n"
"v_cndmask_b32_e64 v113, v117, v113, s[38:39]\n"
"v_cndmask_b32_e64 v114, v118, v114, s[38:39]\n"
"v_cndmask_b32_e64 v115, v119, v115, s[38:39]\n"
"v_mfma_f32_16x16x32_bf16 v[40:43], a[132:135], v[140:143], v[40:43]\n"
"v_mfma_f32_16x16x32_bf16 v[44:47], a[136:139], v[140:143], v[44:47]\n"
"v_mfma_f32_16x16x32_bf16 v[48:51], a[140:143], v[140:143], v[48:51]\n"
"v_mfma_f32_16x16x32_bf16 v[56:59], a[188:191], v[140:143], v[56:59]\n"
"s_waitcnt vmcnt(1)\n"
"ds_write_b128 v187, v[104:107] offset:1024\n"
"v_mfma_f32_16x16x32_bf16 v[40:43], a[144:147], v[144:147], v[40:43]\n"
"v_mfma_f32_16x16x32_bf16 v[44:47], a[148:151], v[144:147], v[44:47]\n"
"v_mfma_f32_16x16x32_bf16 v[48:51], a[152:155], v[144:147], v[48:51]\n"
"v_mfma_f32_16x16x32_bf16 v[52:55], a[192:195], v[144:147], v[52:55]\n"
"s_waitcnt vmcnt(0)\n"
"ds_write_b128 v187, v[108:111] offset:2048\n"
"s_mov_b32 s44, 0x8d1000\n"
"s_mul_i32 s51, s37, 0x3c00\n"
"s_add_u32 s44, s44, s51\n"
"s_add_u32 s46, s44, s69\n"
"s_mul_i32 s48, s35, 0x700\n"
"s_add_u32 s44, s44, s48\n"
"v_mfma_f32_16x16x32_bf16 v[40:43], a[156:159], v[148:151], v[40:43]\n"
"v_mfma_f32_16x16x32_bf16 v[44:47], a[160:163], v[148:151], v[44:47]\n"
"v_mfma_f32_16x16x32_bf16 v[48:51], a[164:167], v[148:151], v[48:51]\n"
"v_mfma_f32_16x16x32_bf16 v[56:59], a[192:195], v[148:151], v[56:59]\n"
"ds_write_b128 v187, v[112:115] offset:3072\n"
"s_waitcnt lgkmcnt(0)\n"
"s_barrier\n"
"ds_read_b128 v[132:135], v184 offset:3072\n"
"ds_read_b128 v[148:151], v184 offset:7168\n"
"ds_read_b128 v[164:167], v184 offset:11264\n"
"ds_read_b128 v[120:123], v184\n"
"ds_read_b128 v[136:139], v184 offset:4096\n"
"ds_read_b128 v[152:155], v184 offset:8192\n"
"ds_read_b128 v[124:127], v184 offset:1024\n"
"ds_read_b128 v[140:143], v184 offset:5120\n"
"ds_read_b128 v[156:159], v184 offset:9216\n"
"ds_read_b128 v[128:131], v184 offset:2048\n"
"ds_read_b128 v[144:147], v184 offset:6144\n"
"ds_read_b128 v[160:163], v184 offset:10240\n"
"v_cndmask_b32_e64 v52, v56, v52, s[38:39]\n"
"v_cndmask_b32_e64 v53, v57, v53, s[38:39]\n"
"v_cndmask_b32_e64 v54, v58, v54, s[38:39]\n"
"v_cndmask_b32_e64 v55, v59, v55, s[38:39]\n"
"s_waitcnt lgkmcnt(11)\n"
"v_pk_add_f32 v[52:53], v[52:53], v[132:133]\n"
"v_pk_add_f32 v[54:55], v[54:55], v[134:135]\n"
"s_waitcnt lgkmcnt(10)\n"
"v_pk_add_f32 v[52:53], v[52:53], v[148:149]\n"
"v_pk_add_f32 v[54:55], v[54:55], v[150:151]\n"
"s_waitcnt lgkmcnt(9)\n"
"v_pk_add_f32 v[52:53], v[52:53], v[164:165]\n"
"v_pk_add_f32 v[54:55], v[54:55], v[166:167]\n"
"v_mov_b32 v56, v52\n"
"v_mov_b32 v57, v53\n"
"v_mov_b32 v58, v54\n"
"v_mov_b32 v59, v55\n"
"v_permlane32_swap_b32 v52, v56\n"
"v_permlane32_swap_b32 v53, v57\n"
"v_permlane32_swap_b32 v54, v58\n"
"v_permlane32_swap_b32 v55, v59\n"
"v_pk_add_f32 v[52:53], v[52:53], v[56:57]\n"
"v_pk_add_f32 v[54:55], v[54:55], v[58:59]\n"
"v_pk_fma_f32 v[32:33], v[52:53], v[38:39], v[32:33] op_sel_hi:[1,0,1]\n"
"v_pk_fma_f32 v[34:35], v[54:55], v[38:39], v[34:35] op_sel_hi:[1,0,1]\n"
"s_waitcnt lgkmcnt(8)\n"
"v_pk_add_f32 v[40:41], v[40:41], v[120:121]\n"
"v_pk_add_f32 v[42:43], v[42:43], v[122:123]\n"
"s_waitcnt lgkmcnt(7)\n"
"v_pk_add_f32 v[40:41], v[40:41], v[136:137]\n"
"v_pk_add_f32 v[42:43], v[42:43], v[138:139]\n"
"s_waitcnt lgkmcnt(6)\n"
"v_pk_add_f32 v[40:41], v[40:41], v[152:153]\n"
"v_pk_add_f32 v[42:43], v[42:43], v[154:155]\n"
"v_pk_fma_f32 v[20:21], v[40:41], v[38:39], v[20:21] op_sel_hi:[1,0,1]\n"
"v_pk_fma_f32 v[22:23], v[42:43], v[38:39], v[22:23] op_sel_hi:[1,0,1]\n"
"s_waitcnt lgkmcnt(5)\n"
"v_pk_add_f32 v[44:45], v[44:45], v[124:125]\n"
"v_pk_add_f32 v[46:47], v[46:47], v[126:127]\n"
"s_waitcnt lgkmcnt(4)\n"
"v_pk_add_f32 v[44:45], v[44:45], v[140:141]\n"
"v_pk_add_f32 v[46:47], v[46:47], v[142:143]\n"
"s_waitcnt lgkmcnt(3)\n"
"v_pk_add_f32 v[44:45], v[44:45], v[156:157]\n"
"v_pk_add_f32 v[46:47], v[46:47], v[158:159]\n"
"v_pk_fma_f32 v[24:25], v[44:45], v[38:39], v[24:25] op_sel_hi:[1,0,1]\n"
"v_pk_fma_f32 v[26:27], v[46:47], v[38:39], v[26:27] op_sel_hi:[1,0,1]\n"
"s_waitcnt lgkmcnt(2)\n"
"v_pk_add_f32 v[48:49], v[48:49], v[128:129]\n"
"v_pk_add_f32 v[50:51], v[50:51], v[130:131]\n"
"s_waitcnt lgkmcnt(1)\n"
"v_pk_add_f32 v[48:49], v[48:49], v[144:145]\n"
"v_pk_add_f32 v[50:51], v[50:51], v[146:147]\n"
"s_waitcnt lgkmcnt(0)\n"
"v_pk_add_f32 v[48:49], v[48:49], v[160:161]\n"
"v_pk_add_f32 v[50:51], v[50:51], v[162:163]\n"
"v_pk_fma_f32 v[28:29], v[48:49], v[38:39], v[28:29] op_sel_hi:[1,0,1]\n"
"v_pk_fma_f32 v[30:31], v[50:51], v[38:39], v[30:31] op_sel_hi:[1,0,1]\n"
"v_cvt_pk_bf16_f32 v60, v20, v21\n"
"v_cvt_pk_bf16_f32 v61, v22, v23\n"
"v_cvt_pk_bf16_f32 v62, v24, v25\n"
"v_cvt_pk_bf16_f32 v63, v26, v27\n"
"buffer_store_dwordx4 v[60:63], v0, s[20:23], s44 offen sc1\n"
"v_cvt_pk_bf16_f32 v64, v28, v29\n"
"v_cvt_pk_bf16_f32 v65, v30, v31\n"
"v_cvt_pk_bf16_f32 v66, v32, v33\n"
"v_cvt_pk_bf16_f32 v67, v34, v35\n"
"v_mov_b32 v69, v64\n"
"v_mov_b32 v71, v65\n"
"s_nop 0\n"
"v_permlane16_swap_b32 v69, v68\n"
"v_permlane16_swap_b32 v71, v70\n"
"v_cndmask_b32_e64 v66, v68, v66, s[38:39]\n"
"v_cndmask_b32_e64 v67, v70, v67, s[38:39]\n"
"s_mov_b64 exec, s[40:41]\n"
"buffer_store_dwordx4 v[64:67], v0, s[20:23], s44 offen offset:1024 sc1\n"
"s_mov_b64 exec, -1\n"
"v_pk_add_f32 v[72:73], v[20:21], v[22:23]\n"
"v_pk_mul_f32 v[74:75], v[20:21], v[20:21]\n"
"v_pk_mul_f32 v[76:77], v[32:33], 0.5 op_sel_hi:[1,0]\n"
"v_pk_mul_f32 v[78:79], v[34:35], 0.5 op_sel_hi:[1,0]\n"
"v_pk_fma_f32 v[74:75], v[22:23], v[22:23], v[74:75]\n"
"v_pk_add_f32 v[72:73], v[72:73], v[24:25]\n"
"v_pk_fma_f32 v[74:75], v[24:25], v[24:25], v[74:75]\n"
"v_pk_add_f32 v[72:73], v[72:73], v[26:27]\n"
"v_pk_fma_f32 v[74:75], v[26:27], v[26:27], v[74:75]\n"
"v_pk_add_f32 v[72:73], v[72:73], v[28:29]\n"
"v_pk_fma_f32 v[74:75], v[28:29], v[28:29], v[74:75]\n"
"v_pk_add_f32 v[72:73], v[72:73], v[30:31]\n"
"v_pk_fma_f32 v[74:75], v[30:31], v[30:31], v[74:75]\n"
"v_pk_add_f32 v[72:73], v[72:73], v[76:77]\n"
"v_pk_fma_f32 v[74:75], v[76:77], v[32:33], v[74:75]\n"
"v_pk_add_f32 v[72:73], v[72:73], v[78:79]\n"
"v_pk_fma_f32 v[74:75], v[78:79], v[34:35], v[74:75]\n"
"v_add_f32 v72, v72, v73\n"
"v_add_f32 v73, v74, v75\n"
"s_nop 1\n"
"v_permlane16_swap_b32 v72, v73\n"
"v_add_f32 v72, v72, v73\n"
"v_mov_b32 v73, v72\n"
"s_nop 1\n"
"v_permlane32_swap_b32 v72, v73\n"
"v_add_f32 v72, v72, v73\n"
"s_mov_b64 exec, s[38:39]\n"
"buffer_store_dword v72, v36, s[20:23], s46 offen sc1\n"
"s_mov_b64 exec, -1\n"
"s_mov_b32 s44, 0x8d1000\n"
"s_mul_i32 s51, s97, 0x1e000\n"
"s_add_u32 s44, s44, s51\n"
"s_mul_i32 s51, s96, 0x7800\n"
"s_add_u32 s44, s44, s51\n"
"s_add_u32 s45, s44, 0x3c00\n"
"s_add_u32 s49, s44, 0x3800\n"
"s_add_u32 s50, s45, 0x3800\n"
"s_waitcnt vmcnt(0)\n"
"s_waitcnt lgkmcnt(0)\n"
"s_barrier\n"
"s_cmp_lg_u32 s96, 0\n"
"s_cbranch_scc1 .Lkw0_48\n"
"s_mov_b64 exec, 0xffff\n"
"buffer_atomic_add v10, v11, s[20:23], s52 offen\n"
"s_mov_b64 exec, -1\n"
".Lkw0_48:\n"
"; A1 layer 1\n"
"ds_read_b128 v[226:229], v4 offset:128\n"
"ds_read_b128 v[230:233], v4 offset:192\n"
"ds_read_b32 v250, v5 offset:256\n"
"ds_read_b32 v251, v5 offset:320\n"
"ds_read_b128 v[182:185], v1 offset:8192\n"
"ds_read_b128 v[186:189], v1 offset:9216\n"
"ds_read_b128 v[190:193], v1 offset:10240\n"
"ds_read_b128 v[194:197], v1 offset:11264\n"
"buffer_load_dword v206, off, s[20:23], s53 offset:0 sc1\n"
"s_mov_b32 s55, 0\n"
"s_waitcnt vmcnt(0)\n"
".Lkpoll_49:\n"
"v_readfirstlane_b32 s48, v206\n"
"s_add_u32 s55, s55, 1\n"
"s_cmp_ge_u32 s48, 64\n"
"s_cbranch_scc1 .Lkdone_50\n"
"buffer_load_dword v206, off, s[20:23], s53 offset:0 sc1\n"
"s_cmp_lt_u32 s55, 0x40000\n"
"s_waitcnt vmcnt(0)\n"
"s_cbranch_scc1 .Lkpoll_49\n"
"s_mov_b32 s48, 0x40000\n"
"buffer_store_dword v10, off, s[20:23], s48 sc1\n"
".Lkdone_50:\n"
"buffer_load_dwordx4 v[38:41], v0, s[20:23], s44 offen sc1\n"
"buffer_load_dwordx4 v[42:45], v0, s[20:23], s45 offen sc1\n"
"buffer_load_dwordx4 v[206:209], v0, s[20:23], s49 offen sc1\n"
"buffer_load_dwordx4 v[210:213], v0, s[20:23], s50 offen sc1\n"
"buffer_load_dwordx4 v[46:49], v0, s[20:23], s44 offen offset:1024 sc1\n"
"buffer_load_dwordx4 v[50:53], v0, s[20:23], s45 offen offset:1024 sc1\n"
"buffer_load_dwordx4 v[54:57], v0, s[20:23], s44 offen offset:2048 sc1\n"
"buffer_load_dwordx4 v[58:61], v0, s[20:23], s45 offen offset:2048 sc1\n"
"buffer_load_dwordx4 v[62:65], v0, s[20:23], s44 offen offset:3072 sc1\n"
"buffer_load_dwordx4 v[66:69], v0, s[20:23], s45 offen offset:3072 sc1\n"
"s_add_u32 s44, s44, 0x1000\n"
"buffer_load_dwordx4 v[70:73], v0, s[20:23], s44 offen sc1\n"
"s_add_u32 s45, s45, 0x1000\n"
"buffer_load_dwordx4 v[74:77], v0, s[20:23], s45 offen sc1\n"
"buffer_load_dwordx4 v[78:81], v0, s[20:23], s44 offen offset:1024 sc1\n"
"buffer_load_dwordx4 v[82:85], v0, s[20:23], s45 offen offset:1024 sc1\n"
"s_waitcnt vmcnt(13) lgkmcnt(3)\n"
"v_mfma_f32_16x16x32_bf16 v[150:153], v[182:185], v[38:41], 0\n"
"s_waitcnt vmcnt(12)\n"
"v_mfma_f32_16x16x32_bf16 v[166:169], v[182:185], v[42:45], 0\n"
"s_waitcnt lgkmcnt(2)\n"
"ds_read_b128 v[198:201], v1 offset:12288\n"
"v_mfma_f32_16x16x32_bf16 v[154:157], v[186:189], v[38:41], 0\n"
"v_mfma_f32_16x16x32_bf16 v[170:173], v[186:189], v[42:45], 0\n"
"s_waitcnt lgkmcnt(2)\n"
"ds_read_b128 v[202:205], v1 offset:13312\n"
"buffer_load_dwordx4 v[86:89], v0, s[20:23], s44 offen offset:2048 sc1\n"
"v_mfma_f32_16x16x32_bf16 v[158:161], v[38:41], v[190:193], 0\n"
"v_mfma_f32_16x16x32_bf16 v[174:177], v[42:45], v[190:193], 0\n"
"s_waitcnt lgkmcnt(2)\n"
"ds_read_b128 v[182:185], v1 offset:14336\n"
"v_mfma_f32_16x16x32_bf16 v[162:165], v[38:41], v[194:197], 0\n"
"v_mfma_f32_16x16x32_bf16 v[178:181], v[42:45], v[194:197], 0\n"
"s_waitcnt vmcnt(10) lgkmcnt(2)\n"
"ds_read_b128 v[186:189], v1 offset:15360\n"
"buffer_load_dwordx4 v[90:93], v0, s[20:23], s45 offen offset:2048 sc1\n"
"v_mfma_f32_16x16x32_bf16 v[150:153], v[198:201], v[46:49], v[150:153]\n"
"s_waitcnt vmcnt(10)\n"
"v_mfma_f32_16x16x32_bf16 v[166:169], v[198:201], v[50:53], v[166:169]\n"
"s_waitcnt lgkmcnt(2)\n"
"ds_read_b128 v[190:193], v1 offset:16384\n"
"v_add_f32 v206, v206, v208\n"
"v_add_f32 v210, v210, v212\n"
"v_mfma_f32_16x16x32_bf16 v[154:157], v[202:205], v[46:49], v[154:157]\n"
"v_mfma_f32_16x16x32_bf16 v[170:173], v[202:205], v[50:53], v[170:173]\n"
"s_waitcnt lgkmcnt(2)\n"
"ds_read_b128 v[194:197], v1 offset:17408\n"
"buffer_load_dwordx4 v[94:97], v0, s[20:23], s44 offen offset:3072 sc1\n"
"v_add_f32 v207, v207, v209\n"
"v_add_f32 v211, v211, v213\n"
"v_mfma_f32_16x16x32_bf16 v[158:161], v[46:49], v[182:185], v[158:161]\n"
"v_mfma_f32_16x16x32_bf16 v[174:177], v[50:53], v[182:185], v[174:177]\n"
"s_waitcnt lgkmcnt(2)\n"
"ds_read_b128 v[198:201], v1 offset:18432\n"
"v_permlane16_swap_b32 v206, v210\n"
"v_permlane16_swap_b32 v207, v211\n"
"v_mfma_f32_16x16x32_bf16 v[162:165], v[46:49], v[186:189], v[162:165]\n"
"v_mfma_f32_16x16x32_bf16 v[178:181], v[50:53], v[186:189], v[178:181]\n"
"s_waitcnt vmcnt(10) lgkmcnt(2)\n"
"ds_read_b128 v[202:205], v1 offset:19456\n"
"buffer_load_dwordx4 v[98:101], v0, s[20:23], s45 offen offset:3072 sc1\n"
"v_add_f32 v206, v206, v210\n"
"v_add_f32 v207, v207, v211\n"
"v_mfma_f32_16x16x32_bf16 v[150:153], v[190:193], v[54:57], v[150:153]\n"
"s_waitcnt vmcnt(10)\n"
"v_mfma_f32_16x16x32_bf16 v[166:169], v[190:193], v[58:61], v[166:169]\n"
"s_waitcnt lgkmcnt(2)\n"
"ds_read_b128 v[182:185], v1 offset:20480\n"
"v_permlane32_swap_b32 v206, v207\n"
"v_add_f32 v206, v206, v207\n"
"v_mfma_f32_16x16x32_bf16 v[154:157], v[194:197], v[54:57], v[154:157]\n"
"v_mfma_f32_16x16x32_bf16 v[170:173], v[194:197], v[58:61], v[170:173]\n"
"s_waitcnt lgkmcnt(2)\n"
"ds_read_b128 v[186:189], v1 offset:21504\n"
"s_add_u32 s44, s44, 0x1000\n"
"buffer_load_dwordx4 v[102:105], v0, s[20:23], s44 offen sc1\n"
"v_mov_b32 v207, v206\n"
"s_nop 1\n"
"v_permlane32_swap_b32 v206, v207\n"
"v_mfma_f32_16x16x32_bf16 v[158:161], v[54:57], v[198:201], v[158:161]\n"
"v_mfma_f32_16x16x32_bf16 v[174:177], v[58:61], v[198:201], v[174:177]\n"
"s_waitcnt lgkmcnt(2)\n"
"ds_read_b128 v[190:193], v1 offset:22528\n"
"v_mul_f32 v206, 0x3b124925, v206\n"
"v_mul_f32 v210, v206, v206\n"
"v_mfma_f32_16x16x32_bf16 v[162:165], v[54:57], v[202:205], v[162:165]\n"
"v_mfma_f32_16x16x32_bf16 v[178:181], v[58:61], v[202:205], v[178:181]\n"
"s_waitcnt vmcnt(10) lgkmcnt(2)\n"
"ds_read_b128 v[194:197], v1 offset:23552\n"
"s_add_u32 s45, s45, 0x1000\n"
"buffer_load_dwordx4 v[106:109], v0, s[20:23], s45 offen sc1\n"
"v_fma_f32 v210, v207, v37, -v210\n"
"v_max_f32 v210, 0, v210\n"
"v_mfma_f32_16x16x32_bf16 v[150:153], v[182:185], v[62:65], v[150:153]\n"
"s_waitcnt vmcnt(10)\n"
"v_mfma_f32_16x16x32_bf16 v[166:169], v[182:185], v[66:69], v[166:169]\n"
"s_waitcnt lgkmcnt(2)\n"
"ds_read_b128 v[198:201], v1 offset:24576\n"
"v_add_f32 v210, 0x3727c5ac, v210\n"
"v_rsq_f32 v216, v210\n"
"v_mfma_f32_16x16x32_bf16 v[154:157], v[186:189], v[62:65], v[154:157]\n"
"v_mfma_f32_16x16x32_bf16 v[170:173], v[186:189], v[66:69], v[170:173]\n"
"s_waitcnt lgkmcnt(2)\n"
"ds_read_b128 v[202:205], v1 offset:25600\n"
"buffer_load_dwordx4 v[110:113], v0, s[20:23], s44 offen offset:1024 sc1\n"
"v_mul_f32_e64 v214, v216, -v206\n"
"ds_write_b32 v6, v216\n"
"v_mfma_f32_16x16x32_bf16 v[158:161], v[62:65], v[190:193], v[158:161]\n"
"v_mfma_f32_16x16x32_bf16 v[174:177], v[66:69], v[190:193], v[174:177]\n"
"s_waitcnt lgkmcnt(3)\n"
"ds_read_b128 v[182:185], v1 offset:26624\n"
"ds_write_b32 v6, v214 offset:128\n"
"v_mov_b32 v217, v216\n"
"v_mfma_f32_16x16x32_bf16 v[162:165], v[62:65], v[194:197], v[162:165]\n"
"v_mfma_f32_16x16x32_bf16 v[178:181], v[66:69], v[194:197], v[178:181]\n"
"s_waitcnt vmcnt(10) lgkmcnt(4)\n"
"ds_read_b128 v[186:189], v1 offset:27648\n"
"buffer_load_dwordx4 v[114:117], v0, s[20:23], s45 offen offset:1024 sc1\n"
"v_mov_b32 v215, v214\n"
"v_permlane16_swap_b32 v216, v217\n"
"v_mfma_f32_16x16x32_bf16 v[150:153], v[198:201], v[70:73], v[150:153]\n"
"s_waitcnt vmcnt(10)\n"
"v_mfma_f32_16x16x32_bf16 v[166:169], v[198:201], v[74:77], v[166:169]\n"
"s_waitcnt lgkmcnt(4)\n"
"ds_read_b128 v[190:193], v1 offset:28672\n"
"v_permlane16_swap_b32 v214, v215\n"
"ds_read_b128 v[218:221], v7\n"
"v_mfma_f32_16x16x32_bf16 v[154:157], v[202:205], v[70:73], v[154:157]\n"
"v_mfma_f32_16x16x32_bf16 v[170:173], v[202:205], v[74:77], v[170:173]\n"
"s_waitcnt lgkmcnt(4)\n"
"ds_read_b128 v[194:197], v1 offset:29696\n"
"buffer_load_dwordx4 v[118:121], v0, s[20:23], s44 offen offset:2048 sc1\n"
"ds_read_b128 v[222:225], v7 offset:64\n"
"ds_read_b128 v[234:237], v7 offset:128\n"
"v_mfma_f32_16x16x32_bf16 v[158:161], v[70:73], v[182:185], v[158:161]\n"
"v_mfma_f32_16x16x32_bf16 v[174:177], v[74:77], v[182:185], v[174:177]\n"
"s_waitcnt lgkmcnt(5)\n"
"ds_read_b128 v[198:201], v1 offset:30720\n"
"ds_read_b128 v[238:241], v7 offset:192\n"
"v_mul_f32 v206, v226, v214\n"
"v_mfma_f32_16x16x32_bf16 v[162:165], v[70:73], v[186:189], v[162:165]\n"
"v_mfma_f32_16x16x32_bf16 v[178:181], v[74:77], v[186:189], v[178:181]\n"
"s_waitcnt vmcnt(10) lgkmcnt(6)\n"
"ds_read_b128 v[202:205], v1 offset:31744\n"
"buffer_load_dwordx4 v[122:125], v0, s[20:23], s45 offen offset:2048 sc1\n"
"v_mul_f32 v207, v227, v214\n"
"v_mul_f32 v208, v228, v214\n"
"v_mfma_f32_16x16x32_bf16 v[150:153], v[190:193], v[78:81], v[150:153]\n"
"s_waitcnt vmcnt(10)\n"
"v_mfma_f32_16x16x32_bf16 v[166:169], v[190:193], v[82:85], v[166:169]\n"
"s_waitcnt lgkmcnt(5)\n"
"ds_read_b128 v[182:185], v1 offset:32768\n"
"v_mul_f32 v209, v229, v214\n"
"v_mul_f32 v210, v230, v214\n"
"v_mfma_f32_16x16x32_bf16 v[154:157], v[194:197], v[78:81], v[154:157]\n"
"v_mfma_f32_16x16x32_bf16 v[170:173], v[194:197], v[82:85], v[170:173]\n"
"s_waitcnt lgkmcnt(3)\n"
"ds_read_b128 v[186:189], v1 offset:33792\n"
"buffer_load_dwordx4 v[126:129], v0, s[20:23], s44 offen offset:3072 sc1\n"
"v_mul_f32 v211, v231, v214\n"
"v_mul_f32 v212, v232, v214\n"
"v_mfma_f32_16x16x32_bf16 v[158:161], v[78:81], v[198:201], v[158:161]\n"
"v_mfma_f32_16x16x32_bf16 v[174:177], v[82:85], v[198:201], v[174:177]\n"
"s_waitcnt lgkmcnt(2)\n"
"ds_read_b128 v[190:193], v1 offset:34816\n"
"v_mul_f32 v213, v233, v214\n"
"v_mul_f32 v226, v226, v215\n"
"v_mfma_f32_16x16x32_bf16 v[162:165], v[78:81], v[202:205], v[162:165]\n"
"v_mfma_f32_16x16x32_bf16 v[178:181], v[82:85], v[202:205], v[178:181]\n"
"s_waitcnt vmcnt(10) lgkmcnt(2)\n"
"ds_read_b128 v[194:197], v1 offset:35840\n"
"buffer_load_dwordx4 v[130:133], v0, s[20:23], s45 offen offset:3072 sc1\n"
"v_mul_f32 v227, v227, v215\n"
"v_mul_f32 v228, v228, v215\n"
"v_mfma_f32_16x16x32_bf16 v[150:153], v[182:185], v[86:89], v[150:153]\n"
"s_waitcnt vmcnt(10)\n"
"v_mfma_f32_16x16x32_bf16 v[166:169], v[182:185], v[90:93], v[166:169]\n"
"s_waitcnt lgkmcnt(2)\n"
"ds_read_b128 v[198:201], v1 offset:36864\n"
"v_mul_f32 v229, v229, v215\n"
"v_mul_f32 v230, v230, v215\n"
"v_mfma_f32_16x16x32_bf16 v[154:157], v[186:189], v[86:89], v[154:157]\n"
"v_mfma_f32_16x16x32_bf16 v[170:173], v[186:189], v[90:93], v[170:173]\n"
"s_waitcnt lgkmcnt(2)\n"
"ds_read_b128 v[202:205], v1 offset:37888\n"
"s_add_u32 s44, s44, 0x1000\n"
"buffer_load_dwordx4 v[134:137], v0, s[20:23], s44 offen sc1\n"
"v_mul_f32 v231, v231, v215\n"
"v_mul_f32 v232, v232, v215\n"
"v_mfma_f32_16x16x32_bf16 v[158:161], v[86:89], v[190:193], v[158:161]\n"
"v_mfma_f32_16x16x32_bf16 v[174:177], v[90:93], v[190:193], v[174:177]\n"
"s_waitcnt lgkmcnt(2)\n"
"ds_read_b128 v[182:185], v1 offset:38912\n"
"v_mul_f32 v233, v233, v215\n"
"v_mul_f32 v242, v234, v250\n"
"v_mfma_f32_16x16x32_bf16 v[162:165], v[86:89], v[194:197], v[162:165]\n"
"v_mfma_f32_16x16x32_bf16 v[178:181], v[90:93], v[194:197], v[178:181]\n"
"s_waitcnt vmcnt(10) lgkmcnt(2)\n"
"ds_read_b128 v[186:189], v1 offset:39936\n"
"s_add_u32 s45, s45, 0x1000\n"
"buffer_load_dwordx4 v[138:141], v0, s[20:23], s45 offen sc1\n"
"v_mul_f32 v243, v235, v250\n"
"v_mul_f32 v244, v236, v250\n"
"v_mfma_f32_16x16x32_bf16 v[150:153], v[198:201], v[94:97], v[150:153]\n"
"s_waitcnt vmcnt(10)\n"
"v_mfma_f32_16x16x32_bf16 v[166:169], v[198:201], v[98:101], v[166:169]\n"
"s_waitcnt lgkmcnt(2)\n"
"ds_read_b128 v[190:193], v1 offset:40960\n"
"v_mul_f32 v245, v237, v250\n"
"v_mul_f32 v246, v238, v250\n"
"v_mfma_f32_16x16x32_bf16 v[154:157], v[202:205], v[94:97], v[154:157]\n"
"v_mfma_f32_16x16x32_bf16 v[170:173], v[202:205], v[98:101], v[170:173]\n"
"s_waitcnt lgkmcnt(2)\n"
"ds_read_b128 v[194:197], v1 offset:41984\n"
"buffer_load_dwordx4 v[142:145], v0, s[20:23], s44 offen offset:1024 sc1\n"
"v_mul_f32 v247, v239, v250\n"
"v_mul_f32 v248, v240, v250\n"
"v_mfma_f32_16x16x32_bf16 v[158:161], v[94:97], v[182:185], v[158:161]\n"
"v_mfma_f32_16x16x32_bf16 v[174:177], v[98:101], v[182:185], v[174:177]\n"
"s_waitcnt lgkmcnt(2)\n"
"ds_read_b128 v[198:201], v1 offset:43008\n"
"v_mul_f32 v249, v241, v250\n"
"v_mul_f32 v234, v234, v251\n"
"v_mfma_f32_16x16x32_bf16 v[162:165], v[94:97], v[186:189], v[162:165]\n"
"v_mfma_f32_16x16x32_bf16 v[178:181], v[98:101], v[186:189], v[178:181]\n"
"s_waitcnt vmcnt(10) lgkmcnt(2)\n"
"ds_read_b128 v[202:205], v1 offset:44032\n"
"buffer_load_dwordx4 v[146:149], v0, s[20:23], s45 offen offset:1024 sc1\n"
"v_mul_f32 v235, v235, v251\n"
"v_mul_f32 v236, v236, v251\n"
"v_mfma_f32_16x16x32_bf16 v[150:153], v[190:193], v[102:105], v[150:153]\n"
"s_waitcnt vmcnt(10)\n"
"v_mfma_f32_16x16x32_bf16 v[166:169], v[190:193], v[106:109], v[166:169]\n"
"s_waitcnt lgkmcnt(2)\n"
"ds_read_b128 v[182:185], v1 offset:45056\n"
"v_mul_f32 v237, v237, v251\n"
"v_mul_f32 v238, v238, v251\n"
"v_mfma_f32_16x16x32_bf16 v[154:157], v[194:197], v[102:105], v[154:157]\n"
"v_mfma_f32_16x16x32_bf16 v[170:173], v[194:197], v[106:109], v[170:173]\n"
"s_waitcnt lgkmcnt(2)\n"
"ds_read_b128 v[186:189], v1 offset:46080\n"
"s_add_u32 m0, s67, 0x20000\n"
"s_add_u32 s60, s67, s64\n"
"s_add_u32 s60, s60, 0xe000\n"
"buffer_load_dwordx4 v0, s[20:23], s60 offen sc1 lds\n"
"v_mul_f32 v239, v239, v251\n"
"v_mul_f32 v240, v240, v251\n"
"v_mfma_f32_16x16x32_bf16 v[158:161], v[102:105], v[198:201], v[158:161]\n"
"v_mfma_f32_16x16x32_bf16 v[174:177], v[106:109], v[198:201], v[174:177]\n"
"s_waitcnt lgkmcnt(2)\n"
"ds_read_b128 v[190:193], v1 offset:47104\n"
"v_mul_f32 v241, v241, v251\n"
"v_mfma_f32_16x16x32_bf16 v[162:165], v[102:105], v[202:205], v[162:165]\n"
"v_mfma_f32_16x16x32_bf16 v[178:181], v[106:109], v[202:205], v[178:181]\n"
"s_waitcnt vmcnt(10) lgkmcnt(2)\n"
"ds_read_b128 v[194:197], v1 offset:48128\n"
"s_add_u32 m0, m0, 0x1000\n"
"s_add_u32 s60, s60, 0x1000\n"
"buffer_load_dwordx4 v0, s[20:23], s60 offen sc1 lds\n"
"v_mfma_f32_16x16x32_bf16 v[150:153], v[182:185], v[110:113], v[150:153]\n"
"s_waitcnt vmcnt(10)\n"
"v_mfma_f32_16x16x32_bf16 v[166:169], v[182:185], v[114:117], v[166:169]\n"
"s_waitcnt lgkmcnt(2)\n"
"ds_read_b128 v[198:201], v1 offset:49152\n"
"v_mfma_f32_16x16x32_bf16 v[154:157], v[186:189], v[110:113], v[154:157]\n"
"v_mfma_f32_16x16x32_bf16 v[170:173], v[186:189], v[114:117], v[170:173]\n"
"s_waitcnt lgkmcnt(2)\n"
"ds_read_b128 v[202:205], v1 offset:50176\n"
"s_add_u32 m0, m0, 0x1000\n"
"s_add_u32 s60, s60, 0x1000\n"
"buffer_load_dwordx4 v0, s[20:23], s60 offen sc1 lds\n"
"v_mfma_f32_16x16x32_bf16 v[158:161], v[110:113], v[190:193], v[158:161]\n"
"v_mfma_f32_16x16x32_bf16 v[174:177], v[114:117], v[190:193], v[174:177]\n"
"s_waitcnt lgkmcnt(2)\n"
"ds_read_b128 v[182:185], v1 offset:51200\n"
"v_mfma_f32_16x16x32_bf16 v[162:165], v[110:113], v[194:197], v[162:165]\n"
"v_mfma_f32_16x16x32_bf16 v[178:181], v[114:117], v[194:197], v[178:181]\n"
"s_waitcnt vmcnt(10) lgkmcnt(2)\n"
"ds_read_b128 v[186:189], v1 offset:52224\n"
"s_add_u32 m0, m0, 0x1000\n"
"s_add_u32 s60, s60, 0x1000\n"
"buffer_load_dwordx4 v0, s[20:23], s60 offen sc1 lds\n"
"v_mfma_f32_16x16x32_bf16 v[150:153], v[198:201], v[118:121], v[150:153]\n"
"s_waitcnt vmcnt(10)\n"
"v_mfma_f32_16x16x32_bf16 v[166:169], v[198:201], v[122:125], v[166:169]\n"
"s_waitcnt lgkmcnt(2)\n"
"ds_read_b128 v[190:193], v1 offset:53248\n"
"v_mfma_f32_16x16x32_bf16 v[154:157], v[202:205], v[118:121], v[154:157]\n"
"v_mfma_f32_16x16x32_bf16 v[170:173], v[202:205], v[122:125], v[170:173]\n"
"s_waitcnt lgkmcnt(2)\n"
"ds_read_b128 v[194:197], v1 offset:54272\n"
"s_add_u32 m0, m0, 0x1000\n"
"s_add_u32 s60, s60, 0x1000\n"
"buffer_load_dwordx4 v0, s[20:23], s60 offen sc1 lds\n"
"v_mfma_f32_16x16x32_bf16 v[158:161], v[118:121], v[182:185], v[158:161]\n"
"v_mfma_f32_16x16x32_bf16 v[174:177], v[122:125], v[182:185], v[174:177]\n"
"s_waitcnt lgkmcnt(2)\n"
"ds_read_b128 v[198:201], v1 offset:55296\n"
"v_mfma_f32_16x16x32_bf16 v[162:165], v[118:121], v[186:189], v[162:165]\n"
"v_mfma_f32_16x16x32_bf16 v[178:181], v[122:125], v[186:189], v[178:181]\n"
"s_waitcnt vmcnt(10) lgkmcnt(2)\n"
"ds_read_b128 v[202:205], v1 offset:56320\n"
"s_add_u32 m0, m0, 0x1000\n"
"s_add_u32 s60, s60, 0x1000\n"
"buffer_load_dwordx4 v0, s[20:23], s60 offen sc1 lds\n"
"v_mfma_f32_16x16x32_bf16 v[150:153], v[190:193], v[126:129], v[150:153]\n"
"s_waitcnt vmcnt(10)\n"
"v_mfma_f32_16x16x32_bf16 v[166:169], v[190:193], v[130:133], v[166:169]\n"
"s_waitcnt lgkmcnt(2)\n"
"ds_read_b128 v[182:185], v1 offset:57344\n"
"v_mfma_f32_16x16x32_bf16 v[154:157], v[194:197], v[126:129], v[154:157]\n"
"v_mfma_f32_16x16x32_bf16 v[170:173], v[194:197], v[130:133], v[170:173]\n"
"s_waitcnt lgkmcnt(2)\n"
"ds_read_b128 v[186:189], v1 offset:58368\n"
"s_add_u32 m0, m0, 0x1000\n"
"s_add_u32 s60, s60, 0x1000\n"
"buffer_load_dwordx4 v0, s[20:23], s60 offen sc1 lds\n"
"v_mfma_f32_16x16x32_bf16 v[158:161], v[126:129], v[198:201], v[158:161]\n"
"v_mfma_f32_16x16x32_bf16 v[174:177], v[130:133], v[198:201], v[174:177]\n"
"s_waitcnt lgkmcnt(2)\n"
"ds_read_b128 v[190:193], v1 offset:59392\n"
"v_mfma_f32_16x16x32_bf16 v[162:165], v[126:129], v[202:205], v[162:165]\n"
"v_mfma_f32_16x16x32_bf16 v[178:181], v[130:133], v[202:205], v[178:181]\n"
"s_waitcnt vmcnt(10) lgkmcnt(2)\n"
"ds_read_b128 v[194:197], v1 offset:60416\n"
"v_mfma_f32_16x16x32_bf16 v[150:153], v[182:185], v[134:137], v[150:153]\n"
"s_waitcnt vmcnt(9)\n"
"v_mfma_f32_16x16x32_bf16 v[166:169], v[182:185], v[138:141], v[166:169]\n"
"s_waitcnt lgkmcnt(2)\n"
"ds_read_b128 v[198:201], v1 offset:61440\n"
"v_mfma_f32_16x16x32_bf16 v[154:157], v[186:189], v[134:137], v[154:157]\n"
"v_mfma_f32_16x16x32_bf16 v[170:173], v[186:189], v[138:141], v[170:173]\n"
"s_waitcnt lgkmcnt(2)\n"
"ds_read_b128 v[202:205], v1 offset:62464\n"
"v_mfma_f32_16x16x32_bf16 v[158:161], v[134:137], v[190:193], v[158:161]\n"
"v_mfma_f32_16x16x32_bf16 v[174:177], v[138:141], v[190:193], v[174:177]\n"
"s_waitcnt lgkmcnt(2)\n"
"ds_read_b128 v[182:185], v1 offset:63488\n"
"v_mfma_f32_16x16x32_bf16 v[162:165], v[134:137], v[194:197], v[162:165]\n"
"v_mfma_f32_16x16x32_bf16 v[178:181], v[138:141], v[194:197], v[178:181]\n"
"s_waitcnt vmcnt(8) lgkmcnt(2)\n"
"ds_read_b128 v[186:189], v1 offset:64512\n"
"v_mfma_f32_16x16x32_bf16 v[150:153], v[198:201], v[142:145], v[150:153]\n"
"s_waitcnt vmcnt(7)\n"
"v_mfma_f32_16x16x32_bf16 v[166:169], v[198:201], v[146:149], v[166:169]\n"
"s_waitcnt lgkmcnt(2)\n"
"s_mov_b32 s46, 0x1091000\n"
"s_mul_i32 s51, s34, 0x40000\n"
"s_add_u32 s46, s46, s51\n"
"s_mul_i32 s51, s97, 0x4000\n"
"s_add_u32 s46, s46, s51\n"
"s_mul_i32 s51, s96, 0x1000\n"
"s_add_u32 s46, s46, s51\n"
"v_mfma_f32_16x16x32_bf16 v[154:157], v[202:205], v[142:145], v[154:157]\n"
"v_mfma_f32_16x16x32_bf16 v[170:173], v[202:205], v[146:149], v[170:173]\n"
"s_waitcnt lgkmcnt(1)\n"
"v_mfma_f32_16x16x32_bf16 v[158:161], v[142:145], v[182:185], v[158:161]\n"
"v_mfma_f32_16x16x32_bf16 v[174:177], v[146:149], v[182:185], v[174:177]\n"
"s_waitcnt lgkmcnt(0)\n"
"v_mfma_f32_16x16x32_bf16 v[162:165], v[142:145], v[186:189], v[162:165]\n"
"v_mfma_f32_16x16x32_bf16 v[178:181], v[146:149], v[186:189], v[178:181]\n"
"v_pk_fma_f32 v[150:151], v[150:151], v[216:217], v[206:207] op_sel_hi:[1,0,1]\n"
"v_pk_fma_f32 v[152:153], v[152:153], v[216:217], v[208:209] op_sel_hi:[1,0,1]\n"
"v_pk_fma_f32 v[154:155], v[154:155], v[216:217], v[210:211] op_sel_hi:[1,0,1]\n"
"v_pk_fma_f32 v[156:157], v[156:157], v[216:217], v[212:213] op_sel_hi:[1,0,1]\n"
"v_cvt_pk_bf16_f32 v190, v150, v151\n"
"v_cvt_pk_bf16_f32 v191, v152, v153\n"
"v_cvt_pk_bf16_f32 v192, v154, v155\n"
"v_cvt_pk_bf16_f32 v193, v156, v157\n"
"buffer_store_dwordx4 v[190:193], v0, s[20:23], s46 offen sc1\n"
"v_pk_fma_f32 v[166:167], v[166:167], v[216:217], v[226:227] op_sel:[0,1,0]\n"
"v_pk_fma_f32 v[168:169], v[168:169], v[216:217], v[228:229] op_sel:[0,1,0]\n"
"v_pk_fma_f32 v[170:171], v[170:171], v[216:217], v[230:231] op_sel:[0,1,0]\n"
"v_pk_fma_f32 v[172:173], v[172:173], v[216:217], v[232:233] op_sel:[0,1,0]\n"
"v_cvt_pk_bf16_f32 v194, v166, v167\n"
"v_cvt_pk_bf16_f32 v195, v168, v169\n"
"v_cvt_pk_bf16_f32 v196, v170, v171\n"
"v_cvt_pk_bf16_f32 v197, v172, v173\n"
"buffer_store_dwordx4 v[194:197], v0, s[20:23], s46 offen offset:1024 sc1\n"
"v_pk_fma_f32 v[158:159], v[158:159], v[218:219], v[242:243]\n"
"v_pk_fma_f32 v[160:161], v[160:161], v[220:221], v[244:245]\n"
"v_pk_fma_f32 v[174:175], v[174:175], v[222:223], v[246:247]\n"
"v_pk_fma_f32 v[176:177], v[176:177], v[224:225], v[248:249]\n"
"v_cvt_pk_bf16_f32 v186, v158, v159\n"
"v_cvt_pk_bf16_f32 v187, v160, v161\n"
"v_cvt_pk_bf16_f32 v188, v174, v175\n"
"v_cvt_pk_bf16_f32 v189, v176, v177\n"
"buffer_store_dwordx4 v[186:189], v0, s[20:23], s46 offen offset:2048 sc1\n"
"v_pk_fma_f32 v[162:163], v[162:163], v[218:219], v[234:235]\n"
"v_pk_fma_f32 v[164:165], v[164:165], v[220:221], v[236:237]\n"
"v_pk_fma_f32 v[178:179], v[178:179], v[222:223], v[238:239]\n"
"v_pk_fma_f32 v[180:181], v[180:181], v[224:225], v[240:241]\n"
"v_cvt_pk_bf16_f32 v182, v162, v163\n"
"v_cvt_pk_bf16_f32 v183, v164, v165\n"
"v_cvt_pk_bf16_f32 v184, v178, v179\n"
"v_cvt_pk_bf16_f32 v185, v180, v181\n"
"s_mov_b64 exec, s[42:43]\n"
"v_mov_b32 v182, 0x3f803f80\n"
"v_mov_b32 v183, 0x3f803f80\n"
"v_mov_b32 v184, 0x3f803f80\n"
"v_mov_b32 v185, 0x3f803f80\n"
"s_mov_b64 exec, -1\n"
"buffer_store_dwordx4 v[182:185], v0, s[20:23], s46 offen offset:3072 sc1\n"
"ds_read_b128 v[226:229], v4\n"
"ds_read_b128 v[230:233], v4 offset:64\n"
"s_lshl_b32 s48, s96, 12\n"
"v_add_u32 v252, s48, v0\n"
"ds_write_b128 v252, v[190:193] offset:32768\n"
"ds_write_b128 v252, v[194:197] offset:33792\n"
"ds_write_b128 v252, v[186:189] offset:34816\n"
"ds_write_b128 v252, v[182:185] offset:35840\n"
"v_mul_f32 v250, 0x3e8b97f6, v216\n"
"v_mul_f32 v251, 0x3e8b97f6, v217\n"
"v_mul_f32 v242, 0x3e8b97f6, v214\n"
"v_mul_f32 v243, 0x3e8b97f6, v215\n"
"s_waitcnt lgkmcnt(5)\n"
"v_pk_mul_f32 v[206:207], v[226:227], v[242:243] op_sel_hi:[1,0]\n"
"v_pk_mul_f32 v[208:209], v[228:229], v[242:243] op_sel_hi:[1,0]\n"
"s_waitcnt lgkmcnt(4)\n"
"v_pk_mul_f32 v[210:211], v[230:231], v[242:243] op_sel_hi:[1,0]\n"
"v_pk_mul_f32 v[212:213], v[232:233], v[242:243] op_sel_hi:[1,0]\n"
"v_pk_mul_f32 v[226:227], v[226:227], v[242:243] op_sel:[0,1]\n"
"v_pk_mul_f32 v[228:229], v[228:229], v[242:243] op_sel:[0,1]\n"
"v_pk_mul_f32 v[230:231], v[230:231], v[242:243] op_sel:[0,1]\n"
"v_pk_mul_f32 v[232:233], v[232:233], v[242:243] op_sel:[0,1]\n"
"s_add_u32 s47, s53, 0x4180\n"
"v_mul_u32_u24 v254, 0x3f8, v0\n"
"s_sub_u32 s50, 15, s97\n"
"s_min_u32 s50, s50, 2\n"
"s_bfm_b64 s[56:57], s50, 0\n"
"s_lshl_b32 s48, s97, 2\n"
"s_lshl_b32 s49, s96, 1\n"
"s_add_u32 s48, s48, s49\n"
"s_lshl_b32 s49, s34, 6\n"
"s_add_u32 s49, s49, s48\n"
"s_lshl_b32 s49, s49, 12\n"
"s_add_u32 s44, s49, 0x1089000\n"
"s_cmp_ge_u32 s48, 8\n"
"s_cselect_b32 s44, s44, 0\n"
"s_cselect_b32 s49, 0, 0x7ff00000\n"
"s_cselect_b32 s46, 2, 0\n"
"v_add_u32 v253, s49, v0\n"
"s_add_u32 s45, s44, 0x1000\n"
"s_waitcnt vmcnt(0)\n"
"s_waitcnt lgkmcnt(0)\n"
"s_barrier\n"
"s_cmp_lg_u32 s96, 0\n"
"s_cbranch_scc1 .Lkw0_52\n"
"s_cmp_eq_u32 s50, 0\n"
"s_cbranch_scc1 .Lknosig_51\n"
"s_mov_b64 exec, s[56:57]\n"
"buffer_atomic_add v10, v254, s[20:23], s47 offen\n"
"s_mov_b64 exec, -1\n"
".Lknosig_51:\n"
".Lkw0_52:\n"
"ds_read_b128 v[150:153], v2\n"
"ds_read_b128 v[154:157], v2 offset:1024\n"
"ds_read_b128 v[158:161], v2 offset:2048\n"
"ds_read_b128 v[162:165], v2 offset:3072\n"
"s_waitcnt lgkmcnt(3)\n"
"v_mfma_f32_16x16x32_bf16 v[218:221], v[150:153], v[38:41], 0\n"
"v_mfma_f32_16x16x32_bf16 v[234:237], v[150:153], v[42:45], 0\n"
"s_waitcnt lgkmcnt(2)\n"
"ds_read_b128 v[166:169], v2 offset:4096\n"
"s_mul_i32 s63, s67, 14\n"
"s_add_u32 m0, s63, 0x12000\n"
"s_add_u32 s60, s63, s66\n"
"buffer_load_dwordx4 v0, s[20:23], s60 offen sc1 lds\n"
"v_mfma_f32_16x16x32_bf16 v[222:225], v[154:157], v[38:41], 0\n"
"v_mfma_f32_16x16x32_bf16 v[238:241], v[154:157], v[42:45], 0\n"
"s_waitcnt lgkmcnt(2)\n"
"ds_read_b128 v[170:173], v2 offset:5120\n"
"v_mfma_f32_16x16x32_bf16 v[218:221], v[158:161], v[46:49], v[218:221]\n"
"v_mfma_f32_16x16x32_bf16 v[234:237], v[158:161], v[50:53], v[234:237]\n"
"s_waitcnt lgkmcnt(2)\n"
"ds_read_b128 v[150:153], v2 offset:6144\n"
"buffer_load_dwordx4 v0, s[20:23], s60 offen offset:1024 sc1 lds\n"
"v_mfma_f32_16x16x32_bf16 v[222:225], v[162:165], v[46:49], v[222:225]\n"
"v_mfma_f32_16x16x32_bf16 v[238:241], v[162:165], v[50:53], v[238:241]\n"
"s_waitcnt lgkmcnt(2)\n"
"ds_read_b128 v[154:157], v2 offset:7168\n"
"v_mfma_f32_16x16x32_bf16 v[218:221], v[166:169], v[54:57], v[218:221]\n"
"v_mfma_f32_16x16x32_bf16 v[234:237], v[166:169], v[58:61], v[234:237]\n"
"s_waitcnt lgkmcnt(2)\n"
"ds_read_b128 v[158:161], v2 offset:8192\n"
"buffer_load_dwordx4 v0, s[20:23], s60 offen offset:2048 sc1 lds\n"
"v_mfma_f32_16x16x32_bf16 v[222:225], v[170:173], v[54:57], v[222:225]\n"
"v_mfma_f32_16x16x32_bf16 v[238:241], v[170:173], v[58:61], v[238:241]\n"
"s_waitcnt lgkmcnt(2)\n"
"ds_read_b128 v[162:165], v2 offset:9216\n"
"v_mfma_f32_16x16x32_bf16 v[218:221], v[150:153], v[62:65], v[218:221]\n"
"v_mfma_f32_16x16x32_bf16 v[234:237], v[150:153], v[66:69], v[234:237]\n"
"s_waitcnt lgkmcnt(2)\n"
"ds_read_b128 v[166:169], v2 offset:10240\n"
"buffer_load_dwordx4 v0, s[20:23], s60 offen offset:3072 sc1 lds\n"
"v_mfma_f32_16x16x32_bf16 v[222:225], v[154:157], v[62:65], v[222:225]\n"
"v_mfma_f32_16x16x32_bf16 v[238:241], v[154:157], v[66:69], v[238:241]\n"
"s_waitcnt lgkmcnt(2)\n"
"ds_read_b128 v[170:173], v2 offset:11264\n"
"v_mfma_f32_16x16x32_bf16 v[218:221], v[158:161], v[70:73], v[218:221]\n"
"v_mfma_f32_16x16x32_bf16 v[234:237], v[158:161], v[74:77], v[234:237]\n"
"s_waitcnt lgkmcnt(2)\n"
"ds_read_b128 v[150:153], v2 offset:12288\n"
"s_add_u32 m0, m0, 0x1000\n"
"s_add_u32 s60, s60, 0x1000\n"
"buffer_load_dwordx4 v0, s[20:23], s60 offen sc1 lds\n"
"v_mfma_f32_16x16x32_bf16 v[222:225], v[162:165], v[70:73], v[222:225]\n"
"v_mfma_f32_16x16x32_bf16 v[238:241], v[162:165], v[74:77], v[238:241]\n"
"s_waitcnt lgkmcnt(2)\n"
"ds_read_b128 v[154:157], v2 offset:13312\n"
"v_mfma_f32_16x16x32_bf16 v[218:221], v[166:169], v[78:81], v[218:221]\n"
"v_mfma_f32_16x16x32_bf16 v[234:237], v[166:169], v[82:85], v[234:237]\n"
"s_waitcnt lgkmcnt(2)\n"
"ds_read_b128 v[158:161], v2 offset:14336\n"
"buffer_load_dwordx4 v0, s[20:23], s60 offen offset:1024 sc1 lds\n"
"v_mfma_f32_16x16x32_bf16 v[222:225], v[170:173], v[78:81], v[222:225]\n"
"v_mfma_f32_16x16x32_bf16 v[238:241], v[170:173], v[82:85], v[238:241]\n"
"s_waitcnt lgkmcnt(2)\n"
"ds_read_b128 v[162:165], v2 offset:15360\n"
"v_mfma_f32_16x16x32_bf16 v[218:221], v[150:153], v[86:89], v[218:221]\n"
"v_mfma_f32_16x16x32_bf16 v[234:237], v[150:153], v[90:93], v[234:237]\n"
"s_waitcnt lgkmcnt(2)\n"
"ds_read_b128 v[166:169], v2 offset:16384\n"
"v_mfma_f32_16x16x32_bf16 v[222:225], v[154:157], v[86:89], v[222:225]\n"
"v_mfma_f32_16x16x32_bf16 v[238:241], v[154:157], v[90:93], v[238:241]\n"
"s_waitcnt lgkmcnt(2)\n"
"ds_read_b128 v[170:173], v2 offset:17408\n"
"v_mfma_f32_16x16x32_bf16 v[218:221], v[158:161], v[94:97], v[218:221]\n"
"v_mfma_f32_16x16x32_bf16 v[234:237], v[158:161], v[98:101], v[234:237]\n"
"s_waitcnt lgkmcnt(2)\n"
"ds_read_b128 v[150:153], v2 offset:18432\n"
"v_mfma_f32_16x16x32_bf16 v[222:225], v[162:165], v[94:97], v[222:225]\n"
"v_mfma_f32_16x16x32_bf16 v[238:241], v[162:165], v[98:101], v[238:241]\n"
"s_waitcnt lgkmcnt(2)\n"
"ds_read_b128 v[154:157], v2 offset:19456\n"
"v_mfma_f32_16x16x32_bf16 v[218:221], v[166:169], v[102:105], v[218:221]\n"
"v_mfma_f32_16x16x32_bf16 v[234:237], v[166:169], v[106:109], v[234:237]\n"
"s_waitcnt lgkmcnt(2)\n"
"ds_read_b128 v[158:161], v2 offset:20480\n"
"v_mfma_f32_16x16x32_bf16 v[222:225], v[170:173], v[102:105], v[222:225]\n"
"v_mfma_f32_16x16x32_bf16 v[238:241], v[170:173], v[106:109], v[238:241]\n"
"s_waitcnt lgkmcnt(2)\n"
"ds_read_b128 v[162:165], v2 offset:21504\n"
"v_mfma_f32_16x16x32_bf16 v[218:221], v[150:153], v[110:113], v[218:221]\n"
"v_mfma_f32_16x16x32_bf16 v[234:237], v[150:153], v[114:117], v[234:237]\n"
"s_waitcnt lgkmcnt(2)\n"
"ds_read_b128 v[166:169], v2 offset:22528\n"
"v_mfma_f32_16x16x32_bf16 v[222:225], v[154:157], v[110:113], v[222:225]\n"
"v_mfma_f32_16x16x32_bf16 v[238:241], v[154:157], v[114:117], v[238:241]\n"
"s_waitcnt lgkmcnt(2)\n"
"ds_read_b128 v[170:173], v2 offset:23552\n"
"v_mfma_f32_16x16x32_bf16 v[218:221], v[158:161], v[118:121], v[218:221]\n"
"v_mfma_f32_16x16x32_bf16 v[234:237], v[158:161], v[122:125], v[234:237]\n"
"s_waitcnt lgkmcnt(2)\n"
"ds_read_b128 v[150:153], v2 offset:24576\n"
"buffer_load_dword v244, off, s[20:23], s68 offset:256 sc1\n"
"v_mfma_f32_16x16x32_bf16 v[222:225], v[162:165], v[118:121], v[222:225]\n"
"v_mfma_f32_16x16x32_bf16 v[238:241], v[162:165], v[122:125], v[238:241]\n"
"s_waitcnt lgkmcnt(2)\n"
"ds_read_b128 v[154:157], v2 offset:25600\n"
"v_mfma_f32_16x16x32_bf16 v[218:221], v[166:169], v[126:129], v[218:221]\n"
"v_mfma_f32_16x16x32_bf16 v[234:237], v[166:169], v[130:133], v[234:237]\n"
"s_waitcnt lgkmcnt(2)\n"
"ds_read_b128 v[158:161], v2 offset:26624\n"
"v_mfma_f32_16x16x32_bf16 v[222:225], v[170:173], v[126:129], v[222:225]\n"
"v_mfma_f32_16x16x32_bf16 v[238:241], v[170:173], v[130:133], v[238:241]\n"
"s_waitcnt lgkmcnt(2)\n"
"ds_read_b128 v[162:165], v2 offset:27648\n"
"v_mfma_f32_16x16x32_bf16 v[218:221], v[150:153], v[134:137], v[218:221]\n"
"v_mfma_f32_16x16x32_bf16 v[234:237], v[150:153], v[138:141], v[234:237]\n"
"s_waitcnt lgkmcnt(2)\n"
"v_mfma_f32_16x16x32_bf16 v[222:225], v[154:157], v[134:137], v[222:225]\n"
"v_mfma_f32_16x16x32_bf16 v[238:241], v[154:157], v[138:141], v[238:241]\n"
"s_waitcnt lgkmcnt(1)\n"
"v_mfma_f32_16x16x32_bf16 v[218:221], v[158:161], v[142:145], v[218:221]\n"
"v_mfma_f32_16x16x32_bf16 v[234:237], v[158:161], v[146:149], v[234:237]\n"
"s_waitcnt lgkmcnt(0)\n"
"v_mfma_f32_16x16x32_bf16 v[222:225], v[162:165], v[142:145], v[222:225]\n"
"v_mfma_f32_16x16x32_bf16 v[238:241], v[162:165], v[146:149], v[238:241]\n"
"s_nop 3\n"
"v_pk_fma_f32 v[218:219], v[218:219], v[250:251], v[206:207] op_sel_hi:[1,0,1]\n"
"v_pk_fma_f32 v[220:221], v[220:221], v[250:251], v[208:209] op_sel_hi:[1,0,1]\n"
"s_nop 0\n"
"v_pk_fma_f32 v[222:223], v[222:223], v[250:251], v[210:211] op_sel_hi:[1,0,1]\n"
"v_pk_fma_f32 v[224:225], v[224:225], v[250:251], v[212:213] op_sel_hi:[1,0,1]\n"
"v_cvt_pk_bf16_f32 v38, v218, v219\n"
"v_cvt_pk_bf16_f32 v39, v220, v221\n"
"v_cvt_pk_bf16_f32 v40, v222, v223\n"
"v_cvt_pk_bf16_f32 v41, v224, v225\n"
"v_pk_fma_f32 v[234:235], v[234:235], v[250:251], v[226:227] op_sel:[0,1,0]\n"
"v_pk_fma_f32 v[236:237], v[236:237], v[250:251], v[228:229] op_sel:[0,1,0]\n"
"v_pk_fma_f32 v[238:239], v[238:239], v[250:251], v[230:231] op_sel:[0,1,0]\n"
"v_pk_fma_f32 v[240:241], v[240:241], v[250:251], v[232:233] op_sel:[0,1,0]\n"
"v_cvt_pk_bf16_f32 v42, v234, v235\n"
"v_cvt_pk_bf16_f32 v43, v236, v237\n"
"v_cvt_pk_bf16_f32 v44, v238, v239\n"
"v_cvt_pk_bf16_f32 v45, v240, v241\n"
"; A2 layer 1\n"
"s_lshl_b32 s48, s96, 12\n"
"v_add_u32 v79, s48, v0\n"
"s_mov_b32 s63, m0\n"
"s_lshl_b32 m0, s96, 13\n"
"s_mov_b32 s55, 0\n"
"s_waitcnt vmcnt(0)\n"
".Lkpoll_53:\n"
"v_readfirstlane_b32 s48, v244\n"
"s_add_u32 s55, s55, 1\n"
"s_cmp_ge_u32 s48, s46\n"
"s_cbranch_scc1 .Lkdone_54\n"
"buffer_load_dword v244, off, s[20:23], s68 offset:256 sc1\n"
"s_cmp_lt_u32 s55, 0x40000\n"
"s_waitcnt vmcnt(0)\n"
"s_cbranch_scc1 .Lkpoll_53\n"
"s_mov_b32 s48, 0x40000\n"
"buffer_store_dword v10, off, s[20:23], s48 sc1\n"
".Lkdone_54:\n"
"buffer_load_dwordx4 v253, s[20:23], s44 offen sc1 lds\n"
"buffer_load_dwordx4 v253, s[20:23], s44 offen offset:1024 sc1 lds\n"
"buffer_load_dwordx4 v253, s[20:23], s44 offen offset:2048 sc1 lds\n"
"buffer_load_dwordx4 v253, s[20:23], s44 offen offset:3072 sc1 lds\n"
"s_add_u32 m0, m0, 0x1000\n"
"s_nop 0\n"
"buffer_load_dwordx4 v253, s[20:23], s45 offen sc1 lds\n"
"buffer_load_dwordx4 v253, s[20:23], s45 offen offset:1024 sc1 lds\n"
"buffer_load_dwordx4 v253, s[20:23], s45 offen offset:2048 sc1 lds\n"
"buffer_load_dwordx4 v253, s[20:23], s45 offen offset:3072 sc1 lds\n"
"s_mov_b32 m0, s63\n"
"s_waitcnt vmcnt(0)\n"
"s_waitcnt lgkmcnt(0)\n"
"s_barrier\n"
"ds_read_b128 v[80:83], v79\n"
"ds_read_b128 v[84:87], v79 offset:1024\n"
"ds_read_b128 v[88:91], v79 offset:4096\n"
"ds_read_b128 v[92:95], v79 offset:5120\n"
"ds_read_b128 v[96:99], v79 offset:2048\n"
"ds_read_b128 v[100:103], v79 offset:3072\n"
"v_mov_b32 v148, 0\n"
"v_mov_b32 v149, 0\n"
"s_waitcnt lgkmcnt(5)\n"
"v_mfma_f32_16x16x32_bf16 v[112:115], v[80:83], v[38:41], v[16:19]\n"
"s_waitcnt lgkmcnt(4)\n"
"v_mfma_f32_16x16x32_bf16 v[116:119], v[84:87], v[38:41], 0\n"
"v_mfma_f32_16x16x32_bf16 v[124:127], v[84:87], v[42:45], v[16:19]\n"
"s_waitcnt lgkmcnt(3)\n"
"v_mfma_f32_16x16x32_bf16 v[128:131], v[88:91], v[38:41], 0\n"
"ds_read_b128 v[80:83], v79 offset:8192\n"
"s_nop 1\n"
"v_exp_f32 v112, v112\n"
"v_exp_f32 v113, v113\n"
"ds_read_b128 v[84:87], v79 offset:9216\n"
"buffer_load_dwordx4 v0, s[20:23], s60 offen offset:2048 sc1 lds\n"
"v_exp_f32 v114, v114\n"
"v_exp_f32 v115, v115\n"
"ds_read_b128 v[104:107], v79 offset:6144\n"
"v_exp_f32 v116, v116\n"
"v_exp_f32 v117, v117\n"
"s_waitcnt lgkmcnt(5)\n"
"v_mfma_f32_16x16x32_bf16 v[132:135], v[92:95], v[38:41], 0\n"
"ds_read_b128 v[108:111], v79 offset:7168\n"
"v_exp_f32 v118, v118\n"
"v_exp_f32 v119, v119\n"
"v_cvt_pk_bf16_f32 v144, v112, v113\n"
"v_cvt_pk_bf16_f32 v145, v114, v115\n"
"v_mfma_f32_16x16x32_bf16 v[136:139], v[88:91], v[42:45], 0\n"
"buffer_load_dwordx4 v0, s[20:23], s60 offen offset:3072 sc1 lds\n"
"v_cvt_pk_bf16_f32 v146, v116, v117\n"
"v_cvt_pk_bf16_f32 v147, v118, v119\n"
"v_mfma_f32_16x16x32_bf16 v[140:143], v[92:95], v[42:45], 0\n"
"s_waitcnt lgkmcnt(5)\n"
"v_mfma_f32_16x16x32_bf16 v[152:155], v[96:99], v[144:147], 0\n"
"s_waitcnt lgkmcnt(4)\n"
"v_exp_f32 v124, v124\n"
"v_exp_f32 v125, v125\n"
"v_mfma_f32_16x16x32_bf16 v[156:159], v[100:103], v[144:147], 0\n"
"s_waitcnt lgkmcnt(3)\n"
"s_add_u32 m0, m0, 0x1000\n"
"s_add_u32 s60, s60, 0x1000\n"
"buffer_load_dwordx4 v0, s[20:23], s60 offen sc1 lds\n"
"v_exp_f32 v126, v126\n"
"v_exp_f32 v127, v127\n"
"v_cvt_pk_bf16_f32 v150, v124, v125\n"
"v_cvt_pk_bf16_f32 v151, v126, v127\n"
"v_mfma_f32_16x16x32_bf16 v[112:115], v[80:83], v[38:41], 0\n"
"ds_read_b128 v[88:91], v79 offset:12288\n"
"v_exp_f32 v128, v128\n"
"v_exp_f32 v129, v129\n"
"v_mfma_f32_16x16x32_bf16 v[160:163], v[96:99], v[148:151], 0\n"
"ds_read_b128 v[92:95], v79 offset:13312\n"
"v_exp_f32 v130, v130\n"
"v_exp_f32 v131, v131\n"
"v_mfma_f32_16x16x32_bf16 v[164:167], v[100:103], v[148:151], 0\n"
"s_waitcnt lgkmcnt(4)\n"
"ds_read_b128 v[96:99], v79 offset:10240\n"
"buffer_load_dwordx4 v0, s[20:23], s60 offen offset:1024 sc1 lds\n"
"v_exp_f32 v132, v132\n"
"v_exp_f32 v133, v133\n"
"v_mfma_f32_16x16x32_bf16 v[116:119], v[84:87], v[38:41], 0\n"
"ds_read_b128 v[100:103], v79 offset:11264\n"
"v_exp_f32 v134, v134\n"
"v_exp_f32 v135, v135\n"
"v_cvt_pk_bf16_f32 v144, v128, v129\n"
"v_cvt_pk_bf16_f32 v145, v130, v131\n"
"v_mfma_f32_16x16x32_bf16 v[120:123], v[80:83], v[42:45], 0\n"
"v_cvt_pk_bf16_f32 v146, v132, v133\n"
"v_cvt_pk_bf16_f32 v147, v134, v135\n"
"v_exp_f32 v136, v136\n"
"v_exp_f32 v137, v137\n"
"v_mfma_f32_16x16x32_bf16 v[124:127], v[84:87], v[42:45], 0\n"
"s_waitcnt lgkmcnt(5)\n"
"buffer_load_dwordx4 v0, s[20:23], s60 offen offset:2048 sc1 lds\n"
"v_exp_f32 v138, v138\n"
"v_exp_f32 v139, v139\n"
"v_mfma_f32_16x16x32_bf16 v[152:155], v[104:107], v[144:147], v[152:155]\n"
"s_waitcnt lgkmcnt(4)\n"
"v_exp_f32 v140, v140\n"
"v_exp_f32 v141, v141\n"
"v_cvt_pk_bf16_f32 v148, v136, v137\n"
"v_cvt_pk_bf16_f32 v149, v138, v139\n"
"v_mfma_f32_16x16x32_bf16 v[156:159], v[108:111], v[144:147], v[156:159]\n"
"s_waitcnt lgkmcnt(3)\n"
"v_exp_f32 v142, v142\n"
"v_exp_f32 v143, v143\n"
"v_cvt_pk_bf16_f32 v150, v140, v141\n"
"v_cvt_pk_bf16_f32 v151, v142, v143\n"
"v_mfma_f32_16x16x32_bf16 v[128:131], v[88:91], v[38:41], 0\n"
"ds_read_b128 v[80:83], v79 offset:16384\n"
"buffer_load_dwordx4 v0, s[20:23], s60 offen offset:3072 sc1 lds\n"
"v_exp_f32 v112, v112\n"
"v_exp_f32 v113, v113\n"
"v_mfma_f32_16x16x32_bf16 v[160:163], v[104:107], v[148:151], v[160:163]\n"
"ds_read_b128 v[84:87], v79 offset:17408\n"
"v_exp_f32 v114, v114\n"
"v_exp_f32 v115, v115\n"
"v_mfma_f32_16x16x32_bf16 v[164:167], v[108:111], v[148:151], v[164:167]\n"
"s_waitcnt lgkmcnt(4)\n"
"ds_read_b128 v[104:107], v79 offset:14336\n"
"v_exp_f32 v116, v116\n"
"v_exp_f32 v117, v117\n"
"v_mfma_f32_16x16x32_bf16 v[132:135], v[92:95], v[38:41], 0\n"
"ds_read_b128 v[108:111], v79 offset:15360\n"
"s_add_u32 m0, m0, 0x1000\n"
"s_add_u32 s60, s60, 0x1000\n"
"buffer_load_dwordx4 v0, s[20:23], s60 offen sc1 lds\n"
"v_exp_f32 v118, v118\n"
"v_exp_f32 v119, v119\n"
"v_cvt_pk_bf16_f32 v144, v112, v113\n"
"v_cvt_pk_bf16_f32 v145, v114, v115\n"
"v_mfma_f32_16x16x32_bf16 v[136:139], v[88:91], v[42:45], 0\n"
"v_cvt_pk_bf16_f32 v146, v116, v117\n"
"v_cvt_pk_bf16_f32 v147, v118, v119\n"
"v_exp_f32 v120, v120\n"
"v_exp_f32 v121, v121\n"
"v_mfma_f32_16x16x32_bf16 v[140:143], v[92:95], v[42:45], 0\n"
"s_waitcnt lgkmcnt(5)\n"
"v_exp_f32 v122, v122\n"
"v_exp_f32 v123, v123\n"
"v_mfma_f32_16x16x32_bf16 v[152:155], v[96:99], v[144:147], v[152:155]\n"
"s_waitcnt lgkmcnt(4)\n"
"buffer_load_dwordx4 v0, s[20:23], s60 offen offset:1024 sc1 lds\n"
"v_exp_f32 v124, v124\n"
"v_exp_f32 v125, v125\n"
"v_cvt_pk_bf16_f32 v148, v120, v121\n"
"v_cvt_pk_bf16_f32 v149, v122, v123\n"
"v_mfma_f32_16x16x32_bf16 v[156:159], v[100:103], v[144:147], v[156:159]\n"
"s_waitcnt lgkmcnt(3)\n"
"v_exp_f32 v126, v126\n"
"v_exp_f32 v127, v127\n"
"v_cvt_pk_bf16_f32 v150, v124, v125\n"
"v_cvt_pk_bf16_f32 v151, v126, v127\n"
"v_mfma_f32_16x16x32_bf16 v[112:115], v[80:83], v[38:41], 0\n"
"ds_read_b128 v[88:91], v79 offset:20480\n"
"v_exp_f32 v128, v128\n"
"v_exp_f32 v129, v129\n"
"v_mfma_f32_16x16x32_bf16 v[160:163], v[96:99], v[148:151], v[160:163]\n"
"ds_read_b128 v[92:95], v79 offset:21504\n"
"s_mul_i32 s63, s67, 6\n"
"s_add_u32 m0, s63, 0xc000\n"
"s_add_u32 s60, s63, s65\n"
"s_add_u32 s60, s60, 0xc000\n"
"buffer_load_dwordx4 v0, s[20:23], s60 offen sc1 lds\n"
"v_exp_f32 v130, v130\n"
"v_exp_f32 v131, v131\n"
"v_mfma_f32_16x16x32_bf16 v[164:167], v[100:103], v[148:151], v[164:167]\n"
"s_waitcnt lgkmcnt(4)\n"
"ds_read_b128 v[96:99], v79 offset:18432\n"
"v_exp_f32 v132, v132\n"
"v_exp_f32 v133, v133\n"
"v_mfma_f32_16x16x32_bf16 v[116:119], v[84:87], v[38:41], 0\n"
"ds_read_b128 v[100:103], v79 offset:19456\n"
"v_exp_f32 v134, v134\n"
"v_exp_f32 v135, v135\n"
"v_cvt_pk_bf16_f32 v144, v128, v129\n"
"v_cvt_pk_bf16_f32 v145, v130, v131\n"
"v_mfma_f32_16x16x32_bf16 v[120:123], v[80:83], v[42:45], 0\n"
"buffer_load_dwordx4 v0, s[20:23], s60 offen offset:1024 sc1 lds\n"
"v_cvt_pk_bf16_f32 v146, v132, v133\n"
"v_cvt_pk_bf16_f32 v147, v134, v135\n"
"v_exp_f32 v136, v136\n"
"v_exp_f32 v137, v137\n"
"v_mfma_f32_16x16x32_bf16 v[124:127], v[84:87], v[42:45], 0\n"
"s_waitcnt lgkmcnt(5)\n"
"v_exp_f32 v138, v138\n"
"v_exp_f32 v139, v139\n"
"v_mfma_f32_16x16x32_bf16 v[152:155], v[104:107], v[144:147], v[152:155]\n"
"s_waitcnt lgkmcnt(4)\n"
"v_exp_f32 v140, v140\n"
"v_exp_f32 v141, v141\n"
"v_cvt_pk_bf16_f32 v148, v136, v137\n"
"v_cvt_pk_bf16_f32 v149, v138, v139\n"
"v_mfma_f32_16x16x32_bf16 v[156:159], v[108:111], v[144:147], v[156:159]\n"
"s_waitcnt lgkmcnt(3)\n"
"buffer_load_dwordx4 v0, s[20:23], s60 offen offset:2048 sc1 lds\n"
"v_exp_f32 v142, v142\n"
"v_exp_f32 v143, v143\n"
"v_cvt_pk_bf16_f32 v150, v140, v141\n"
"v_cvt_pk_bf16_f32 v151, v142, v143\n"
"v_mfma_f32_16x16x32_bf16 v[128:131], v[88:91], v[38:41], 0\n"
"ds_read_b128 v[80:83], v79 offset:24576\n"
"v_exp_f32 v112, v112\n"
"v_exp_f32 v113, v113\n"
"v_mfma_f32_16x16x32_bf16 v[160:163], v[104:107], v[148:151], v[160:163]\n"
"ds_read_b128 v[84:87], v79 offset:25600\n"
"v_exp_f32 v114, v114\n"
"v_exp_f32 v115, v115\n"
"v_mfma_f32_16x16x32_bf16 v[164:167], v[108:111], v[148:151], v[164:167]\n"
"s_waitcnt lgkmcnt(4)\n"
"ds_read_b128 v[104:107], v79 offset:22528\n"
"v_exp_f32 v116, v116\n"
"v_exp_f32 v117, v117\n"
"v_mfma_f32_16x16x32_bf16 v[132:135], v[92:95], v[38:41], 0\n"
"ds_read_b128 v[108:111], v79 offset:23552\n"
"buffer_load_dwordx4 v0, s[20:23], s60 offen offset:3072 sc1 lds\n"
"v_exp_f32 v118, v118\n"
"v_exp_f32 v119, v119\n"
"v_cvt_pk_bf16_f32 v144, v112, v113\n"
"v_cvt_pk_bf16_f32 v145, v114, v115\n"
"v_mfma_f32_16x16x32_bf16 v[136:139], v[88:91], v[42:45], 0\n"
"v_cvt_pk_bf16_f32 v146, v116, v117\n"
"v_cvt_pk_bf16_f32 v147, v118, v119\n"
"v_exp_f32 v120, v120\n"
"v_exp_f32 v121, v121\n"
"v_mfma_f32_16x16x32_bf16 v[140:143], v[92:95], v[42:45], 0\n"
"s_waitcnt lgkmcnt(5)\n"
"v_exp_f32 v122, v122\n"
"v_exp_f32 v123, v123\n"
"v_mfma_f32_16x16x32_bf16 v[152:155], v[96:99], v[144:147], v[152:155]\n"
"s_waitcnt lgkmcnt(4)\n"
"s_add_u32 m0, m0, 0x1000\n"
"s_add_u32 s60, s60, 0x1000\n"
"buffer_load_dwordx4 v0, s[20:23], s60 offen sc1 lds\n"
"v_exp_f32 v124, v124\n"
"v_exp_f32 v125, v125\n"
"v_cvt_pk_bf16_f32 v148, v120, v121\n"
"v_cvt_pk_bf16_f32 v149, v122, v123\n"
"v_mfma_f32_16x16x32_bf16 v[156:159], v[100:103], v[144:147], v[156:159]\n"
"s_waitcnt lgkmcnt(3)\n"
"v_exp_f32 v126, v126\n"
"v_exp_f32 v127, v127\n"
"v_cvt_pk_bf16_f32 v150, v124, v125\n"
"v_cvt_pk_bf16_f32 v151, v126, v127\n"
"v_mfma_f32_16x16x32_bf16 v[112:115], v[80:83], v[38:41], 0\n"
"ds_read_b128 v[88:91], v79 offset:28672\n"
"v_exp_f32 v128, v128\n"
"v_exp_f32 v129, v129\n"
"v_mfma_f32_16x16x32_bf16 v[160:163], v[96:99], v[148:151], v[160:163]\n"
"ds_read_b128 v[92:95], v79 offset:29696\n"
"buffer_load_dwordx4 v0, s[20:23], s60 offen offset:1024 sc1 lds\n"
"v_exp_f32 v130, v130\n"
"v_exp_f32 v131, v131\n"
"v_mfma_f32_16x16x32_bf16 v[164:167], v[100:103], v[148:151], v[164:167]\n"
"s_waitcnt lgkmcnt(4)\n"
"ds_read_b128 v[96:99], v79 offset:26624\n"
"v_exp_f32 v132, v132\n"
"v_exp_f32 v133, v133\n"
"v_mfma_f32_16x16x32_bf16 v[116:119], v[84:87], v[38:41], 0\n"
"ds_read_b128 v[100:103], v79 offset:27648\n"
"v_exp_f32 v134, v134\n"
"v_exp_f32 v135, v135\n"
"v_cvt_pk_bf16_f32 v144, v128, v129\n"
"v_cvt_pk_bf16_f32 v145, v130, v131\n"
"v_mfma_f32_16x16x32_bf16 v[120:123], v[80:83], v[42:45], 0\n"
"s_mul_i32 s63, s67, 7\n"
"s_min_u32 s63, s63, 0x4c00\n"
"s_add_u32 m0, s63, 0x20000\n"
"s_add_u32 s60, s63, s65\n"
"s_add_u32 s60, s60, 0x12000\n"
"buffer_load_dwordx4 v0, s[20:23], s60 offen sc1 lds\n"
"v_cvt_pk_bf16_f32 v146, v132, v133\n"
"v_cvt_pk_bf16_f32 v147, v134, v135\n"
"v_exp_f32 v136, v136\n"
"v_exp_f32 v137, v137\n"
"v_mfma_f32_16x16x32_bf16 v[124:127], v[84:87], v[42:45], 0\n"
"s_waitcnt lgkmcnt(5)\n"
"v_exp_f32 v138, v138\n"
"v_exp_f32 v139, v139\n"
"v_mfma_f32_16x16x32_bf16 v[152:155], v[104:107], v[144:147], v[152:155]\n"
"s_waitcnt lgkmcnt(4)\n"
"v_exp_f32 v140, v140\n"
"v_exp_f32 v141, v141\n"
"v_cvt_pk_bf16_f32 v148, v136, v137\n"
"v_cvt_pk_bf16_f32 v149, v138, v139\n"
"v_mfma_f32_16x16x32_bf16 v[156:159], v[108:111], v[144:147], v[156:159]\n"
"s_waitcnt lgkmcnt(3)\n"
"buffer_load_dwordx4 v0, s[20:23], s60 offen offset:1024 sc1 lds\n"
"v_exp_f32 v142, v142\n"
"v_exp_f32 v143, v143\n"
"v_cvt_pk_bf16_f32 v150, v140, v141\n"
"v_cvt_pk_bf16_f32 v151, v142, v143\n"
"v_mfma_f32_16x16x32_bf16 v[128:131], v[88:91], v[38:41], 0\n"
"ds_read_b128 v[80:83], v79 offset:32768\n"
"v_exp_f32 v112, v112\n"
"v_exp_f32 v113, v113\n"
"v_mfma_f32_16x16x32_bf16 v[160:163], v[104:107], v[148:151], v[160:163]\n"
"ds_read_b128 v[84:87], v79 offset:33792\n"
"v_exp_f32 v114, v114\n"
"v_exp_f32 v115, v115\n"
"v_mfma_f32_16x16x32_bf16 v[164:167], v[108:111], v[148:151], v[164:167]\n"
"s_waitcnt lgkmcnt(4)\n"
"ds_read_b128 v[104:107], v79 offset:30720\n"
"buffer_load_dwordx4 v0, s[20:23], s60 offen offset:2048 sc1 lds\n"
"v_exp_f32 v116, v116\n"
"v_exp_f32 v117, v117\n"
"v_mfma_f32_16x16x32_bf16 v[132:135], v[92:95], v[38:41], 0\n"
"ds_read_b128 v[108:111], v79 offset:31744\n"
"v_exp_f32 v118, v118\n"
"v_exp_f32 v119, v119\n"
"v_cvt_pk_bf16_f32 v144, v112, v113\n"
"v_cvt_pk_bf16_f32 v145, v114, v115\n"
"v_mfma_f32_16x16x32_bf16 v[136:139], v[88:91], v[42:45], 0\n"
"v_cvt_pk_bf16_f32 v146, v116, v117\n"
"v_cvt_pk_bf16_f32 v147, v118, v119\n"
"v_exp_f32 v120, v120\n"
"v_exp_f32 v121, v121\n"
"v_mfma_f32_16x16x32_bf16 v[140:143], v[92:95], v[42:45], 0\n"
"s_waitcnt lgkmcnt(5)\n"
"buffer_load_dwordx4 v0, s[20:23], s60 offen offset:3072 sc1 lds\n"
"v_exp_f32 v122, v122\n"
"v_exp_f32 v123, v123\n"
"v_mfma_f32_16x16x32_bf16 v[152:155], v[96:99], v[144:147], v[152:155]\n"
"s_waitcnt lgkmcnt(4)\n"
"v_exp_f32 v124, v124\n"
"v_exp_f32 v125, v125\n"
"v_cvt_pk_bf16_f32 v148, v120, v121\n"
"v_cvt_pk_bf16_f32 v149, v122, v123\n"
"v_mfma_f32_16x16x32_bf16 v[156:159], v[100:103], v[144:147], v[156:159]\n"
"s_waitcnt lgkmcnt(3)\n"
"v_exp_f32 v126, v126\n"
"v_exp_f32 v127, v127\n"
"v_cvt_pk_bf16_f32 v150, v124, v125\n"
"v_cvt_pk_bf16_f32 v151, v126, v127\n"
"v_mfma_f32_16x16x32_bf16 v[112:115], v[80:83], v[38:41], v[12:15]\n"
"s_add_u32 m0, m0, 0x1000\n"
"s_add_u32 s60, s60, 0x1000\n"
"buffer_load_dwordx4 v0, s[20:23], s60 offen sc1 lds\n"
"v_exp_f32 v128, v128\n"
"v_exp_f32 v129, v129\n"
"v_mfma_f32_16x16x32_bf16 v[160:163], v[96:99], v[148:151], v[160:163]\n"
"v_exp_f32 v130, v130\n"
"v_exp_f32 v131, v131\n"
"v_mfma_f32_16x16x32_bf16 v[164:167], v[100:103], v[148:151], v[164:167]\n"
"ds_read_b128 v[96:99], v79 offset:34816\n"
"v_exp_f32 v132, v132\n"
"v_exp_f32 v133, v133\n"
"ds_read_b128 v[100:103], v79 offset:35840\n"
"buffer_load_dwordx4 v0, s[20:23], s60 offen offset:1024 sc1 lds\n"
"v_exp_f32 v134, v134\n"
"v_exp_f32 v135, v135\n"
"v_cvt_pk_bf16_f32 v144, v128, v129\n"
"v_cvt_pk_bf16_f32 v145, v130, v131\n"
"v_mfma_f32_16x16x32_bf16 v[120:123], v[80:83], v[42:45], 0\n"
"s_waitcnt lgkmcnt(4)\n"
"v_cvt_pk_bf16_f32 v146, v132, v133\n"
"v_cvt_pk_bf16_f32 v147, v134, v135\n"
"v_exp_f32 v136, v136\n"
"v_exp_f32 v137, v137\n"
"v_mfma_f32_16x16x32_bf16 v[124:127], v[84:87], v[42:45], v[12:15]\n"
"s_waitcnt lgkmcnt(3)\n"
"v_exp_f32 v138, v138\n"
"v_exp_f32 v139, v139\n"
"v_mfma_f32_16x16x32_bf16 v[152:155], v[104:107], v[144:147], v[152:155]\n"
"s_waitcnt lgkmcnt(2)\n"
"buffer_load_dwordx4 v0, s[20:23], s60 offen offset:2048 sc1 lds\n"
"v_exp_f32 v140, v140\n"
"v_exp_f32 v141, v141\n"
"v_cvt_pk_bf16_f32 v148, v136, v137\n"
"v_cvt_pk_bf16_f32 v149, v138, v139\n"
"v_mfma_f32_16x16x32_bf16 v[156:159], v[108:111], v[144:147], v[156:159]\n"
"v_exp_f32 v142, v142\n"
"v_exp_f32 v143, v143\n"
"v_cvt_pk_bf16_f32 v150, v140, v141\n"
"v_cvt_pk_bf16_f32 v151, v142, v143\n"
"v_exp_f32 v112, v112\n"
"v_exp_f32 v113, v113\n"
"v_mfma_f32_16x16x32_bf16 v[160:163], v[104:107], v[148:151], v[160:163]\n"
"v_exp_f32 v114, v114\n"
"v_exp_f32 v115, v115\n"
"v_mfma_f32_16x16x32_bf16 v[164:167], v[108:111], v[148:151], v[164:167]\n"
"s_mov_b32 s44, 0x1691000\n"
"s_mul_i32 s51, s97, 0x20000\n"
"s_add_u32 s44, s44, s51\n"
"s_mul_i32 s51, s96, 0x8000\n"
"s_add_u32 s44, s44, s51\n"
"s_mul_i32 s51, s34, 0x400\n"
"s_add_u32 s44, s44, s51\n"
"s_add_u32 s45, s44, 0x4000\n"
"v_cvt_pk_bf16_f32 v144, v112, v113\n"
"v_cvt_pk_bf16_f32 v145, v114, v115\n"
"v_mov_b32 v146, 0\n"
"v_mov_b32 v147, 0\n"
"v_exp_f32 v120, v120\n"
"v_exp_f32 v121, v121\n"
"v_exp_f32 v122, v122\n"
"v_exp_f32 v123, v123\n"
"s_waitcnt lgkmcnt(1)\n"
"v_mfma_f32_16x16x32_bf16 v[152:155], v[96:99], v[144:147], v[152:155]\n"
"s_waitcnt lgkmcnt(0)\n"
"v_exp_f32 v124, v124\n"
"v_exp_f32 v125, v125\n"
"v_cvt_pk_bf16_f32 v148, v120, v121\n"
"v_cvt_pk_bf16_f32 v149, v122, v123\n"
"v_mfma_f32_16x16x32_bf16 v[156:159], v[100:103], v[144:147], v[156:159]\n"
"v_exp_f32 v126, v126\n"
"v_exp_f32 v127, v127\n"
"v_cvt_pk_bf16_f32 v150, v124, v125\n"
"v_cvt_pk_bf16_f32 v151, v126, v127\n"
"s_nop 1\n"
"v_mfma_f32_16x16x32_bf16 v[160:163], v[96:99], v[148:151], v[160:163]\n"
"v_mfma_f32_16x16x32_bf16 v[164:167], v[100:103], v[148:151], v[164:167]\n"
"v_mov_b32 v114, v156\n"
"s_nop 6\n"
"v_mov_b32 v115, v164\n"
"s_nop 1\n"
"v_permlane16_swap_b32 v114, v115\n"
"v_mov_b32 v112, v115\n"
"s_nop 1\n"
"v_permlane32_swap_b32 v112, v115\n"
"v_add_u32 v114, 0xde800000, v115\n"
"v_cmp_lt_u32 vcc, 0x3c000000, v114\n"
"s_nop 0\n"
"s_cbranch_vccnz .Lkfallback_56\n"
".Lknorm_55:\n"
"v_rcp_f32 v112, v115\n"
"s_nop 0\n"
"v_mov_b32 v113, v112\n"
"s_nop 1\n"
"v_permlane16_swap_b32 v112, v113\n"
"v_pk_mul_f32 v[152:153], v[152:153], v[112:113] op_sel_hi:[1,0]\n"
"v_pk_mul_f32 v[154:155], v[154:155], v[112:113] op_sel_hi:[1,0]\n"
"v_pk_mul_f32 v[156:157], v[156:157], v[112:113] op_sel_hi:[1,0]\n"
"v_pk_mul_f32 v[158:159], v[158:159], v[112:113] op_sel_hi:[1,0]\n"
"v_cvt_pk_bf16_f32 v144, v152, v153\n"
"v_cvt_pk_bf16_f32 v145, v154, v155\n"
"v_cvt_pk_bf16_f32 v146, v156, v157\n"
"v_cvt_pk_bf16_f32 v147, v158, v159\n"
"buffer_store_dwordx4 v[144:147], v0, s[20:23], s44 offen sc1\n"
"v_pk_mul_f32 v[160:161], v[160:161], v[112:113] op_sel:[0,1]\n"
"v_pk_mul_f32 v[162:163], v[162:163], v[112:113] op_sel:[0,1]\n"
"v_pk_mul_f32 v[164:165], v[164:165], v[112:113] op_sel:[0,1]\n"
"v_pk_mul_f32 v[166:167], v[166:167], v[112:113] op_sel:[0,1]\n"
"v_cvt_pk_bf16_f32 v148, v160, v161\n"
"v_cvt_pk_bf16_f32 v149, v162, v163\n"
"v_cvt_pk_bf16_f32 v150, v164, v165\n"
"v_cvt_pk_bf16_f32 v151, v166, v167\n"
"buffer_store_dwordx4 v[148:151], v0, s[20:23], s45 offen sc1\n"
"s_waitcnt lgkmcnt(0)\n"
"s_barrier\n"
"s_mul_i32 s63, s67, 12\n"
"s_add_u32 m0, s63, 0\n"
"s_add_u32 s60, s63, s65\n"
"buffer_load_dwordx4 v0, s[20:23], s60 offen sc1 lds\n"
"buffer_load_dwordx4 v0, s[20:23], s60 offen offset:1024 sc1 lds\n"
"buffer_load_dwordx4 v0, s[20:23], s60 offen offset:2048 sc1 lds\n"
"buffer_load_dwordx4 v0, s[20:23], s60 offen offset:3072 sc1 lds\n"
"s_add_u32 m0, m0, 0x1000\n"
"s_add_u32 s60, s60, 0x1000\n"
"buffer_load_dwordx4 v0, s[20:23], s60 offen sc1 lds\n"
"buffer_load_dwordx4 v0, s[20:23], s60 offen offset:1024 sc1 lds\n"
"s_mov_b32 s44, 0x1691000\n"
"s_mul_i32 s51, s37, 0x4000\n"
"s_add_u32 s44, s44, s51\n"
"s_waitcnt vmcnt(6)\n"
"s_waitcnt lgkmcnt(0)\n"
"s_barrier\n"
"s_cmp_lg_u32 s96, 0\n"
"s_cbranch_scc1 .Lkw0_61\n"
"s_mov_b64 exec, 0xffff\n"
"buffer_atomic_add v10, v11, s[20:23], s52 offen\n"
"s_mov_b64 exec, -1\n"
".Lkw0_61:\n"
"ds_read_b128 v[102:105], v1 offset:8192\n"
"ds_read_b128 v[106:109], v1 offset:9216\n"
"ds_read_b128 v[110:113], v1 offset:10240\n"
"ds_read_b128 v[114:117], v1 offset:57344\n"
"ds_read_b128 v[118:121], v1 offset:11264\n"
"ds_read_b128 v[122:125], v1 offset:12288\n"
"ds_read_b128 v[126:129], v1 offset:13312\n"
"ds_read_b128 v[130:133], v1 offset:14336\n"
"; B layer 1\n"
"buffer_load_dword v190, off, s[20:23], s53 offset:0 sc1\n"
"s_mov_b32 s55, 0\n"
"s_waitcnt vmcnt(0)\n"
".Lkpoll_62:\n"
"v_readfirstlane_b32 s48, v190\n"
"s_add_u32 s55, s55, 1\n"
"s_cmp_ge_u32 s48, 0x50\n"
"s_cbranch_scc1 .Lkdone_63\n"
"buffer_load_dword v190, off, s[20:23], s53 offset:0 sc1\n"
"s_cmp_lt_u32 s55, 0x40000\n"
"s_waitcnt vmcnt(0)\n"
"s_cbranch_scc1 .Lkpoll_62\n"
"s_mov_b32 s48, 0x40000\n"
"buffer_store_dword v10, off, s[20:23], s48 sc1\n"
".Lkdone_63:\n"
"buffer_load_dwordx4 v[38:41], v0, s[20:23], s44 offen sc1\n"
"buffer_load_dwordx4 v[42:45], v0, s[20:23], s44 offen offset:1024 sc1\n"
"buffer_load_dwordx4 v[46:49], v0, s[20:23], s44 offen offset:2048 sc1\n"
"buffer_load_dwordx4 v[50:53], v0, s[20:23], s44 offen offset:3072 sc1\n"
"s_add_u32 s44, s44, 0x1000\n"
"buffer_load_dwordx4 v[54:57], v0, s[20:23], s44 offen sc1\n"
"buffer_load_dwordx4 v[58:61], v0, s[20:23], s44 offen offset:1024 sc1\n"
"buffer_load_dwordx4 v[62:65], v0, s[20:23], s44 offen offset:2048 sc1\n"
"buffer_load_dwordx4 v[66:69], v0, s[20:23], s44 offen offset:3072 sc1\n"
"s_waitcnt vmcnt(7) lgkmcnt(7)\n"
"v_mfma_f32_16x16x32_bf16 v[142:145], v[102:105], v[38:41], 0\n"
"s_waitcnt lgkmcnt(6)\n"
"ds_read_b128 v[134:137], v1 offset:15360\n"
"s_add_u32 s44, s44, 0x1000\n"
"buffer_load_dwordx4 v[70:73], v0, s[20:23], s44 offen sc1\n"
"v_mfma_f32_16x16x32_bf16 v[146:149], v[106:109], v[38:41], 0\n"
"s_waitcnt lgkmcnt(6)\n"
"ds_read_b128 v[138:141], v1 offset:16384\n"
"v_mfma_f32_16x16x32_bf16 v[150:153], v[110:113], v[38:41], 0\n"
"s_waitcnt lgkmcnt(6)\n"
"ds_read_b128 v[102:105], v1 offset:58368\n"
"v_mfma_f32_16x16x32_bf16 v[154:157], v[114:117], v[38:41], 0\n"
"s_waitcnt vmcnt(7) lgkmcnt(6)\n"
"ds_read_b128 v[106:109], v1 offset:17408\n"
"buffer_load_dwordx4 v[74:77], v0, s[20:23], s44 offen offset:1024 sc1\n"
"v_mfma_f32_16x16x32_bf16 v[142:145], v[118:121], v[42:45], v[142:145]\n"
"v_mfma_f32_16x16x32_bf16 v[158:161], v[114:117], v[42:45], 0\n"
"s_waitcnt lgkmcnt(6)\n"
"ds_read_b128 v[110:113], v1 offset:18432\n"
"v_mfma_f32_16x16x32_bf16 v[146:149], v[122:125], v[42:45], v[146:149]\n"
"s_waitcnt lgkmcnt(6)\n"
"ds_read_b128 v[114:117], v1 offset:19456\n"
"v_mfma_f32_16x16x32_bf16 v[150:153], v[126:129], v[42:45], v[150:153]\n"
"s_waitcnt vmcnt(7) lgkmcnt(6)\n"
"ds_read_b128 v[118:121], v1 offset:20480\n"
"v_mfma_f32_16x16x32_bf16 v[142:145], v[130:133], v[46:49], v[142:145]\n"
"s_waitcnt lgkmcnt(6)\n"
"ds_read_b128 v[122:125], v1 offset:21504\n"
"buffer_load_dwordx4 v[78:81], v0, s[20:23], s44 offen offset:2048 sc1\n"
"v_mfma_f32_16x16x32_bf16 v[146:149], v[134:137], v[46:49], v[146:149]\n"
"s_waitcnt lgkmcnt(6)\n"
"ds_read_b128 v[126:129], v1 offset:22528\n"
"v_mfma_f32_16x16x32_bf16 v[150:153], v[138:141], v[46:49], v[150:153]\n"
"s_waitcnt lgkmcnt(6)\n"
"ds_read_b128 v[130:133], v1 offset:59392\n"
"v_mfma_f32_16x16x32_bf16 v[154:157], v[102:105], v[46:49], v[154:157]\n"
"s_waitcnt vmcnt(7) lgkmcnt(6)\n"
"ds_read_b128 v[134:137], v1 offset:23552\n"
"buffer_load_dwordx4 v[82:85], v0, s[20:23], s44 offen offset:3072 sc1\n"
"v_mfma_f32_16x16x32_bf16 v[142:145], v[106:109], v[50:53], v[142:145]\n"
"v_mfma_f32_16x16x32_bf16 v[158:161], v[102:105], v[50:53], v[158:161]\n"
"s_waitcnt lgkmcnt(6)\n"
"ds_read_b128 v[138:141], v1 offset:24576\n"
"v_mfma_f32_16x16x32_bf16 v[146:149], v[110:113], v[50:53], v[146:149]\n"
"s_waitcnt lgkmcnt(6)\n"
"ds_read_b128 v[102:105], v1 offset:25600\n"
"v_mfma_f32_16x16x32_bf16 v[150:153], v[114:117], v[50:53], v[150:153]\n"
"s_waitcnt vmcnt(7) lgkmcnt(6)\n"
"ds_read_b128 v[106:109], v1 offset:26624\n"
"v_mfma_f32_16x16x32_bf16 v[142:145], v[118:121], v[54:57], v[142:145]\n"
"s_waitcnt lgkmcnt(6)\n"
"ds_read_b128 v[110:113], v1 offset:27648\n"
"s_add_u32 s44, s44, 0x1000\n"
"buffer_load_dwordx4 v[86:89], v0, s[20:23], s44 offen sc1\n"
"v_mfma_f32_16x16x32_bf16 v[146:149], v[122:125], v[54:57], v[146:149]\n"
"s_waitcnt lgkmcnt(6)\n"
"ds_read_b128 v[114:117], v1 offset:28672\n"
"v_mfma_f32_16x16x32_bf16 v[150:153], v[126:129], v[54:57], v[150:153]\n"
"s_waitcnt lgkmcnt(6)\n"
"ds_read_b128 v[118:121], v1 offset:60416\n"
"v_mfma_f32_16x16x32_bf16 v[154:157], v[130:133], v[54:57], v[154:157]\n"
"s_waitcnt vmcnt(7) lgkmcnt(6)\n"
"ds_read_b128 v[122:125], v1 offset:29696\n"
"buffer_load_dwordx4 v[90:93], v0, s[20:23], s44 offen offset:1024 sc1\n"
"v_mfma_f32_16x16x32_bf16 v[142:145], v[134:137], v[58:61], v[142:145]\n"
"v_mfma_f32_16x16x32_bf16 v[158:161], v[130:133], v[58:61], v[158:161]\n"
"s_waitcnt lgkmcnt(6)\n"
"ds_read_b128 v[126:129], v1 offset:30720\n"
"v_mfma_f32_16x16x32_bf16 v[146:149], v[138:141], v[58:61], v[146:149]\n"
"s_waitcnt lgkmcnt(6)\n"
"ds_read_b128 v[130:133], v1 offset:31744\n"
"v_mfma_f32_16x16x32_bf16 v[150:153], v[102:105], v[58:61], v[150:153]\n"
"s_waitcnt vmcnt(7) lgkmcnt(6)\n"
"ds_read_b128 v[134:137], v1 offset:32768\n"
"v_mfma_f32_16x16x32_bf16 v[142:145], v[106:109], v[62:65], v[142:145]\n"
"s_waitcnt lgkmcnt(6)\n"
"ds_read_b128 v[138:141], v1 offset:33792\n"
"buffer_load_dwordx4 v[94:97], v0, s[20:23], s44 offen offset:2048 sc1\n"
"v_mfma_f32_16x16x32_bf16 v[146:149], v[110:113], v[62:65], v[146:149]\n"
"s_waitcnt lgkmcnt(6)\n"
"ds_read_b128 v[102:105], v1 offset:34816\n"
"v_mfma_f32_16x16x32_bf16 v[150:153], v[114:117], v[62:65], v[150:153]\n"
"s_waitcnt lgkmcnt(6)\n"
"ds_read_b128 v[106:109], v1 offset:61440\n"
"v_mfma_f32_16x16x32_bf16 v[154:157], v[118:121], v[62:65], v[154:157]\n"
"s_waitcnt vmcnt(7) lgkmcnt(6)\n"
"ds_read_b128 v[110:113], v1 offset:35840\n"
"buffer_load_dwordx4 v[98:101], v0, s[20:23], s44 offen offset:3072 sc1\n"
"v_mfma_f32_16x16x32_bf16 v[142:145], v[122:125], v[66:69], v[142:145]\n"
"v_mfma_f32_16x16x32_bf16 v[158:161], v[118:121], v[66:69], v[158:161]\n"
"s_waitcnt lgkmcnt(6)\n"
"ds_read_b128 v[114:117], v1 offset:36864\n"
"v_mfma_f32_16x16x32_bf16 v[146:149], v[126:129], v[66:69], v[146:149]\n"
"s_waitcnt lgkmcnt(6)\n"
"ds_read_b128 v[118:121], v1 offset:37888\n"
"v_mfma_f32_16x16x32_bf16 v[150:153], v[130:133], v[66:69], v[150:153]\n"
"s_waitcnt vmcnt(7) lgkmcnt(6)\n"
"ds_read_b128 v[122:125], v1 offset:38912\n"
"v_mfma_f32_16x16x32_bf16 v[142:145], v[134:137], v[70:73], v[142:145]\n"
"s_waitcnt lgkmcnt(6)\n"
"ds_read_b128 v[126:129], v1 offset:39936\n"
"v_mfma_f32_16x16x32_bf16 v[146:149], v[138:141], v[70:73], v[146:149]\n"
"s_waitcnt lgkmcnt(6)\n"
"ds_read_b128 v[130:133], v1 offset:40960\n"
"v_mfma_f32_16x16x32_bf16 v[150:153], v[102:105], v[70:73], v[150:153]\n"
"s_waitcnt lgkmcnt(6)\n"
"ds_read_b128 v[134:137], v1 offset:62464\n"
"v_mfma_f32_16x16x32_bf16 v[154:157], v[106:109], v[70:73], v[154:157]\n"
"s_waitcnt vmcnt(6) lgkmcnt(6)\n"
"ds_read_b128 v[138:141], v1 offset:41984\n"
"v_mfma_f32_16x16x32_bf16 v[142:145], v[110:113], v[74:77], v[142:145]\n"
"v_mfma_f32_16x16x32_bf16 v[158:161], v[106:109], v[74:77], v[158:161]\n"
"s_waitcnt lgkmcnt(6)\n"
"ds_read_b128 v[102:105], v1 offset:43008\n"
"v_mfma_f32_16x16x32_bf16 v[146:149], v[114:117], v[74:77], v[146:149]\n"
"s_waitcnt lgkmcnt(6)\n"
"ds_read_b128 v[106:109], v1 offset:44032\n"
"v_mfma_f32_16x16x32_bf16 v[150:153], v[118:121], v[74:77], v[150:153]\n"
"s_waitcnt vmcnt(5) lgkmcnt(6)\n"
"ds_read_b128 v[110:113], v1 offset:45056\n"
"v_mfma_f32_16x16x32_bf16 v[142:145], v[122:125], v[78:81], v[142:145]\n"
"s_waitcnt lgkmcnt(6)\n"
"ds_read_b128 v[114:117], v1 offset:46080\n"
"v_mfma_f32_16x16x32_bf16 v[146:149], v[126:129], v[78:81], v[146:149]\n"
"s_waitcnt lgkmcnt(6)\n"
"ds_read_b128 v[118:121], v1 offset:47104\n"
"v_mfma_f32_16x16x32_bf16 v[150:153], v[130:133], v[78:81], v[150:153]\n"
"s_waitcnt lgkmcnt(6)\n"
"ds_read_b128 v[122:125], v1 offset:63488\n"
"v_mfma_f32_16x16x32_bf16 v[154:157], v[134:137], v[78:81], v[154:157]\n"
"s_waitcnt vmcnt(4) lgkmcnt(6)\n"
"ds_read_b128 v[126:129], v1 offset:48128\n"
"v_mfma_f32_16x16x32_bf16 v[142:145], v[138:141], v[82:85], v[142:145]\n"
"v_mfma_f32_16x16x32_bf16 v[158:161], v[134:137], v[82:85], v[158:161]\n"
"s_waitcnt lgkmcnt(6)\n"
"ds_read_b128 v[130:133], v1 offset:49152\n"
"v_mfma_f32_16x16x32_bf16 v[146:149], v[102:105], v[82:85], v[146:149]\n"
"s_waitcnt lgkmcnt(6)\n"
"ds_read_b128 v[134:137], v1 offset:50176\n"
"v_mfma_f32_16x16x32_bf16 v[150:153], v[106:109], v[82:85], v[150:153]\n"
"s_waitcnt vmcnt(3) lgkmcnt(6)\n"
"ds_read_b128 v[138:141], v1 offset:51200\n"
"v_mfma_f32_16x16x32_bf16 v[142:145], v[110:113], v[86:89], v[142:145]\n"
"s_waitcnt lgkmcnt(6)\n"
"ds_read_b128 v[102:105], v1 offset:52224\n"
"v_mfma_f32_16x16x32_bf16 v[146:149], v[114:117], v[86:89], v[146:149]\n"
"s_waitcnt lgkmcnt(6)\n"
"ds_read_b128 v[106:109], v1 offset:53248\n"
"v_mfma_f32_16x16x32_bf16 v[150:153], v[118:121], v[86:89], v[150:153]\n"
"s_waitcnt lgkmcnt(6)\n"
"ds_read_b128 v[110:113], v1 offset:64512\n"
"v_mfma_f32_16x16x32_bf16 v[154:157], v[122:125], v[86:89], v[154:157]\n"
"s_waitcnt vmcnt(2) lgkmcnt(6)\n"
"ds_read_b128 v[114:117], v1 offset:54272\n"
"v_mfma_f32_16x16x32_bf16 v[142:145], v[126:129], v[90:93], v[142:145]\n"
"v_mfma_f32_16x16x32_bf16 v[158:161], v[122:125], v[90:93], v[158:161]\n"
"s_waitcnt lgkmcnt(6)\n"
"ds_read_b128 v[118:121], v1 offset:55296\n"
"v_mfma_f32_16x16x32_bf16 v[146:149], v[130:133], v[90:93], v[146:149]\n"
"s_waitcnt lgkmcnt(6)\n"
"ds_read_b128 v[122:125], v1 offset:56320\n"
"v_mfma_f32_16x16x32_bf16 v[150:153], v[134:137], v[90:93], v[150:153]\n"
"s_waitcnt vmcnt(1) lgkmcnt(6)\n"
"v_mfma_f32_16x16x32_bf16 v[142:145], v[138:141], v[94:97], v[142:145]\n"
"s_waitcnt lgkmcnt(5)\n"
"v_mfma_f32_16x16x32_bf16 v[146:149], v[102:105], v[94:97], v[146:149]\n"
"s_waitcnt lgkmcnt(4)\n"
"s_mov_b32 s45, 0xab1000\n"
"s_mul_i32 s51, s37, 0x3c00\n"
"s_add_u32 s45, s45, s51\n"
"v_mfma_f32_16x16x32_bf16 v[150:153], v[106:109], v[94:97], v[150:153]\n"
"s_waitcnt lgkmcnt(3)\n"
"s_add_u32 s46, s45, s69\n"
"v_mfma_f32_16x16x32_bf16 v[154:157], v[110:113], v[94:97], v[154:157]\n"
"s_waitcnt vmcnt(0) lgkmcnt(2)\n"
"s_mul_i32 s48, s35, 0x700\n"
"s_add_u32 s45, s45, s48\n"
"v_mfma_f32_16x16x32_bf16 v[142:145], v[114:117], v[98:101], v[142:145]\n"
"v_mfma_f32_16x16x32_bf16 v[158:161], v[110:113], v[98:101], v[158:161]\n"
"s_waitcnt lgkmcnt(1)\n"
"v_mfma_f32_16x16x32_bf16 v[146:149], v[118:121], v[98:101], v[146:149]\n"
"s_waitcnt lgkmcnt(0)\n"
"v_mfma_f32_16x16x32_bf16 v[150:153], v[122:125], v[98:101], v[150:153]\n"
"s_nop 3\n"
"v_cndmask_b32_e64 v154, v158, v154, s[38:39]\n"
"v_cndmask_b32_e64 v155, v159, v155, s[38:39]\n"
"v_cndmask_b32_e64 v156, v160, v156, s[38:39]\n"
"v_cndmask_b32_e64 v157, v161, v157, s[38:39]\n"
"v_pk_add_f32 v[20:21], v[20:21], v[142:143]\n"
"v_pk_add_f32 v[22:23], v[22:23], v[144:145]\n"
"v_pk_add_f32 v[24:25], v[24:25], v[146:147]\n"
"v_pk_add_f32 v[26:27], v[26:27], v[148:149]\n"
"v_pk_add_f32 v[28:29], v[28:29], v[150:151]\n"
"v_pk_add_f32 v[30:31], v[30:31], v[152:153]\n"
"v_mov_b32 v158, v154\n"
"v_mov_b32 v159, v155\n"
"v_mov_b32 v160, v156\n"
"v_mov_b32 v161, v157\n"
"v_permlane32_swap_b32 v154, v158\n"
"v_permlane32_swap_b32 v155, v159\n"
"v_permlane32_swap_b32 v156, v160\n"
"v_permlane32_swap_b32 v157, v161\n"
"v_pk_add_f32 v[32:33], v[32:33], v[154:155]\n"
"v_pk_add_f32 v[34:35], v[34:35], v[156:157]\n"
"v_pk_add_f32 v[32:33], v[32:33], v[158:159]\n"
"v_pk_add_f32 v[34:35], v[34:35], v[160:161]\n"
"v_cvt_pk_bf16_f32 v38, v20, v21\n"
"v_cvt_pk_bf16_f32 v39, v22, v23\n"
"v_cvt_pk_bf16_f32 v40, v24, v25\n"
"v_cvt_pk_bf16_f32 v41, v26, v27\n"
"buffer_store_dwordx4 v[38:41], v0, s[20:23], s45 offen sc1\n"
"v_cvt_pk_bf16_f32 v42, v28, v29\n"
"v_cvt_pk_bf16_f32 v43, v30, v31\n"
"v_cvt_pk_bf16_f32 v44, v32, v33\n"
"v_cvt_pk_bf16_f32 v45, v34, v35\n"
"v_mov_b32 v47, v42\n"
"v_mov_b32 v49, v43\n"
"s_nop 0\n"
"v_permlane16_swap_b32 v47, v46\n"
"v_permlane16_swap_b32 v49, v48\n"
"v_cndmask_b32_e64 v44, v46, v44, s[38:39]\n"
"v_cndmask_b32_e64 v45, v48, v45, s[38:39]\n"
"s_mov_b64 exec, s[40:41]\n"
"buffer_store_dwordx4 v[42:45], v0, s[20:23], s45 offen offset:1024 sc1\n"
"s_mov_b64 exec, -1\n"
"v_pk_add_f32 v[50:51], v[20:21], v[22:23]\n"
"v_pk_mul_f32 v[52:53], v[20:21], v[20:21]\n"
"v_pk_mul_f32 v[54:55], v[32:33], 0.5 op_sel_hi:[1,0]\n"
"v_pk_mul_f32 v[56:57], v[34:35], 0.5 op_sel_hi:[1,0]\n"
"v_pk_fma_f32 v[52:53], v[22:23], v[22:23], v[52:53]\n"
"v_pk_add_f32 v[50:51], v[50:51], v[24:25]\n"
"v_pk_fma_f32 v[52:53], v[24:25], v[24:25], v[52:53]\n"
"v_pk_add_f32 v[50:51], v[50:51], v[26:27]\n"
"v_pk_fma_f32 v[52:53], v[26:27], v[26:27], v[52:53]\n"
"v_pk_add_f32 v[50:51], v[50:51], v[28:29]\n"
"v_pk_fma_f32 v[52:53], v[28:29], v[28:29], v[52:53]\n"
"v_pk_add_f32 v[50:51], v[50:51], v[30:31]\n"
"v_pk_fma_f32 v[52:53], v[30:31], v[30:31], v[52:53]\n"
"v_pk_add_f32 v[50:51], v[50:51], v[54:55]\n"
"v_pk_fma_f32 v[52:53], v[54:55], v[32:33], v[52:53]\n"
"v_pk_add_f32 v[50:51], v[50:51], v[56:57]\n"
"v_pk_fma_f32 v[52:53], v[56:57], v[34:35], v[52:53]\n"
"v_add_f32 v50, v50, v51\n"
"v_add_f32 v51, v52, v53\n"
"s_nop 1\n"
"v_permlane16_swap_b32 v50, v51\n"
"v_add_f32 v50, v50, v51\n"
"v_mov_b32 v51, v50\n"
"s_nop 1\n"
"v_permlane32_swap_b32 v50, v51\n"
"v_add_f32 v50, v50, v51\n"
"s_mov_b64 exec, s[38:39]\n"
"buffer_store_dword v50, v36, s[20:23], s46 offen sc1\n"
"s_mov_b64 exec, -1\n"
"s_barrier\n"
"buffer_load_dwordx4 v0, s[20:23], s60 offen offset:2048 sc1 lds\n"
"buffer_load_dwordx4 v0, s[20:23], s60 offen offset:3072 sc1 lds\n"
"s_add_u32 m0, m0, 0x1000\n"
"s_add_u32 s60, s60, 0x1000\n"
"buffer_load_dwordx4 v0, s[20:23], s60 offen sc1 lds\n"
"buffer_load_dwordx4 v0, s[20:23], s60 offen offset:1024 sc1 lds\n"
"buffer_load_dwordx4 v0, s[20:23], s60 offen offset:2048 sc1 lds\n"
"buffer_load_dwordx4 v0, s[20:23], s60 offen offset:3072 sc1 lds\n"
"s_mov_b32 s44, 0xab1000\n"
"s_mul_i32 s51, s97, 0x1e000\n"
"s_add_u32 s44, s44, s51\n"
"s_mul_i32 s51, s96, 0x7800\n"
"s_add_u32 s44, s44, s51\n"
"s_add_u32 s45, s44, 0x3c00\n"
"s_add_u32 s49, s44, 0x3800\n"
"s_add_u32 s50, s45, 0x3800\n"
"s_mov_b32 s46, 0xab4800\n"
"s_mul_i32 s51, s37, 0x3c00\n"
"s_add_u32 s46, s46, s51\n"
"s_waitcnt vmcnt(6)\n"
"s_waitcnt lgkmcnt(0)\n"
"s_barrier\n"
"s_cmp_lg_u32 s96, 0\n"
"s_cbranch_scc1 .Lkw0_64\n"
"s_mov_b64 exec, 0xffff\n"
"buffer_atomic_add v10, v11, s[20:23], s52 offen\n"
"s_mov_b64 exec, -1\n"
".Lkw0_64:\n"
"s_waitcnt vmcnt(0)\n"
"s_waitcnt lgkmcnt(0)\n"
"s_barrier\n"
"ds_read_b128 v[194:197], v4 offset:768\n"
"ds_read_b128 v[198:201], v4 offset:832\n"
"ds_read_b128 v[202:205], v4 offset:896\n"
"ds_read_b128 v[206:209], v0\n"
"ds_read_b128 v[210:213], v0 offset:1024\n"
"ds_read_b128 v[214:217], v0 offset:2048\n"
"ds_read_b128 v[218:221], v0 offset:3072\n"
"; C layer 1\n"
"buffer_load_dword v230, off, s[20:23], s53 offset:0 sc1\n"
"s_mov_b32 s55, 0\n"
"s_waitcnt vmcnt(0)\n"
".Lkpoll_65:\n"
"v_readfirstlane_b32 s48, v230\n"
"s_add_u32 s55, s55, 1\n"
"s_cmp_ge_u32 s48, 0x60\n"
"s_cbranch_scc1 .Lkdone_66\n"
"buffer_load_dword v230, off, s[20:23], s53 offset:0 sc1\n"
"s_cmp_lt_u32 s55, 0x40000\n"
"s_waitcnt vmcnt(0)\n"
"s_cbranch_scc1 .Lkpoll_65\n"
"s_mov_b32 s48, 0x40000\n"
"buffer_store_dword v10, off, s[20:23], s48 sc1\n"
".Lkdone_66:\n"
"buffer_load_dwordx4 v[38:41], v0, s[20:23], s44 offen sc1\n"
"buffer_load_dwordx4 v[42:45], v0, s[20:23], s45 offen sc1\n"
"buffer_load_dwordx4 v[230:233], v0, s[20:23], s49 offen sc1\n"
"buffer_load_dwordx4 v[234:237], v0, s[20:23], s50 offen sc1\n"
"buffer_load_dwordx4 v[46:49], v0, s[20:23], s44 offen offset:1024 sc1\n"
"buffer_load_dwordx4 v[50:53], v0, s[20:23], s45 offen offset:1024 sc1\n"
"buffer_load_dwordx4 v[54:57], v0, s[20:23], s44 offen offset:2048 sc1\n"
"buffer_load_dwordx4 v[58:61], v0, s[20:23], s45 offen offset:2048 sc1\n"
"buffer_load_dwordx4 v[62:65], v0, s[20:23], s44 offen offset:3072 sc1\n"
"buffer_load_dwordx4 v[66:69], v0, s[20:23], s45 offen offset:3072 sc1\n"
"s_add_u32 s44, s44, 0x1000\n"
"buffer_load_dwordx4 v[70:73], v0, s[20:23], s44 offen sc1\n"
"s_add_u32 s45, s45, 0x1000\n"
"buffer_load_dwordx4 v[74:77], v0, s[20:23], s45 offen sc1\n"
"buffer_load_dwordx4 v[78:81], v0, s[20:23], s44 offen offset:1024 sc1\n"
"buffer_load_dwordx4 v[82:85], v0, s[20:23], s45 offen offset:1024 sc1\n"
"s_waitcnt vmcnt(13) lgkmcnt(3)\n"
"v_mfma_f32_16x16x32_bf16 v[150:153], v[206:209], v[38:41], 0\n"
"s_waitcnt vmcnt(12)\n"
"v_mfma_f32_16x16x32_bf16 v[178:181], v[206:209], v[42:45], 0\n"
"s_waitcnt lgkmcnt(2)\n"
"ds_read_b128 v[222:225], v0 offset:7168\n"
"v_mfma_f32_16x16x32_bf16 v[154:157], v[210:213], v[38:41], 0\n"
"v_mfma_f32_16x16x32_bf16 v[182:185], v[210:213], v[42:45], 0\n"
"s_waitcnt lgkmcnt(2)\n"
"ds_read_b128 v[226:229], v0 offset:8192\n"
"buffer_load_dwordx4 v[86:89], v0, s[20:23], s44 offen offset:2048 sc1\n"
"v_mfma_f32_16x16x32_bf16 v[158:161], v[214:217], v[38:41], 0\n"
"v_mfma_f32_16x16x32_bf16 v[186:189], v[214:217], v[42:45], 0\n"
"s_waitcnt lgkmcnt(2)\n"
"ds_read_b128 v[206:209], v0 offset:9216\n"
"v_mfma_f32_16x16x32_bf16 v[162:165], v[218:221], v[38:41], 0\n"
"v_mfma_f32_16x16x32_bf16 v[190:193], v[218:221], v[42:45], 0\n"
"s_waitcnt vmcnt(10) lgkmcnt(2)\n"
"ds_read_b128 v[210:213], v0 offset:10240\n"
"buffer_load_dwordx4 v[90:93], v0, s[20:23], s45 offen offset:2048 sc1\n"
"v_mfma_f32_16x16x32_bf16 v[150:153], v[222:225], v[46:49], v[150:153]\n"
"s_waitcnt vmcnt(10)\n"
"v_mfma_f32_16x16x32_bf16 v[178:181], v[222:225], v[50:53], v[178:181]\n"
"s_waitcnt lgkmcnt(2)\n"
"ds_read_b128 v[214:217], v0 offset:14336\n"
"v_add_f32 v230, v230, v232\n"
"v_mfma_f32_16x16x32_bf16 v[154:157], v[226:229], v[46:49], v[154:157]\n"
"v_mfma_f32_16x16x32_bf16 v[182:185], v[226:229], v[50:53], v[182:185]\n"
"s_waitcnt lgkmcnt(2)\n"
"ds_read_b128 v[218:221], v0 offset:15360\n"
"buffer_load_dwordx4 v[94:97], v0, s[20:23], s44 offen offset:3072 sc1\n"
"v_add_f32 v234, v234, v236\n"
"v_mfma_f32_16x16x32_bf16 v[158:161], v[206:209], v[46:49], v[158:161]\n"
"v_mfma_f32_16x16x32_bf16 v[186:189], v[206:209], v[50:53], v[186:189]\n"
"s_waitcnt lgkmcnt(2)\n"
"ds_read_b128 v[222:225], v0 offset:16384\n"
"v_permlane16_swap_b32 v230, v234\n"
"v_mfma_f32_16x16x32_bf16 v[162:165], v[210:213], v[46:49], v[162:165]\n"
"v_mfma_f32_16x16x32_bf16 v[190:193], v[210:213], v[50:53], v[190:193]\n"
"s_waitcnt vmcnt(10) lgkmcnt(2)\n"
"ds_read_b128 v[226:229], v0 offset:17408\n"
"buffer_load_dwordx4 v[98:101], v0, s[20:23], s45 offen offset:3072 sc1\n"
"v_add_f32 v230, v230, v234\n"
"v_mfma_f32_16x16x32_bf16 v[150:153], v[214:217], v[54:57], v[150:153]\n"
"s_waitcnt vmcnt(10)\n"
"v_mfma_f32_16x16x32_bf16 v[178:181], v[214:217], v[58:61], v[178:181]\n"
"s_waitcnt lgkmcnt(2)\n"
"ds_read_b128 v[206:209], v0 offset:21504\n"
"v_mov_b32 v234, v230\n"
"v_mfma_f32_16x16x32_bf16 v[154:157], v[218:221], v[54:57], v[154:157]\n"
"v_mfma_f32_16x16x32_bf16 v[182:185], v[218:221], v[58:61], v[182:185]\n"
"s_waitcnt lgkmcnt(2)\n"
"ds_read_b128 v[210:213], v0 offset:22528\n"
"s_add_u32 s44, s44, 0x1000\n"
"buffer_load_dwordx4 v[102:105], v0, s[20:23], s44 offen sc1\n"
"v_permlane32_swap_b32 v230, v234\n"
"v_mfma_f32_16x16x32_bf16 v[158:161], v[222:225], v[54:57], v[158:161]\n"
"v_mfma_f32_16x16x32_bf16 v[186:189], v[222:225], v[58:61], v[186:189]\n"
"s_waitcnt lgkmcnt(2)\n"
"ds_read_b128 v[214:217], v0 offset:23552\n"
"v_add_f32 v230, v230, v234\n"
"v_mfma_f32_16x16x32_bf16 v[162:165], v[226:229], v[54:57], v[162:165]\n"
"v_mfma_f32_16x16x32_bf16 v[190:193], v[226:229], v[58:61], v[190:193]\n"
"s_waitcnt vmcnt(10) lgkmcnt(2)\n"
"ds_read_b128 v[218:221], v0 offset:24576\n"
"s_add_u32 s45, s45, 0x1000\n"
"buffer_load_dwordx4 v[106:109], v0, s[20:23], s45 offen sc1\n"
"v_mul_f32 v242, 0xbb124925, v230\n"
"v_mfma_f32_16x16x32_bf16 v[150:153], v[206:209], v[62:65], v[150:153]\n"
"s_waitcnt vmcnt(10)\n"
"v_mfma_f32_16x16x32_bf16 v[178:181], v[206:209], v[66:69], v[178:181]\n"
"s_waitcnt lgkmcnt(2)\n"
"ds_read_b128 v[222:225], v0 offset:28672\n"
"v_mov_b32 v243, v242\n"
"v_mfma_f32_16x16x32_bf16 v[154:157], v[210:213], v[62:65], v[154:157]\n"
"v_mfma_f32_16x16x32_bf16 v[182:185], v[210:213], v[66:69], v[182:185]\n"
"s_waitcnt lgkmcnt(2)\n"
"ds_read_b128 v[226:229], v0 offset:29696\n"
"buffer_load_dwordx4 v[110:113], v0, s[20:23], s44 offen offset:1024 sc1\n"
"v_permlane16_swap_b32 v242, v243\n"
"v_mfma_f32_16x16x32_bf16 v[158:161], v[214:217], v[62:65], v[158:161]\n"
"v_mfma_f32_16x16x32_bf16 v[186:189], v[214:217], v[66:69], v[186:189]\n"
"s_waitcnt lgkmcnt(2)\n"
"ds_read_b128 v[206:209], v0 offset:30720\n"
"v_mfma_f32_16x16x32_bf16 v[162:165], v[218:221], v[62:65], v[162:165]\n"
"v_mfma_f32_16x16x32_bf16 v[190:193], v[218:221], v[66:69], v[190:193]\n"
"s_waitcnt vmcnt(10) lgkmcnt(2)\n"
"ds_read_b128 v[210:213], v0 offset:31744\n"
"buffer_load_dwordx4 v[114:117], v0, s[20:23], s45 offen offset:1024 sc1\n"
"v_mfma_f32_16x16x32_bf16 v[150:153], v[222:225], v[70:73], v[150:153]\n"
"s_waitcnt vmcnt(10)\n"
"v_mfma_f32_16x16x32_bf16 v[178:181], v[222:225], v[74:77], v[178:181]\n"
"s_waitcnt lgkmcnt(2)\n"
"ds_read_b128 v[214:217], v0 offset:35840\n"
"v_mfma_f32_16x16x32_bf16 v[154:157], v[226:229], v[70:73], v[154:157]\n"
"v_mfma_f32_16x16x32_bf16 v[182:185], v[226:229], v[74:77], v[182:185]\n"
"s_waitcnt lgkmcnt(2)\n"
"ds_read_b128 v[218:221], v0 offset:36864\n"
"buffer_load_dwordx4 v[118:121], v0, s[20:23], s44 offen offset:2048 sc1\n"
"v_mfma_f32_16x16x32_bf16 v[158:161], v[206:209], v[70:73], v[158:161]\n"
"v_mfma_f32_16x16x32_bf16 v[186:189], v[206:209], v[74:77], v[186:189]\n"
"s_waitcnt lgkmcnt(2)\n"
"ds_read_b128 v[222:225], v0 offset:37888\n"
"v_mfma_f32_16x16x32_bf16 v[162:165], v[210:213], v[70:73], v[162:165]\n"
"v_mfma_f32_16x16x32_bf16 v[190:193], v[210:213], v[74:77], v[190:193]\n"
"s_waitcnt vmcnt(10) lgkmcnt(2)\n"
"ds_read_b128 v[226:229], v0 offset:38912\n"
"buffer_load_dwordx4 v[122:125], v0, s[20:23], s45 offen offset:2048 sc1\n"
"v_mfma_f32_16x16x32_bf16 v[150:153], v[214:217], v[78:81], v[150:153]\n"
"s_waitcnt vmcnt(10)\n"
"v_mfma_f32_16x16x32_bf16 v[178:181], v[214:217], v[82:85], v[178:181]\n"
"s_waitcnt lgkmcnt(2)\n"
"ds_read_b128 v[206:209], v0 offset:43008\n"
"v_mfma_f32_16x16x32_bf16 v[154:157], v[218:221], v[78:81], v[154:157]\n"
"v_mfma_f32_16x16x32_bf16 v[182:185], v[218:221], v[82:85], v[182:185]\n"
"s_waitcnt lgkmcnt(2)\n"
"ds_read_b128 v[210:213], v0 offset:44032\n"
"buffer_load_dwordx4 v[126:129], v0, s[20:23], s44 offen offset:3072 sc1\n"
"v_mfma_f32_16x16x32_bf16 v[158:161], v[222:225], v[78:81], v[158:161]\n"
"v_mfma_f32_16x16x32_bf16 v[186:189], v[222:225], v[82:85], v[186:189]\n"
"s_waitcnt lgkmcnt(2)\n"
"ds_read_b128 v[214:217], v0 offset:45056\n"
"v_mfma_f32_16x16x32_bf16 v[162:165], v[226:229], v[78:81], v[162:165]\n"
"v_mfma_f32_16x16x32_bf16 v[190:193], v[226:229], v[82:85], v[190:193]\n"
"s_waitcnt vmcnt(10) lgkmcnt(2)\n"
"ds_read_b128 v[218:221], v0 offset:46080\n"
"buffer_load_dwordx4 v[130:133], v0, s[20:23], s45 offen offset:3072 sc1\n"
"v_mfma_f32_16x16x32_bf16 v[150:153], v[206:209], v[86:89], v[150:153]\n"
"s_waitcnt vmcnt(10)\n"
"v_mfma_f32_16x16x32_bf16 v[178:181], v[206:209], v[90:93], v[178:181]\n"
"s_waitcnt lgkmcnt(2)\n"
"ds_read_b128 v[222:225], v0 offset:50176\n"
"v_mfma_f32_16x16x32_bf16 v[154:157], v[210:213], v[86:89], v[154:157]\n"
"v_mfma_f32_16x16x32_bf16 v[182:185], v[210:213], v[90:93], v[182:185]\n"
"s_waitcnt lgkmcnt(2)\n"
"ds_read_b128 v[226:229], v0 offset:51200\n"
"s_add_u32 s44, s44, 0x1000\n"
"buffer_load_dwordx4 v[134:137], v0, s[20:23], s44 offen sc1\n"
"v_mfma_f32_16x16x32_bf16 v[158:161], v[214:217], v[86:89], v[158:161]\n"
"v_mfma_f32_16x16x32_bf16 v[186:189], v[214:217], v[90:93], v[186:189]\n"
"s_waitcnt lgkmcnt(2)\n"
"ds_read_b128 v[206:209], v0 offset:52224\n"
"v_mfma_f32_16x16x32_bf16 v[162:165], v[218:221], v[86:89], v[162:165]\n"
"v_mfma_f32_16x16x32_bf16 v[190:193], v[218:221], v[90:93], v[190:193]\n"
"s_waitcnt vmcnt(10) lgkmcnt(2)\n"
"ds_read_b128 v[210:213], v0 offset:53248\n"
"s_add_u32 s45, s45, 0x1000\n"
"buffer_load_dwordx4 v[138:141], v0, s[20:23], s45 offen sc1\n"
"v_mfma_f32_16x16x32_bf16 v[150:153], v[222:225], v[94:97], v[150:153]\n"
"s_waitcnt vmcnt(10)\n"
"v_mfma_f32_16x16x32_bf16 v[178:181], v[222:225], v[98:101], v[178:181]\n"
"s_waitcnt lgkmcnt(2)\n"
"ds_read_b128 v[214:217], v0 offset:57344\n"
"v_mfma_f32_16x16x32_bf16 v[154:157], v[226:229], v[94:97], v[154:157]\n"
"v_mfma_f32_16x16x32_bf16 v[182:185], v[226:229], v[98:101], v[182:185]\n"
"s_waitcnt lgkmcnt(2)\n"
"ds_read_b128 v[218:221], v0 offset:58368\n"
"buffer_load_dwordx4 v[142:145], v0, s[20:23], s44 offen offset:1024 sc1\n"
"v_mfma_f32_16x16x32_bf16 v[158:161], v[206:209], v[94:97], v[158:161]\n"
"v_mfma_f32_16x16x32_bf16 v[186:189], v[206:209], v[98:101], v[186:189]\n"
"s_waitcnt lgkmcnt(2)\n"
"ds_read_b128 v[222:225], v0 offset:59392\n"
"v_mfma_f32_16x16x32_bf16 v[162:165], v[210:213], v[94:97], v[162:165]\n"
"v_mfma_f32_16x16x32_bf16 v[190:193], v[210:213], v[98:101], v[190:193]\n"
"s_waitcnt vmcnt(10) lgkmcnt(2)\n"
"ds_read_b128 v[226:229], v0 offset:60416\n"
"buffer_load_dwordx4 v[146:149], v0, s[20:23], s45 offen offset:1024 sc1\n"
"v_mfma_f32_16x16x32_bf16 v[150:153], v[214:217], v[102:105], v[150:153]\n"
"s_waitcnt vmcnt(10)\n"
"v_mfma_f32_16x16x32_bf16 v[178:181], v[214:217], v[106:109], v[178:181]\n"
"s_waitcnt lgkmcnt(2)\n"
"ds_read_b128 v[206:209], v0 offset:64512\n"
"v_mfma_f32_16x16x32_bf16 v[154:157], v[218:221], v[102:105], v[154:157]\n"
"v_mfma_f32_16x16x32_bf16 v[182:185], v[218:221], v[106:109], v[182:185]\n"
"s_waitcnt lgkmcnt(2)\n"
"ds_read_b128 v[210:213], v1\n"
"buffer_load_dwordx4 v[238:241], v0, s[20:23], s46 offen sc1\n"
"v_mfma_f32_16x16x32_bf16 v[158:161], v[222:225], v[102:105], v[158:161]\n"
"v_mfma_f32_16x16x32_bf16 v[186:189], v[222:225], v[106:109], v[186:189]\n"
"s_waitcnt lgkmcnt(2)\n"
"ds_read_b128 v[214:217], v1 offset:1024\n"
"v_mfma_f32_16x16x32_bf16 v[162:165], v[226:229], v[102:105], v[162:165]\n"
"v_mfma_f32_16x16x32_bf16 v[190:193], v[226:229], v[106:109], v[190:193]\n"
"s_waitcnt vmcnt(10) lgkmcnt(2)\n"
"ds_read_b128 v[218:221], v1 offset:2048\n"
"v_mfma_f32_16x16x32_bf16 v[150:153], v[206:209], v[110:113], v[150:153]\n"
"s_waitcnt vmcnt(9)\n"
"v_mfma_f32_16x16x32_bf16 v[178:181], v[206:209], v[114:117], v[178:181]\n"
"s_waitcnt lgkmcnt(2)\n"
"ds_read_b128 v[222:225], v1 offset:6144\n"
"v_mul_f32 v166, v194, v242\n"
"v_mul_f32 v167, v195, v242\n"
"v_mfma_f32_16x16x32_bf16 v[154:157], v[210:213], v[110:113], v[154:157]\n"
"v_mfma_f32_16x16x32_bf16 v[182:185], v[210:213], v[114:117], v[182:185]\n"
"s_waitcnt lgkmcnt(2)\n"
"ds_read_b128 v[226:229], v1 offset:7168\n"
"v_mul_f32 v168, v196, v242\n"
"v_mul_f32 v169, v197, v242\n"
"v_mfma_f32_16x16x32_bf16 v[158:161], v[214:217], v[110:113], v[158:161]\n"
"v_mfma_f32_16x16x32_bf16 v[186:189], v[214:217], v[114:117], v[186:189]\n"
"s_waitcnt lgkmcnt(2)\n"
"ds_read_b128 v[206:209], v2\n"
"v_mul_f32 v194, v194, v243\n"
"v_mul_f32 v195, v195, v243\n"
"v_mfma_f32_16x16x32_bf16 v[162:165], v[218:221], v[110:113], v[162:165]\n"
"v_mfma_f32_16x16x32_bf16 v[190:193], v[218:221], v[114:117], v[190:193]\n"
"s_waitcnt vmcnt(8) lgkmcnt(2)\n"
"ds_read_b128 v[210:213], v2 offset:1024\n"
"v_mul_f32 v196, v196, v243\n"
"v_mul_f32 v197, v197, v243\n"
"v_mfma_f32_16x16x32_bf16 v[150:153], v[222:225], v[118:121], v[150:153]\n"
"s_waitcnt vmcnt(7)\n"
"v_mfma_f32_16x16x32_bf16 v[178:181], v[222:225], v[122:125], v[178:181]\n"
"s_waitcnt lgkmcnt(2)\n"
"ds_read_b128 v[214:217], v2 offset:5120\n"
"v_mul_f32 v170, v198, v242\n"
"v_mul_f32 v171, v199, v242\n"
"v_mfma_f32_16x16x32_bf16 v[154:157], v[226:229], v[118:121], v[154:157]\n"
"v_mfma_f32_16x16x32_bf16 v[182:185], v[226:229], v[122:125], v[182:185]\n"
"s_waitcnt lgkmcnt(2)\n"
"ds_read_b128 v[218:221], v2 offset:6144\n"
"v_mul_f32 v172, v200, v242\n"
"v_mul_f32 v173, v201, v242\n"
"v_mfma_f32_16x16x32_bf16 v[158:161], v[206:209], v[118:121], v[158:161]\n"
"v_mfma_f32_16x16x32_bf16 v[186:189], v[206:209], v[122:125], v[186:189]\n"
"s_waitcnt lgkmcnt(2)\n"
"ds_read_b128 v[222:225], v2 offset:7168\n"
"v_mul_f32 v198, v198, v243\n"
"v_mul_f32 v199, v199, v243\n"
"v_mfma_f32_16x16x32_bf16 v[162:165], v[210:213], v[118:121], v[162:165]\n"
"v_mfma_f32_16x16x32_bf16 v[190:193], v[210:213], v[122:125], v[190:193]\n"
"s_waitcnt vmcnt(6) lgkmcnt(2)\n"
"ds_read_b128 v[226:229], v2 offset:8192\n"
"v_mul_f32 v200, v200, v243\n"
"v_mul_f32 v201, v201, v243\n"
"v_mfma_f32_16x16x32_bf16 v[150:153], v[214:217], v[126:129], v[150:153]\n"
"s_waitcnt vmcnt(5)\n"
"v_mfma_f32_16x16x32_bf16 v[178:181], v[214:217], v[130:133], v[178:181]\n"
"s_waitcnt lgkmcnt(2)\n"
"ds_read_b128 v[206:209], v2 offset:12288\n"
"v_mul_f32 v174, v202, v242\n"
"v_mul_f32 v175, v203, v242\n"
"v_mfma_f32_16x16x32_bf16 v[154:157], v[218:221], v[126:129], v[154:157]\n"
"v_mfma_f32_16x16x32_bf16 v[182:185], v[218:221], v[130:133], v[182:185]\n"
"s_waitcnt lgkmcnt(2)\n"
"ds_read_b128 v[210:213], v2 offset:13312\n"
"v_mul_f32 v176, v204, v242\n"
"v_mul_f32 v177, v205, v242\n"
"v_mfma_f32_16x16x32_bf16 v[158:161], v[222:225], v[126:129], v[158:161]\n"
"v_mfma_f32_16x16x32_bf16 v[186:189], v[222:225], v[130:133], v[186:189]\n"
"s_waitcnt lgkmcnt(2)\n"
"ds_read_b128 v[214:217], v2 offset:14336\n"
"v_mul_f32 v202, v202, v243\n"
"v_mul_f32 v203, v203, v243\n"
"v_mfma_f32_16x16x32_bf16 v[162:165], v[226:229], v[126:129], v[162:165]\n"
"v_mfma_f32_16x16x32_bf16 v[190:193], v[226:229], v[130:133], v[190:193]\n"
"s_waitcnt vmcnt(4) lgkmcnt(2)\n"
"ds_read_b128 v[218:221], v2 offset:15360\n"
"v_mul_f32 v204, v204, v243\n"
"v_mul_f32 v205, v205, v243\n"
"v_mfma_f32_16x16x32_bf16 v[150:153], v[206:209], v[134:137], v[150:153]\n"
"s_waitcnt vmcnt(3)\n"
"v_mfma_f32_16x16x32_bf16 v[178:181], v[206:209], v[138:141], v[178:181]\n"
"s_waitcnt lgkmcnt(2)\n"
"ds_read_b128 v[222:225], v2 offset:19456\n"
"v_mfma_f32_16x16x32_bf16 v[154:157], v[210:213], v[134:137], v[154:157]\n"
"v_mfma_f32_16x16x32_bf16 v[182:185], v[210:213], v[138:141], v[182:185]\n"
"s_waitcnt lgkmcnt(2)\n"
"ds_read_b128 v[226:229], v2 offset:20480\n"
"v_mfma_f32_16x16x32_bf16 v[158:161], v[214:217], v[134:137], v[158:161]\n"
"v_mfma_f32_16x16x32_bf16 v[186:189], v[214:217], v[138:141], v[186:189]\n"
"s_waitcnt lgkmcnt(2)\n"
"ds_read_b128 v[206:209], v2 offset:21504\n"
"v_mfma_f32_16x16x32_bf16 v[162:165], v[218:221], v[134:137], v[162:165]\n"
"v_mfma_f32_16x16x32_bf16 v[190:193], v[218:221], v[138:141], v[190:193]\n"
"s_waitcnt vmcnt(2) lgkmcnt(2)\n"
"ds_read_b128 v[210:213], v2 offset:22528\n"
"v_mfma_f32_16x16x32_bf16 v[150:153], v[222:225], v[142:145], v[150:153]\n"
"s_waitcnt vmcnt(1)\n"
"v_mfma_f32_16x16x32_bf16 v[178:181], v[222:225], v[146:149], v[178:181]\n"
"ds_read_b128 v[214:217], v0 offset:4096\n"
"s_mov_b32 s44, 0x1f91000\n"
"s_mul_i32 s51, s97, 0x70000\n"
"s_add_u32 s44, s44, s51\n"
"s_mul_i32 s51, s96, 0x1c000\n"
"s_add_u32 s44, s44, s51\n"
"s_mul_i32 s51, s34, 0xe00\n"
"s_add_u32 s44, s44, s51\n"
"s_waitcnt lgkmcnt(3)\n"
"v_mfma_f32_16x16x32_bf16 v[154:157], v[226:229], v[142:145], v[154:157]\n"
"v_mfma_f32_16x16x32_bf16 v[182:185], v[226:229], v[146:149], v[182:185]\n"
"s_waitcnt lgkmcnt(2)\n"
"ds_read_b128 v[218:221], v0 offset:5120\n"
"s_add_u32 s45, s44, 0xe000\n"
"v_mfma_f32_16x16x32_bf16 v[158:161], v[206:209], v[142:145], v[158:161]\n"
"v_mfma_f32_16x16x32_bf16 v[186:189], v[206:209], v[146:149], v[186:189]\n"
"s_waitcnt lgkmcnt(2)\n"
"ds_read_b128 v[222:225], v0 offset:6144\n"
"v_mfma_f32_16x16x32_bf16 v[162:165], v[210:213], v[142:145], v[162:165]\n"
"v_mfma_f32_16x16x32_bf16 v[190:193], v[210:213], v[146:149], v[190:193]\n"
"s_waitcnt lgkmcnt(2)\n"
"ds_read_b128 v[226:229], v0 offset:11264\n"
"v_mfma_f32_16x16x32_bf16 v[166:169], v[214:217], v[38:41], v[166:169]\n"
"v_mfma_f32_16x16x32_bf16 v[194:197], v[214:217], v[42:45], v[194:197]\n"
"s_waitcnt lgkmcnt(2)\n"
"ds_read_b128 v[206:209], v0 offset:12288\n"
"v_mfma_f32_16x16x32_bf16 v[170:173], v[218:221], v[38:41], v[170:173]\n"
"v_mfma_f32_16x16x32_bf16 v[198:201], v[218:221], v[42:45], v[198:201]\n"
"s_waitcnt lgkmcnt(2)\n"
"ds_read_b128 v[210:213], v0 offset:13312\n"
"ds_read_b128 v[230:233], v4 offset:512\n"
"ds_read_b128 v[234:237], v4 offset:576\n"
"v_mfma_f32_16x16x32_bf16 v[174:177], v[222:225], v[38:41], v[174:177]\n"
"v_mfma_f32_16x16x32_bf16 v[202:205], v[222:225], v[42:45], v[202:205]\n"
"s_waitcnt lgkmcnt(4)\n"
"ds_read_b128 v[214:217], v0 offset:18432\n"
"v_mfma_f32_16x16x32_bf16 v[166:169], v[226:229], v[46:49], v[166:169]\n"
"v_mfma_f32_16x16x32_bf16 v[194:197], v[226:229], v[50:53], v[194:197]\n"
"s_waitcnt lgkmcnt(2)\n"
"ds_read_b128 v[218:221], v0 offset:19456\n"
"v_fmac_f32 v150, v230, v242\n"
"v_fmac_f32 v151, v231, v242\n"
"v_fmac_f32 v152, v232, v242\n"
"v_mfma_f32_16x16x32_bf16 v[170:173], v[206:209], v[46:49], v[170:173]\n"
"v_mfma_f32_16x16x32_bf16 v[198:201], v[206:209], v[50:53], v[198:201]\n"
"s_waitcnt lgkmcnt(2)\n"
"ds_read_b128 v[222:225], v0 offset:20480\n"
"v_fmac_f32 v153, v233, v242\n"
"v_fmac_f32 v154, v234, v242\n"
"v_fmac_f32 v155, v235, v242\n"
"v_mfma_f32_16x16x32_bf16 v[174:177], v[210:213], v[46:49], v[174:177]\n"
"v_mfma_f32_16x16x32_bf16 v[202:205], v[210:213], v[50:53], v[202:205]\n"
"s_waitcnt lgkmcnt(2)\n"
"ds_read_b128 v[226:229], v0 offset:25600\n"
"v_fmac_f32 v156, v236, v242\n"
"v_fmac_f32 v157, v237, v242\n"
"v_cvt_pk_bf16_f32 v246, v150, v151\n"
"v_mfma_f32_16x16x32_bf16 v[166:169], v[214:217], v[54:57], v[166:169]\n"
"v_mfma_f32_16x16x32_bf16 v[194:197], v[214:217], v[58:61], v[194:197]\n"
"s_waitcnt lgkmcnt(2)\n"
"ds_read_b128 v[206:209], v0 offset:26624\n"
"v_cvt_pk_bf16_f32 v247, v152, v153\n"
"v_cvt_pk_bf16_f32 v248, v154, v155\n"
"v_cvt_pk_bf16_f32 v249, v156, v157\n"
"v_mfma_f32_16x16x32_bf16 v[170:173], v[218:221], v[54:57], v[170:173]\n"
"v_mfma_f32_16x16x32_bf16 v[198:201], v[218:221], v[58:61], v[198:201]\n"
"s_waitcnt lgkmcnt(2)\n"
"ds_read_b128 v[210:213], v0 offset:27648\n"
"v_pk_max_i16 v246, v246, 0\n"
"v_pk_max_i16 v247, v247, 0\n"
"v_pk_max_i16 v248, v248, 0\n"
"v_mfma_f32_16x16x32_bf16 v[174:177], v[222:225], v[54:57], v[174:177]\n"
"v_mfma_f32_16x16x32_bf16 v[202:205], v[222:225], v[58:61], v[202:205]\n"
"s_waitcnt lgkmcnt(2)\n"
"ds_read_b128 v[214:217], v0 offset:32768\n"
"v_pk_max_i16 v249, v249, 0\n"
"buffer_store_dwordx4 v[246:249], v0, s[20:23], s44 offen sc1\n"
"v_mfma_f32_16x16x32_bf16 v[166:169], v[226:229], v[62:65], v[166:169]\n"
"v_mfma_f32_16x16x32_bf16 v[194:197], v[226:229], v[66:69], v[194:197]\n"
"s_waitcnt lgkmcnt(2)\n"
"ds_read_b128 v[218:221], v0 offset:33792\n"
"v_fmac_f32 v178, v230, v243\n"
"v_fmac_f32 v179, v231, v243\n"
"v_fmac_f32 v180, v232, v243\n"
"v_mfma_f32_16x16x32_bf16 v[170:173], v[206:209], v[62:65], v[170:173]\n"
"v_mfma_f32_16x16x32_bf16 v[198:201], v[206:209], v[66:69], v[198:201]\n"
"s_waitcnt lgkmcnt(2)\n"
"ds_read_b128 v[222:225], v0 offset:34816\n"
"v_fmac_f32 v181, v233, v243\n"
"v_fmac_f32 v182, v234, v243\n"
"v_fmac_f32 v183, v235, v243\n"
"v_mfma_f32_16x16x32_bf16 v[174:177], v[210:213], v[62:65], v[174:177]\n"
"v_mfma_f32_16x16x32_bf16 v[202:205], v[210:213], v[66:69], v[202:205]\n"
"s_waitcnt lgkmcnt(2)\n"
"ds_read_b128 v[226:229], v0 offset:39936\n"
"v_fmac_f32 v184, v236, v243\n"
"v_fmac_f32 v185, v237, v243\n"
"v_cvt_pk_bf16_f32 v250, v178, v179\n"
"v_mfma_f32_16x16x32_bf16 v[166:169], v[214:217], v[70:73], v[166:169]\n"
"v_mfma_f32_16x16x32_bf16 v[194:197], v[214:217], v[74:77], v[194:197]\n"
"s_waitcnt lgkmcnt(2)\n"
"ds_read_b128 v[206:209], v0 offset:40960\n"
"v_cvt_pk_bf16_f32 v251, v180, v181\n"
"v_cvt_pk_bf16_f32 v252, v182, v183\n"
"v_cvt_pk_bf16_f32 v253, v184, v185\n"
"v_mfma_f32_16x16x32_bf16 v[170:173], v[218:221], v[70:73], v[170:173]\n"
"v_mfma_f32_16x16x32_bf16 v[198:201], v[218:221], v[74:77], v[198:201]\n"
"s_waitcnt lgkmcnt(2)\n"
"ds_read_b128 v[210:213], v0 offset:41984\n"
"v_pk_max_i16 v250, v250, 0\n"
"v_pk_max_i16 v251, v251, 0\n"
"v_pk_max_i16 v252, v252, 0\n"
"v_mfma_f32_16x16x32_bf16 v[174:177], v[222:225], v[70:73], v[174:177]\n"
"v_mfma_f32_16x16x32_bf16 v[202:205], v[222:225], v[74:77], v[202:205]\n"
"s_waitcnt lgkmcnt(2)\n"
"ds_read_b128 v[214:217], v0 offset:47104\n"
"v_pk_max_i16 v253, v253, 0\n"
"buffer_store_dwordx4 v[250:253], v0, s[20:23], s45 offen sc1\n"
"v_mfma_f32_16x16x32_bf16 v[166:169], v[226:229], v[78:81], v[166:169]\n"
"v_mfma_f32_16x16x32_bf16 v[194:197], v[226:229], v[82:85], v[194:197]\n"
"s_waitcnt lgkmcnt(2)\n"
"ds_read_b128 v[218:221], v0 offset:48128\n"
"ds_read_b128 v[230:233], v4 offset:640\n"
"ds_read_b128 v[234:237], v4 offset:704\n"
"v_mfma_f32_16x16x32_bf16 v[170:173], v[206:209], v[78:81], v[170:173]\n"
"v_mfma_f32_16x16x32_bf16 v[198:201], v[206:209], v[82:85], v[198:201]\n"
"s_waitcnt lgkmcnt(4)\n"
"ds_read_b128 v[222:225], v0 offset:49152\n"
"v_mfma_f32_16x16x32_bf16 v[174:177], v[210:213], v[78:81], v[174:177]\n"
"v_mfma_f32_16x16x32_bf16 v[202:205], v[210:213], v[82:85], v[202:205]\n"
"s_waitcnt lgkmcnt(2)\n"
"ds_read_b128 v[226:229], v0 offset:54272\n"
"v_fmac_f32 v158, v230, v242\n"
"v_fmac_f32 v159, v231, v242\n"
"v_fmac_f32 v160, v232, v242\n"
"v_mfma_f32_16x16x32_bf16 v[166:169], v[214:217], v[86:89], v[166:169]\n"
"v_mfma_f32_16x16x32_bf16 v[194:197], v[214:217], v[90:93], v[194:197]\n"
"s_waitcnt lgkmcnt(2)\n"
"ds_read_b128 v[206:209], v0 offset:55296\n"
"v_fmac_f32 v161, v233, v242\n"
"v_fmac_f32 v162, v234, v242\n"
"v_fmac_f32 v163, v235, v242\n"
"v_mfma_f32_16x16x32_bf16 v[170:173], v[218:221], v[86:89], v[170:173]\n"
"v_mfma_f32_16x16x32_bf16 v[198:201], v[218:221], v[90:93], v[198:201]\n"
"s_waitcnt lgkmcnt(2)\n"
"ds_read_b128 v[210:213], v0 offset:56320\n"
"v_fmac_f32 v164, v236, v242\n"
"v_fmac_f32 v165, v237, v242\n"
"v_cvt_pk_bf16_f32 v246, v158, v159\n"
"v_mfma_f32_16x16x32_bf16 v[174:177], v[222:225], v[86:89], v[174:177]\n"
"v_mfma_f32_16x16x32_bf16 v[202:205], v[222:225], v[90:93], v[202:205]\n"
"s_waitcnt lgkmcnt(2)\n"
"ds_read_b128 v[214:217], v0 offset:61440\n"
"v_cvt_pk_bf16_f32 v247, v160, v161\n"
"v_cvt_pk_bf16_f32 v248, v162, v163\n"
"v_cvt_pk_bf16_f32 v249, v164, v165\n"
"v_mfma_f32_16x16x32_bf16 v[166:169], v[226:229], v[94:97], v[166:169]\n"
"v_mfma_f32_16x16x32_bf16 v[194:197], v[226:229], v[98:101], v[194:197]\n"
"s_waitcnt lgkmcnt(2)\n"
"ds_read_b128 v[218:221], v0 offset:62464\n"
"v_pk_max_i16 v246, v246, 0\n"
"v_pk_max_i16 v247, v247, 0\n"
"v_pk_max_i16 v248, v248, 0\n"
"v_mfma_f32_16x16x32_bf16 v[170:173], v[206:209], v[94:97], v[170:173]\n"
"v_mfma_f32_16x16x32_bf16 v[198:201], v[206:209], v[98:101], v[198:201]\n"
"s_waitcnt lgkmcnt(2)\n"
"ds_read_b128 v[222:225], v0 offset:63488\n"
"v_pk_max_i16 v249, v249, 0\n"
"buffer_store_dwordx4 v[246:249], v0, s[20:23], s44 offen offset:1024 sc1\n"
"v_mfma_f32_16x16x32_bf16 v[174:177], v[210:213], v[94:97], v[174:177]\n"
"v_mfma_f32_16x16x32_bf16 v[202:205], v[210:213], v[98:101], v[202:205]\n"
"s_waitcnt lgkmcnt(2)\n"
"ds_read_b128 v[226:229], v1 offset:3072\n"
"v_fmac_f32 v186, v230, v243\n"
"v_fmac_f32 v187, v231, v243\n"
"v_fmac_f32 v188, v232, v243\n"
"v_mfma_f32_16x16x32_bf16 v[166:169], v[214:217], v[102:105], v[166:169]\n"
"v_mfma_f32_16x16x32_bf16 v[194:197], v[214:217], v[106:109], v[194:197]\n"
"s_waitcnt lgkmcnt(2)\n"
"ds_read_b128 v[206:209], v1 offset:4096\n"
"v_fmac_f32 v189, v233, v243\n"
"v_fmac_f32 v190, v234, v243\n"
"v_fmac_f32 v191, v235, v243\n"
"v_mfma_f32_16x16x32_bf16 v[170:173], v[218:221], v[102:105], v[170:173]\n"
"v_mfma_f32_16x16x32_bf16 v[198:201], v[218:221], v[106:109], v[198:201]\n"
"s_waitcnt lgkmcnt(2)\n"
"ds_read_b128 v[210:213], v1 offset:5120\n"
"v_fmac_f32 v192, v236, v243\n"
"v_fmac_f32 v193, v237, v243\n"
"v_cvt_pk_bf16_f32 v250, v186, v187\n"
"v_mfma_f32_16x16x32_bf16 v[174:177], v[222:225], v[102:105], v[174:177]\n"
"v_mfma_f32_16x16x32_bf16 v[202:205], v[222:225], v[106:109], v[202:205]\n"
"s_waitcnt lgkmcnt(2)\n"
"ds_read_b128 v[214:217], v2 offset:2048\n"
"v_cvt_pk_bf16_f32 v251, v188, v189\n"
"v_cvt_pk_bf16_f32 v252, v190, v191\n"
"v_cvt_pk_bf16_f32 v253, v192, v193\n"
"v_mfma_f32_16x16x32_bf16 v[166:169], v[226:229], v[110:113], v[166:169]\n"
"v_mfma_f32_16x16x32_bf16 v[194:197], v[226:229], v[114:117], v[194:197]\n"
"s_waitcnt lgkmcnt(2)\n"
"ds_read_b128 v[218:221], v2 offset:3072\n"
"v_pk_max_i16 v250, v250, 0\n"
"v_pk_max_i16 v251, v251, 0\n"
"v_pk_max_i16 v252, v252, 0\n"
"v_mfma_f32_16x16x32_bf16 v[170:173], v[206:209], v[110:113], v[170:173]\n"
"v_mfma_f32_16x16x32_bf16 v[198:201], v[206:209], v[114:117], v[198:201]\n"
"s_waitcnt lgkmcnt(2)\n"
"ds_read_b128 v[222:225], v2 offset:4096\n"
"v_pk_max_i16 v253, v253, 0\n"
"buffer_store_dwordx4 v[250:253], v0, s[20:23], s45 offen offset:1024 sc1\n"
"v_mfma_f32_16x16x32_bf16 v[174:177], v[210:213], v[110:113], v[174:177]\n"
"v_mfma_f32_16x16x32_bf16 v[202:205], v[210:213], v[114:117], v[202:205]\n"
"s_waitcnt lgkmcnt(2)\n"
"ds_read_b128 v[226:229], v2 offset:9216\n"
"v_mfma_f32_16x16x32_bf16 v[166:169], v[214:217], v[118:121], v[166:169]\n"
"v_mfma_f32_16x16x32_bf16 v[194:197], v[214:217], v[122:125], v[194:197]\n"
"s_waitcnt vmcnt(4) lgkmcnt(2)\n"
"ds_read_b128 v[206:209], v2 offset:10240\n"
"v_add_f32 v238, v238, v240\n"
"v_mfma_f32_16x16x32_bf16 v[170:173], v[218:221], v[118:121], v[170:173]\n"
"v_mfma_f32_16x16x32_bf16 v[198:201], v[218:221], v[122:125], v[198:201]\n"
"s_waitcnt lgkmcnt(2)\n"
"ds_read_b128 v[210:213], v2 offset:11264\n"
"v_add_f32 v239, v239, v241\n"
"v_mfma_f32_16x16x32_bf16 v[174:177], v[222:225], v[118:121], v[174:177]\n"
"v_mfma_f32_16x16x32_bf16 v[202:205], v[222:225], v[122:125], v[202:205]\n"
"s_waitcnt lgkmcnt(2)\n"
"ds_read_b128 v[214:217], v2 offset:16384\n"
"v_permlane16_swap_b32 v238, v239\n"
"v_mfma_f32_16x16x32_bf16 v[166:169], v[226:229], v[126:129], v[166:169]\n"
"v_mfma_f32_16x16x32_bf16 v[194:197], v[226:229], v[130:133], v[194:197]\n"
"s_waitcnt lgkmcnt(2)\n"
"ds_read_b128 v[218:221], v2 offset:17408\n"
"v_add_f32 v238, v238, v239\n"
"v_mfma_f32_16x16x32_bf16 v[170:173], v[206:209], v[126:129], v[170:173]\n"
"v_mfma_f32_16x16x32_bf16 v[198:201], v[206:209], v[130:133], v[198:201]\n"
"s_waitcnt lgkmcnt(2)\n"
"ds_read_b128 v[222:225], v2 offset:18432\n"
"v_mov_b32 v239, v238\n"
"v_mfma_f32_16x16x32_bf16 v[174:177], v[210:213], v[126:129], v[174:177]\n"
"v_mfma_f32_16x16x32_bf16 v[202:205], v[210:213], v[130:133], v[202:205]\n"
"s_waitcnt lgkmcnt(2)\n"
"ds_read_b128 v[226:229], v2 offset:23552\n"
"v_permlane32_swap_b32 v238, v239\n"
"v_mfma_f32_16x16x32_bf16 v[166:169], v[214:217], v[134:137], v[166:169]\n"
"v_mfma_f32_16x16x32_bf16 v[194:197], v[214:217], v[138:141], v[194:197]\n"
"s_waitcnt lgkmcnt(2)\n"
"ds_read_b128 v[206:209], v2 offset:24576\n"
"v_add_f32 v238, v238, v239\n"
"v_mfma_f32_16x16x32_bf16 v[170:173], v[218:221], v[134:137], v[170:173]\n"
"v_mfma_f32_16x16x32_bf16 v[198:201], v[218:221], v[138:141], v[198:201]\n"
"s_waitcnt lgkmcnt(2)\n"
"ds_read_b128 v[210:213], v2 offset:25600\n"
"v_mov_b32 v239, v238\n"
"v_mfma_f32_16x16x32_bf16 v[174:177], v[222:225], v[134:137], v[174:177]\n"
"v_mfma_f32_16x16x32_bf16 v[202:205], v[222:225], v[138:141], v[202:205]\n"
"s_waitcnt lgkmcnt(2)\n"
"v_permlane16_swap_b32 v238, v239\n"
"v_mfma_f32_16x16x32_bf16 v[166:169], v[226:229], v[142:145], v[166:169]\n"
"v_mfma_f32_16x16x32_bf16 v[194:197], v[226:229], v[146:149], v[194:197]\n"
"s_waitcnt lgkmcnt(1)\n"
"v_mul_f32 v238, 0x3b124925, v238\n"
"v_mfma_f32_16x16x32_bf16 v[170:173], v[206:209], v[142:145], v[170:173]\n"
"v_mfma_f32_16x16x32_bf16 v[198:201], v[206:209], v[146:149], v[198:201]\n"
"s_waitcnt lgkmcnt(0)\n"
"v_mul_f32 v238, v238, v238\n"
"v_mfma_f32_16x16x32_bf16 v[174:177], v[210:213], v[142:145], v[174:177]\n"
"v_mfma_f32_16x16x32_bf16 v[202:205], v[210:213], v[146:149], v[202:205]\n"
"v_fma_f32 v238, v239, v37, -v238\n"
"v_max_f32 v238, 0, v238\n"
"v_add_f32 v238, 0x3727c5ac, v238\n"
"v_rsq_f32 v244, v238\n"
"v_cvt_pk_bf16_f32 v246, v166, v167\n"
"v_cvt_pk_bf16_f32 v247, v168, v169\n"
"v_cvt_pk_bf16_f32 v248, v170, v171\n"
"v_cvt_pk_bf16_f32 v249, v172, v173\n"
"v_pk_max_i16 v246, v246, 0\n"
"v_pk_max_i16 v247, v247, 0\n"
"v_pk_max_i16 v248, v248, 0\n"
"v_pk_max_i16 v249, v249, 0\n"
"buffer_store_dwordx4 v[246:249], v0, s[20:23], s44 offen offset:2048 sc1\n"
"v_cvt_pk_bf16_f32 v250, v194, v195\n"
"v_cvt_pk_bf16_f32 v251, v196, v197\n"
"v_cvt_pk_bf16_f32 v252, v198, v199\n"
"v_cvt_pk_bf16_f32 v253, v200, v201\n"
"v_pk_max_i16 v250, v250, 0\n"
"v_pk_max_i16 v251, v251, 0\n"
"v_pk_max_i16 v252, v252, 0\n"
"v_pk_max_i16 v253, v253, 0\n"
"buffer_store_dwordx4 v[250:253], v0, s[20:23], s45 offen offset:2048 sc1\n"
"v_cvt_pk_bf16_f32 v206, v174, v175\n"
"v_cvt_pk_bf16_f32 v207, v176, v177\n"
"v_cvt_pk_bf16_f32 v208, v202, v203\n"
"v_cvt_pk_bf16_f32 v209, v204, v205\n"
"v_pk_max_i16 v206, v206, 0\n"
"v_pk_max_i16 v207, v207, 0\n"
"v_pk_max_i16 v208, v208, 0\n"
"v_pk_max_i16 v209, v209, 0\n"
"s_nop 0\n"
"v_permlane16_swap_b32 v206, v208\n"
"v_permlane16_swap_b32 v207, v209\n"
"buffer_store_dwordx4 v[206:209], v8, s[20:23], s44 offen offset:3072 sc1\n"
"s_mov_b32 s49, 0x1f91000\n"
"s_mul_i32 s51, s97, 0x70000\n"
"s_add_u32 s49, s49, s51\n"
"s_mul_i32 s51, s36, 0x38000\n"
"s_add_u32 s49, s49, s51\n"
"s_mul_i32 s51, s96, 0x3800\n"
"s_add_u32 s49, s49, s51\n"
"s_add_u32 s48, s96, 0\n"
"s_and_b32 s48, s48, 3\n"
"s_mul_i32 s50, s48, 0xe000\n"
"s_add_u32 s44, s49, s50\n"
"s_mul_i32 s48, s48, 0x3000\n"
"v_add_u32 v184, s48, v0\n"
"s_add_u32 s48, s96, 1\n"
"s_and_b32 s48, s48, 3\n"
"s_mul_i32 s50, s48, 0xe000\n"
"s_add_u32 s45, s49, s50\n"
"s_mul_i32 s48, s48, 0x3000\n"
"v_add_u32 v185, s48, v0\n"
"s_add_u32 s48, s96, 2\n"
"s_and_b32 s48, s48, 3\n"
"s_mul_i32 s50, s48, 0xe000\n"
"s_add_u32 s46, s49, s50\n"
"s_mul_i32 s48, s48, 0x3000\n"
"s_add_u32 s48, s48, 0x1000\n"
"v_add_u32 v186, s48, v0\n"
"s_add_u32 s48, s96, 3\n"
"s_and_b32 s48, s48, 3\n"
"s_mul_i32 s50, s48, 0xe000\n"
"s_add_u32 s47, s49, s50\n"
"s_mul_i32 s48, s48, 0x3000\n"
"s_add_u32 s48, s48, 0x2000\n"
"v_add_u32 v187, s48, v0\n"
"v_mov_b32 v38, v244\n"
"s_waitcnt vmcnt(0)\n"
"s_waitcnt lgkmcnt(0)\n"
"s_barrier\n"
"s_cmp_lg_u32 s96, 0\n"
"s_cbranch_scc1 .Lkw0_67\n"
"s_mov_b64 exec, 0xffff\n"
"buffer_atomic_add v10, v11, s[20:23], s52 offen\n"
"s_mov_b64 exec, -1\n"
".Lkw0_67:\n"
"; D layer 1\n"
"buffer_load_dword v120, off, s[20:23], s53 offset:0 sc1\n"
"s_mov_b32 s55, 0\n"
"s_waitcnt vmcnt(0)\n"
".Lkpoll_68:\n"
"v_readfirstlane_b32 s48, v120\n"
"s_add_u32 s55, s55, 1\n"
"s_cmp_ge_u32 s48, 0x70\n"
"s_cbranch_scc1 .Lkdone_69\n"
"buffer_load_dword v120, off, s[20:23], s53 offset:0 sc1\n"
"s_cmp_lt_u32 s55, 0x40000\n"
"s_waitcnt vmcnt(0)\n"
"s_cbranch_scc1 .Lkpoll_68\n"
"s_mov_b32 s48, 0x40000\n"
"buffer_store_dword v10, off, s[20:23], s48 sc1\n"
".Lkdone_69:\n"
"buffer_load_dwordx4 v[120:123], v0, s[20:23], s44 offen sc1\n"
"buffer_load_dwordx4 v[124:127], v0, s[20:23], s45 offen sc1\n"
"buffer_load_dwordx4 v[128:131], v0, s[20:23], s46 offen sc1\n"
"buffer_load_dwordx4 v[132:135], v0, s[20:23], s47 offen sc1\n"
"buffer_load_dwordx4 v[136:139], v0, s[20:23], s44 offen offset:1024 sc1\n"
"buffer_load_dwordx4 v[140:143], v0, s[20:23], s45 offen offset:1024 sc1\n"
"buffer_load_dwordx4 v[144:147], v0, s[20:23], s46 offen offset:1024 sc1\n"
"buffer_load_dwordx4 v[148:151], v0, s[20:23], s47 offen offset:1024 sc1\n"
"buffer_load_dwordx4 v[152:155], v0, s[20:23], s44 offen offset:2048 sc1\n"
"buffer_load_dwordx4 v[156:159], v0, s[20:23], s45 offen offset:2048 sc1\n"
"buffer_load_dwordx4 v[160:163], v0, s[20:23], s46 offen offset:2048 sc1\n"
"buffer_load_dwordx4 v[164:167], v0, s[20:23], s47 offen offset:2048 sc1\n"
"s_waitcnt vmcnt(11)\n"
"v_mfma_f32_16x16x32_bf16 v[40:43], a[0:3], v[120:123], 0\n"
"v_mfma_f32_16x16x32_bf16 v[44:47], a[4:7], v[120:123], 0\n"
"v_mfma_f32_16x16x32_bf16 v[48:51], a[8:11], v[120:123], 0\n"
"v_mfma_f32_16x16x32_bf16 v[52:55], a[168:171], v[120:123], 0\n"
"s_waitcnt vmcnt(10)\n"
"buffer_load_dwordx4 v[168:171], v0, s[20:23], s44 offen offset:3072 sc1\n"
"v_mfma_f32_16x16x32_bf16 v[60:63], a[0:3], v[124:127], 0\n"
"v_mfma_f32_16x16x32_bf16 v[64:67], a[4:7], v[124:127], 0\n"
"v_mfma_f32_16x16x32_bf16 v[68:71], a[8:11], v[124:127], 0\n"
"v_mfma_f32_16x16x32_bf16 v[72:75], a[168:171], v[124:127], 0\n"
"s_waitcnt vmcnt(10)\n"
"buffer_load_dwordx4 v[172:175], v0, s[20:23], s45 offen offset:3072 sc1\n"
"v_mfma_f32_16x16x32_bf16 v[80:83], a[0:3], v[128:131], 0\n"
"v_mfma_f32_16x16x32_bf16 v[84:87], a[4:7], v[128:131], 0\n"
"v_mfma_f32_16x16x32_bf16 v[88:91], a[8:11], v[128:131], 0\n"
"v_mfma_f32_16x16x32_bf16 v[92:95], a[168:171], v[128:131], 0\n"
"s_waitcnt vmcnt(10)\n"
"buffer_load_dwordx4 v[176:179], v0, s[20:23], s46 offen offset:3072 sc1\n"
"v_mfma_f32_16x16x32_bf16 v[100:103], a[0:3], v[132:135], 0\n"
"v_mfma_f32_16x16x32_bf16 v[104:107], a[4:7], v[132:135], 0\n"
"v_mfma_f32_16x16x32_bf16 v[108:111], a[8:11], v[132:135], 0\n"
"v_mfma_f32_16x16x32_bf16 v[112:115], a[168:171], v[132:135], 0\n"
"s_waitcnt vmcnt(10)\n"
"buffer_load_dwordx4 v[180:183], v0, s[20:23], s47 offen offset:3072 sc1\n"
"v_mfma_f32_16x16x32_bf16 v[40:43], a[12:15], v[136:139], v[40:43]\n"
"v_mfma_f32_16x16x32_bf16 v[44:47], a[16:19], v[136:139], v[44:47]\n"
"v_mfma_f32_16x16x32_bf16 v[48:51], a[20:23], v[136:139], v[48:51]\n"
"v_mfma_f32_16x16x32_bf16 v[56:59], a[168:171], v[136:139], 0\n"
"s_waitcnt vmcnt(10)\n"
"s_add_u32 s44, s44, 0x1000\n"
"buffer_load_dwordx4 v[120:123], v0, s[20:23], s44 offen sc1\n"
"v_mfma_f32_16x16x32_bf16 v[60:63], a[12:15], v[140:143], v[60:63]\n"
"v_mfma_f32_16x16x32_bf16 v[64:67], a[16:19], v[140:143], v[64:67]\n"
"v_mfma_f32_16x16x32_bf16 v[68:71], a[20:23], v[140:143], v[68:71]\n"
"v_mfma_f32_16x16x32_bf16 v[76:79], a[168:171], v[140:143], 0\n"
"s_waitcnt vmcnt(10)\n"
"s_add_u32 s45, s45, 0x1000\n"
"buffer_load_dwordx4 v[124:127], v0, s[20:23], s45 offen sc1\n"
"v_mfma_f32_16x16x32_bf16 v[80:83], a[12:15], v[144:147], v[80:83]\n"
"v_mfma_f32_16x16x32_bf16 v[84:87], a[16:19], v[144:147], v[84:87]\n"
"v_mfma_f32_16x16x32_bf16 v[88:91], a[20:23], v[144:147], v[88:91]\n"
"v_mfma_f32_16x16x32_bf16 v[96:99], a[168:171], v[144:147], 0\n"
"s_waitcnt vmcnt(10)\n"
"s_add_u32 s46, s46, 0x1000\n"
"buffer_load_dwordx4 v[128:131], v0, s[20:23], s46 offen sc1\n"
"v_mfma_f32_16x16x32_bf16 v[100:103], a[12:15], v[148:151], v[100:103]\n"
"v_mfma_f32_16x16x32_bf16 v[104:107], a[16:19], v[148:151], v[104:107]\n"
"v_mfma_f32_16x16x32_bf16 v[108:111], a[20:23], v[148:151], v[108:111]\n"
"v_mfma_f32_16x16x32_bf16 v[116:119], a[168:171], v[148:151], 0\n"
"s_waitcnt vmcnt(10)\n"
"s_add_u32 s47, s47, 0x1000\n"
"buffer_load_dwordx4 v[132:135], v0, s[20:23], s47 offen sc1\n"
"v_mfma_f32_16x16x32_bf16 v[40:43], a[24:27], v[152:155], v[40:43]\n"
"v_mfma_f32_16x16x32_bf16 v[44:47], a[28:31], v[152:155], v[44:47]\n"
"v_mfma_f32_16x16x32_bf16 v[48:51], a[32:35], v[152:155], v[48:51]\n"
"v_mfma_f32_16x16x32_bf16 v[52:55], a[172:175], v[152:155], v[52:55]\n"
"s_waitcnt vmcnt(10)\n"
"buffer_load_dwordx4 v[136:139], v0, s[20:23], s44 offen offset:1024 sc1\n"
"v_mfma_f32_16x16x32_bf16 v[60:63], a[24:27], v[156:159], v[60:63]\n"
"v_mfma_f32_16x16x32_bf16 v[64:67], a[28:31], v[156:159], v[64:67]\n"
"v_mfma_f32_16x16x32_bf16 v[68:71], a[32:35], v[156:159], v[68:71]\n"
"v_mfma_f32_16x16x32_bf16 v[72:75], a[172:175], v[156:159], v[72:75]\n"
"s_waitcnt vmcnt(10)\n"
"buffer_load_dwordx4 v[140:143], v0, s[20:23], s45 offen offset:1024 sc1\n"
"v_mfma_f32_16x16x32_bf16 v[80:83], a[24:27], v[160:163], v[80:83]\n"
"v_mfma_f32_16x16x32_bf16 v[84:87], a[28:31], v[160:163], v[84:87]\n"
"v_mfma_f32_16x16x32_bf16 v[88:91], a[32:35], v[160:163], v[88:91]\n"
"v_mfma_f32_16x16x32_bf16 v[92:95], a[172:175], v[160:163], v[92:95]\n"
"s_waitcnt vmcnt(10)\n"
"buffer_load_dwordx4 v[144:147], v0, s[20:23], s46 offen offset:1024 sc1\n"
"v_mfma_f32_16x16x32_bf16 v[100:103], a[24:27], v[164:167], v[100:103]\n"
"v_mfma_f32_16x16x32_bf16 v[104:107], a[28:31], v[164:167], v[104:107]\n"
"v_mfma_f32_16x16x32_bf16 v[108:111], a[32:35], v[164:167], v[108:111]\n"
"v_mfma_f32_16x16x32_bf16 v[112:115], a[172:175], v[164:167], v[112:115]\n"
"s_waitcnt vmcnt(10)\n"
"buffer_load_dwordx4 v[148:151], v0, s[20:23], s47 offen offset:1024 sc1\n"
"v_mfma_f32_16x16x32_bf16 v[40:43], a[36:39], v[168:171], v[40:43]\n"
"v_mfma_f32_16x16x32_bf16 v[44:47], a[40:43], v[168:171], v[44:47]\n"
"v_mfma_f32_16x16x32_bf16 v[48:51], a[44:47], v[168:171], v[48:51]\n"
"v_mfma_f32_16x16x32_bf16 v[56:59], a[172:175], v[168:171], v[56:59]\n"
"s_waitcnt vmcnt(10)\n"
"buffer_load_dwordx4 v[152:155], v0, s[20:23], s44 offen offset:2048 sc1\n"
"v_mfma_f32_16x16x32_bf16 v[60:63], a[36:39], v[172:175], v[60:63]\n"
"v_mfma_f32_16x16x32_bf16 v[64:67], a[40:43], v[172:175], v[64:67]\n"
"v_mfma_f32_16x16x32_bf16 v[68:71], a[44:47], v[172:175], v[68:71]\n"
"v_mfma_f32_16x16x32_bf16 v[76:79], a[172:175], v[172:175], v[76:79]\n"
"s_waitcnt vmcnt(10)\n"
"buffer_load_dwordx4 v[156:159], v0, s[20:23], s45 offen offset:2048 sc1\n"
"v_mfma_f32_16x16x32_bf16 v[80:83], a[36:39], v[176:179], v[80:83]\n"
"v_mfma_f32_16x16x32_bf16 v[84:87], a[40:43], v[176:179], v[84:87]\n"
"v_mfma_f32_16x16x32_bf16 v[88:91], a[44:47], v[176:179], v[88:91]\n"
"v_mfma_f32_16x16x32_bf16 v[96:99], a[172:175], v[176:179], v[96:99]\n"
"s_waitcnt vmcnt(10)\n"
"buffer_load_dwordx4 v[160:163], v0, s[20:23], s46 offen offset:2048 sc1\n"
"v_mfma_f32_16x16x32_bf16 v[100:103], a[36:39], v[180:183], v[100:103]\n"
"v_mfma_f32_16x16x32_bf16 v[104:107], a[40:43], v[180:183], v[104:107]\n"
"v_mfma_f32_16x16x32_bf16 v[108:111], a[44:47], v[180:183], v[108:111]\n"
"v_mfma_f32_16x16x32_bf16 v[116:119], a[172:175], v[180:183], v[116:119]\n"
"s_waitcnt vmcnt(10)\n"
"buffer_load_dwordx4 v[164:167], v0, s[20:23], s47 offen offset:2048 sc1\n"
"v_mfma_f32_16x16x32_bf16 v[40:43], a[48:51], v[120:123], v[40:43]\n"
"v_mfma_f32_16x16x32_bf16 v[44:47], a[52:55], v[120:123], v[44:47]\n"
"v_mfma_f32_16x16x32_bf16 v[48:51], a[56:59], v[120:123], v[48:51]\n"
"v_mfma_f32_16x16x32_bf16 v[52:55], a[176:179], v[120:123], v[52:55]\n"
"s_waitcnt vmcnt(10)\n"
"buffer_load_dwordx4 v[168:171], v0, s[20:23], s44 offen offset:3072 sc1\n"
"v_mfma_f32_16x16x32_bf16 v[60:63], a[48:51], v[124:127], v[60:63]\n"
"v_mfma_f32_16x16x32_bf16 v[64:67], a[52:55], v[124:127], v[64:67]\n"
"v_mfma_f32_16x16x32_bf16 v[68:71], a[56:59], v[124:127], v[68:71]\n"
"v_mfma_f32_16x16x32_bf16 v[72:75], a[176:179], v[124:127], v[72:75]\n"
"s_waitcnt vmcnt(10)\n"
"buffer_load_dwordx4 v[172:175], v0, s[20:23], s45 offen offset:3072 sc1\n"
"v_mfma_f32_16x16x32_bf16 v[80:83], a[48:51], v[128:131], v[80:83]\n"
"v_mfma_f32_16x16x32_bf16 v[84:87], a[52:55], v[128:131], v[84:87]\n"
"v_mfma_f32_16x16x32_bf16 v[88:91], a[56:59], v[128:131], v[88:91]\n"
"v_mfma_f32_16x16x32_bf16 v[92:95], a[176:179], v[128:131], v[92:95]\n"
"s_waitcnt vmcnt(10)\n"
"buffer_load_dwordx4 v[176:179], v0, s[20:23], s46 offen offset:3072 sc1\n"
"v_mfma_f32_16x16x32_bf16 v[100:103], a[48:51], v[132:135], v[100:103]\n"
"v_mfma_f32_16x16x32_bf16 v[104:107], a[52:55], v[132:135], v[104:107]\n"
"v_mfma_f32_16x16x32_bf16 v[108:111], a[56:59], v[132:135], v[108:111]\n"
"v_mfma_f32_16x16x32_bf16 v[112:115], a[176:179], v[132:135], v[112:115]\n"
"s_waitcnt vmcnt(10)\n"
"buffer_load_dwordx4 v[180:183], v0, s[20:23], s47 offen offset:3072 sc1\n"
"v_mfma_f32_16x16x32_bf16 v[40:43], a[60:63], v[136:139], v[40:43]\n"
"v_mfma_f32_16x16x32_bf16 v[44:47], a[64:67], v[136:139], v[44:47]\n"
"v_mfma_f32_16x16x32_bf16 v[48:51], a[68:71], v[136:139], v[48:51]\n"
"v_mfma_f32_16x16x32_bf16 v[56:59], a[176:179], v[136:139], v[56:59]\n"
"s_waitcnt vmcnt(10)\n"
"s_add_u32 s44, s44, 0x1000\n"
"buffer_load_dwordx4 v[120:123], v0, s[20:23], s44 offen sc1\n"
"v_mfma_f32_16x16x32_bf16 v[60:63], a[60:63], v[140:143], v[60:63]\n"
"v_mfma_f32_16x16x32_bf16 v[64:67], a[64:67], v[140:143], v[64:67]\n"
"v_mfma_f32_16x16x32_bf16 v[68:71], a[68:71], v[140:143], v[68:71]\n"
"v_mfma_f32_16x16x32_bf16 v[76:79], a[176:179], v[140:143], v[76:79]\n"
"s_waitcnt vmcnt(10)\n"
"s_add_u32 s45, s45, 0x1000\n"
"buffer_load_dwordx4 v[124:127], v0, s[20:23], s45 offen sc1\n"
"v_mfma_f32_16x16x32_bf16 v[80:83], a[60:63], v[144:147], v[80:83]\n"
"v_mfma_f32_16x16x32_bf16 v[84:87], a[64:67], v[144:147], v[84:87]\n"
"v_mfma_f32_16x16x32_bf16 v[88:91], a[68:71], v[144:147], v[88:91]\n"
"v_mfma_f32_16x16x32_bf16 v[96:99], a[176:179], v[144:147], v[96:99]\n"
"s_waitcnt vmcnt(10)\n"
"s_add_u32 s46, s46, 0x1000\n"
"buffer_load_dwordx4 v[128:131], v0, s[20:23], s46 offen sc1\n"
"v_mfma_f32_16x16x32_bf16 v[100:103], a[60:63], v[148:151], v[100:103]\n"
"v_mfma_f32_16x16x32_bf16 v[104:107], a[64:67], v[148:151], v[104:107]\n"
"v_mfma_f32_16x16x32_bf16 v[108:111], a[68:71], v[148:151], v[108:111]\n"
"v_mfma_f32_16x16x32_bf16 v[116:119], a[176:179], v[148:151], v[116:119]\n"
"s_waitcnt vmcnt(10)\n"
"s_add_u32 s47, s47, 0x1000\n"
"buffer_load_dwordx4 v[132:135], v0, s[20:23], s47 offen sc1\n"
"v_mfma_f32_16x16x32_bf16 v[40:43], a[72:75], v[152:155], v[40:43]\n"
"v_mfma_f32_16x16x32_bf16 v[44:47], a[76:79], v[152:155], v[44:47]\n"
"v_mfma_f32_16x16x32_bf16 v[48:51], a[80:83], v[152:155], v[48:51]\n"
"v_mfma_f32_16x16x32_bf16 v[52:55], a[180:183], v[152:155], v[52:55]\n"
"s_waitcnt vmcnt(10)\n"
"buffer_load_dwordx4 v[136:139], v0, s[20:23], s44 offen offset:1024 sc1\n"
"v_mfma_f32_16x16x32_bf16 v[60:63], a[72:75], v[156:159], v[60:63]\n"
"v_mfma_f32_16x16x32_bf16 v[64:67], a[76:79], v[156:159], v[64:67]\n"
"v_mfma_f32_16x16x32_bf16 v[68:71], a[80:83], v[156:159], v[68:71]\n"
"v_mfma_f32_16x16x32_bf16 v[72:75], a[180:183], v[156:159], v[72:75]\n"
"s_waitcnt vmcnt(10)\n"
"buffer_load_dwordx4 v[140:143], v0, s[20:23], s45 offen offset:1024 sc1\n"
"v_mfma_f32_16x16x32_bf16 v[80:83], a[72:75], v[160:163], v[80:83]\n"
"v_mfma_f32_16x16x32_bf16 v[84:87], a[76:79], v[160:163], v[84:87]\n"
"v_mfma_f32_16x16x32_bf16 v[88:91], a[80:83], v[160:163], v[88:91]\n"
"v_mfma_f32_16x16x32_bf16 v[92:95], a[180:183], v[160:163], v[92:95]\n"
"s_waitcnt vmcnt(10)\n"
"buffer_load_dwordx4 v[144:147], v0, s[20:23], s46 offen offset:1024 sc1\n"
"v_mfma_f32_16x16x32_bf16 v[100:103], a[72:75], v[164:167], v[100:103]\n"
"v_mfma_f32_16x16x32_bf16 v[104:107], a[76:79], v[164:167], v[104:107]\n"
"v_mfma_f32_16x16x32_bf16 v[108:111], a[80:83], v[164:167], v[108:111]\n"
"v_mfma_f32_16x16x32_bf16 v[112:115], a[180:183], v[164:167], v[112:115]\n"
"s_waitcnt vmcnt(10)\n"
"buffer_load_dwordx4 v[148:151], v0, s[20:23], s47 offen offset:1024 sc1\n"
"v_mfma_f32_16x16x32_bf16 v[40:43], a[84:87], v[168:171], v[40:43]\n"
"v_mfma_f32_16x16x32_bf16 v[44:47], a[88:91], v[168:171], v[44:47]\n"
"v_mfma_f32_16x16x32_bf16 v[48:51], a[92:95], v[168:171], v[48:51]\n"
"v_mfma_f32_16x16x32_bf16 v[56:59], a[180:183], v[168:171], v[56:59]\n"
"s_waitcnt vmcnt(10)\n"
"buffer_load_dwordx4 v[152:155], v0, s[20:23], s45 offen offset:2048 sc1\n"
"v_mfma_f32_16x16x32_bf16 v[60:63], a[84:87], v[172:175], v[60:63]\n"
"v_mfma_f32_16x16x32_bf16 v[64:67], a[88:91], v[172:175], v[64:67]\n"
"v_mfma_f32_16x16x32_bf16 v[68:71], a[92:95], v[172:175], v[68:71]\n"
"v_mfma_f32_16x16x32_bf16 v[76:79], a[180:183], v[172:175], v[76:79]\n"
"s_waitcnt vmcnt(10)\n"
"buffer_load_dwordx4 v[156:159], v0, s[20:23], s45 offen offset:3072 sc1\n"
"v_mfma_f32_16x16x32_bf16 v[80:83], a[84:87], v[176:179], v[80:83]\n"
"v_mfma_f32_16x16x32_bf16 v[84:87], a[88:91], v[176:179], v[84:87]\n"
"v_mfma_f32_16x16x32_bf16 v[88:91], a[92:95], v[176:179], v[88:91]\n"
"v_mfma_f32_16x16x32_bf16 v[96:99], a[180:183], v[176:179], v[96:99]\n"
"s_waitcnt vmcnt(10)\n"
"s_add_u32 s45, s45, 0x1000\n"
"buffer_load_dwordx4 v[160:163], v0, s[20:23], s45 offen sc1\n"
"v_mfma_f32_16x16x32_bf16 v[100:103], a[84:87], v[180:183], v[100:103]\n"
"v_mfma_f32_16x16x32_bf16 v[104:107], a[88:91], v[180:183], v[104:107]\n"
"v_mfma_f32_16x16x32_bf16 v[108:111], a[92:95], v[180:183], v[108:111]\n"
"v_mfma_f32_16x16x32_bf16 v[116:119], a[180:183], v[180:183], v[116:119]\n"
"s_waitcnt vmcnt(10)\n"
"buffer_load_dwordx4 v[164:167], v0, s[20:23], s45 offen offset:1024 sc1\n"
"v_mfma_f32_16x16x32_bf16 v[40:43], a[96:99], v[120:123], v[40:43]\n"
"v_mfma_f32_16x16x32_bf16 v[44:47], a[100:103], v[120:123], v[44:47]\n"
"v_mfma_f32_16x16x32_bf16 v[48:51], a[104:107], v[120:123], v[48:51]\n"
"v_mfma_f32_16x16x32_bf16 v[52:55], a[184:187], v[120:123], v[52:55]\n"
"s_waitcnt vmcnt(10)\n"
"buffer_load_dwordx4 v[168:171], v0, s[20:23], s46 offen offset:2048 sc1\n"
"v_mfma_f32_16x16x32_bf16 v[60:63], a[96:99], v[124:127], v[60:63]\n"
"v_mfma_f32_16x16x32_bf16 v[64:67], a[100:103], v[124:127], v[64:67]\n"
"v_mfma_f32_16x16x32_bf16 v[68:71], a[104:107], v[124:127], v[68:71]\n"
"v_mfma_f32_16x16x32_bf16 v[72:75], a[184:187], v[124:127], v[72:75]\n"
"s_waitcnt vmcnt(10)\n"
"buffer_load_dwordx4 v[172:175], v0, s[20:23], s46 offen offset:3072 sc1\n"
"v_mfma_f32_16x16x32_bf16 v[80:83], a[96:99], v[128:131], v[80:83]\n"
"v_mfma_f32_16x16x32_bf16 v[84:87], a[100:103], v[128:131], v[84:87]\n"
"v_mfma_f32_16x16x32_bf16 v[88:91], a[104:107], v[128:131], v[88:91]\n"
"v_mfma_f32_16x16x32_bf16 v[92:95], a[184:187], v[128:131], v[92:95]\n"
"s_waitcnt vmcnt(10)\n"
"s_add_u32 s46, s46, 0x1000\n"
"buffer_load_dwordx4 v[176:179], v0, s[20:23], s46 offen sc1\n"
"v_mfma_f32_16x16x32_bf16 v[100:103], a[96:99], v[132:135], v[100:103]\n"
"v_mfma_f32_16x16x32_bf16 v[104:107], a[100:103], v[132:135], v[104:107]\n"
"v_mfma_f32_16x16x32_bf16 v[108:111], a[104:107], v[132:135], v[108:111]\n"
"v_mfma_f32_16x16x32_bf16 v[112:115], a[184:187], v[132:135], v[112:115]\n"
"s_waitcnt vmcnt(10)\n"
"buffer_load_dwordx4 v[180:183], v0, s[20:23], s46 offen offset:1024 sc1\n"
"v_mfma_f32_16x16x32_bf16 v[40:43], a[108:111], v[136:139], v[40:43]\n"
"v_mfma_f32_16x16x32_bf16 v[44:47], a[112:115], v[136:139], v[44:47]\n"
"v_mfma_f32_16x16x32_bf16 v[48:51], a[116:119], v[136:139], v[48:51]\n"
"v_mfma_f32_16x16x32_bf16 v[56:59], a[184:187], v[136:139], v[56:59]\n"
"s_waitcnt vmcnt(10)\n"
"buffer_load_dwordx4 v[120:123], v0, s[20:23], s47 offen offset:2048 sc1\n"
"v_mfma_f32_16x16x32_bf16 v[60:63], a[108:111], v[140:143], v[60:63]\n"
"v_mfma_f32_16x16x32_bf16 v[64:67], a[112:115], v[140:143], v[64:67]\n"
"v_mfma_f32_16x16x32_bf16 v[68:71], a[116:119], v[140:143], v[68:71]\n"
"v_mfma_f32_16x16x32_bf16 v[76:79], a[184:187], v[140:143], v[76:79]\n"
"s_waitcnt vmcnt(10)\n"
"buffer_load_dwordx4 v[124:127], v0, s[20:23], s47 offen offset:3072 sc1\n"
"v_mfma_f32_16x16x32_bf16 v[80:83], a[108:111], v[144:147], v[80:83]\n"
"v_mfma_f32_16x16x32_bf16 v[84:87], a[112:115], v[144:147], v[84:87]\n"
"v_mfma_f32_16x16x32_bf16 v[88:91], a[116:119], v[144:147], v[88:91]\n"
"v_mfma_f32_16x16x32_bf16 v[96:99], a[184:187], v[144:147], v[96:99]\n"
"s_waitcnt vmcnt(10)\n"
"s_add_u32 s47, s47, 0x1000\n"
"buffer_load_dwordx4 v[128:131], v0, s[20:23], s47 offen sc1\n"
"v_mfma_f32_16x16x32_bf16 v[100:103], a[108:111], v[148:151], v[100:103]\n"
"v_mfma_f32_16x16x32_bf16 v[104:107], a[112:115], v[148:151], v[104:107]\n"
"v_mfma_f32_16x16x32_bf16 v[108:111], a[116:119], v[148:151], v[108:111]\n"
"v_mfma_f32_16x16x32_bf16 v[116:119], a[184:187], v[148:151], v[116:119]\n"
"s_waitcnt vmcnt(10)\n"
"buffer_load_dwordx4 v[132:135], v0, s[20:23], s47 offen offset:1024 sc1\n"
"v_mfma_f32_16x16x32_bf16 v[60:63], a[120:123], v[152:155], v[60:63]\n"
"v_mfma_f32_16x16x32_bf16 v[64:67], a[124:127], v[152:155], v[64:67]\n"
"v_mfma_f32_16x16x32_bf16 v[68:71], a[128:131], v[152:155], v[68:71]\n"
"v_mfma_f32_16x16x32_bf16 v[72:75], a[188:191], v[152:155], v[72:75]\n"
"s_waitcnt vmcnt(10)\n"
"buffer_load_dwordx4 v[136:139], v0, s[20:23], s44 offen offset:2048 sc1\n"
"v_mfma_f32_16x16x32_bf16 v[60:63], a[132:135], v[156:159], v[60:63]\n"
"v_mfma_f32_16x16x32_bf16 v[64:67], a[136:139], v[156:159], v[64:67]\n"
"v_mfma_f32_16x16x32_bf16 v[68:71], a[140:143], v[156:159], v[68:71]\n"
"v_mfma_f32_16x16x32_bf16 v[76:79], a[188:191], v[156:159], v[76:79]\n"
"s_waitcnt vmcnt(10)\n"
"buffer_load_dwordx4 v[140:143], v0, s[20:23], s44 offen offset:3072 sc1\n"
"v_mfma_f32_16x16x32_bf16 v[60:63], a[144:147], v[160:163], v[60:63]\n"
"v_mfma_f32_16x16x32_bf16 v[64:67], a[148:151], v[160:163], v[64:67]\n"
"v_mfma_f32_16x16x32_bf16 v[68:71], a[152:155], v[160:163], v[68:71]\n"
"v_mfma_f32_16x16x32_bf16 v[72:75], a[192:195], v[160:163], v[72:75]\n"
"s_waitcnt vmcnt(10)\n"
"s_add_u32 s44, s44, 0x1000\n"
"buffer_load_dwordx4 v[144:147], v0, s[20:23], s44 offen sc1\n"
"v_mfma_f32_16x16x32_bf16 v[60:63], a[156:159], v[164:167], v[60:63]\n"
"v_mfma_f32_16x16x32_bf16 v[64:67], a[160:163], v[164:167], v[64:67]\n"
"v_mfma_f32_16x16x32_bf16 v[68:71], a[164:167], v[164:167], v[68:71]\n"
"v_mfma_f32_16x16x32_bf16 v[76:79], a[192:195], v[164:167], v[76:79]\n"
"s_waitcnt vmcnt(10)\n"
"buffer_load_dwordx4 v[148:151], v0, s[20:23], s44 offen offset:1024 sc1\n"
"v_mfma_f32_16x16x32_bf16 v[80:83], a[120:123], v[168:171], v[80:83]\n"
"v_mfma_f32_16x16x32_bf16 v[84:87], a[124:127], v[168:171], v[84:87]\n"
"v_mfma_f32_16x16x32_bf16 v[88:91], a[128:131], v[168:171], v[88:91]\n"
"v_mfma_f32_16x16x32_bf16 v[92:95], a[188:191], v[168:171], v[92:95]\n"
"s_waitcnt vmcnt(10)\n"
"ds_write_b128 v185, v[60:63]\n"
"v_cndmask_b32_e64 v72, v76, v72, s[38:39]\n"
"v_cndmask_b32_e64 v73, v77, v73, s[38:39]\n"
"v_cndmask_b32_e64 v74, v78, v74, s[38:39]\n"
"v_cndmask_b32_e64 v75, v79, v75, s[38:39]\n"
"v_mfma_f32_16x16x32_bf16 v[80:83], a[132:135], v[172:175], v[80:83]\n"
"v_mfma_f32_16x16x32_bf16 v[84:87], a[136:139], v[172:175], v[84:87]\n"
"v_mfma_f32_16x16x32_bf16 v[88:91], a[140:143], v[172:175], v[88:91]\n"
"v_mfma_f32_16x16x32_bf16 v[96:99], a[188:191], v[172:175], v[96:99]\n"
"s_waitcnt vmcnt(9)\n"
"ds_write_b128 v185, v[64:67] offset:1024\n"
"v_mfma_f32_16x16x32_bf16 v[80:83], a[144:147], v[176:179], v[80:83]\n"
"v_mfma_f32_16x16x32_bf16 v[84:87], a[148:151], v[176:179], v[84:87]\n"
"v_mfma_f32_16x16x32_bf16 v[88:91], a[152:155], v[176:179], v[88:91]\n"
"v_mfma_f32_16x16x32_bf16 v[92:95], a[192:195], v[176:179], v[92:95]\n"
"s_waitcnt vmcnt(8)\n"
"ds_write_b128 v185, v[68:71] offset:2048\n"
"v_mfma_f32_16x16x32_bf16 v[80:83], a[156:159], v[180:183], v[80:83]\n"
"v_mfma_f32_16x16x32_bf16 v[84:87], a[160:163], v[180:183], v[84:87]\n"
"v_mfma_f32_16x16x32_bf16 v[88:91], a[164:167], v[180:183], v[88:91]\n"
"v_mfma_f32_16x16x32_bf16 v[96:99], a[192:195], v[180:183], v[96:99]\n"
"s_waitcnt vmcnt(7)\n"
"ds_write_b128 v185, v[72:75] offset:3072\n"
"v_mfma_f32_16x16x32_bf16 v[100:103], a[120:123], v[120:123], v[100:103]\n"
"v_mfma_f32_16x16x32_bf16 v[104:107], a[124:127], v[120:123], v[104:107]\n"
"v_mfma_f32_16x16x32_bf16 v[108:111], a[128:131], v[120:123], v[108:111]\n"
"v_mfma_f32_16x16x32_bf16 v[112:115], a[188:191], v[120:123], v[112:115]\n"
"s_waitcnt vmcnt(6)\n"
"ds_write_b128 v186, v[80:83]\n"
"v_cndmask_b32_e64 v92, v96, v92, s[38:39]\n"
"v_cndmask_b32_e64 v93, v97, v93, s[38:39]\n"
"v_cndmask_b32_e64 v94, v98, v94, s[38:39]\n"
"v_cndmask_b32_e64 v95, v99, v95, s[38:39]\n"
"v_mfma_f32_16x16x32_bf16 v[100:103], a[132:135], v[124:127], v[100:103]\n"
"v_mfma_f32_16x16x32_bf16 v[104:107], a[136:139], v[124:127], v[104:107]\n"
"v_mfma_f32_16x16x32_bf16 v[108:111], a[140:143], v[124:127], v[108:111]\n"
"v_mfma_f32_16x16x32_bf16 v[116:119], a[188:191], v[124:127], v[116:119]\n"
"s_waitcnt vmcnt(5)\n"
"ds_write_b128 v186, v[84:87] offset:1024\n"
"v_mfma_f32_16x16x32_bf16 v[100:103], a[144:147], v[128:131], v[100:103]\n"
"v_mfma_f32_16x16x32_bf16 v[104:107], a[148:151], v[128:131], v[104:107]\n"
"v_mfma_f32_16x16x32_bf16 v[108:111], a[152:155], v[128:131], v[108:111]\n"
"v_mfma_f32_16x16x32_bf16 v[112:115], a[192:195], v[128:131], v[112:115]\n"
"s_waitcnt vmcnt(4)\n"
"ds_write_b128 v186, v[88:91] offset:2048\n"
"v_mfma_f32_16x16x32_bf16 v[100:103], a[156:159], v[132:135], v[100:103]\n"
"v_mfma_f32_16x16x32_bf16 v[104:107], a[160:163], v[132:135], v[104:107]\n"
"v_mfma_f32_16x16x32_bf16 v[108:111], a[164:167], v[132:135], v[108:111]\n"
"v_mfma_f32_16x16x32_bf16 v[116:119], a[192:195], v[132:135], v[116:119]\n"
"s_waitcnt vmcnt(3)\n"
"ds_write_b128 v186, v[92:95] offset:3072\n"
"v_mfma_f32_16x16x32_bf16 v[40:43], a[120:123], v[136:139], v[40:43]\n"
"v_mfma_f32_16x16x32_bf16 v[44:47], a[124:127], v[136:139], v[44:47]\n"
"v_mfma_f32_16x16x32_bf16 v[48:51], a[128:131], v[136:139], v[48:51]\n"
"v_mfma_f32_16x16x32_bf16 v[52:55], a[188:191], v[136:139], v[52:55]\n"
"s_waitcnt vmcnt(2)\n"
"ds_write_b128 v187, v[100:103]\n"
"v_cndmask_b32_e64 v112, v116, v112, s[38:39]\n"
"v_cndmask_b32_e64 v113, v117, v113, s[38:39]\n"
"v_cndmask_b32_e64 v114, v118, v114, s[38:39]\n"
"v_cndmask_b32_e64 v115, v119, v115, s[38:39]\n"
"v_mfma_f32_16x16x32_bf16 v[40:43], a[132:135], v[140:143], v[40:43]\n"
"v_mfma_f32_16x16x32_bf16 v[44:47], a[136:139], v[140:143], v[44:47]\n"
"v_mfma_f32_16x16x32_bf16 v[48:51], a[140:143], v[140:143], v[48:51]\n"
"v_mfma_f32_16x16x32_bf16 v[56:59], a[188:191], v[140:143], v[56:59]\n"
"s_waitcnt vmcnt(1)\n"
"ds_write_b128 v187, v[104:107] offset:1024\n"
"v_mfma_f32_16x16x32_bf16 v[40:43], a[144:147], v[144:147], v[40:43]\n"
"v_mfma_f32_16x16x32_bf16 v[44:47], a[148:151], v[144:147], v[44:47]\n"
"v_mfma_f32_16x16x32_bf16 v[48:51], a[152:155], v[144:147], v[48:51]\n"
"v_mfma_f32_16x16x32_bf16 v[52:55], a[192:195], v[144:147], v[52:55]\n"
"s_waitcnt vmcnt(0)\n"
"ds_write_b128 v187, v[108:111] offset:2048\n"
"v_mfma_f32_16x16x32_bf16 v[40:43], a[156:159], v[148:151], v[40:43]\n"
"v_mfma_f32_16x16x32_bf16 v[44:47], a[160:163], v[148:151], v[44:47]\n"
"v_mfma_f32_16x16x32_bf16 v[48:51], a[164:167], v[148:151], v[48:51]\n"
"v_mfma_f32_16x16x32_bf16 v[56:59], a[192:195], v[148:151], v[56:59]\n"
"ds_write_b128 v187, v[112:115] offset:3072\n"
"s_waitcnt lgkmcnt(0)\n"
"s_barrier\n"
"ds_read_b128 v[132:135], v184 offset:3072\n"
"ds_read_b128 v[148:151], v184 offset:7168\n"
"ds_read_b128 v[164:167], v184 offset:11264\n"
"ds_read_b128 v[120:123], v184\n"
"ds_read_b128 v[136:139], v184 offset:4096\n"
"ds_read_b128 v[152:155], v184 offset:8192\n"
"ds_read_b128 v[124:127], v184 offset:1024\n"
"ds_read_b128 v[140:143], v184 offset:5120\n"
"ds_read_b128 v[156:159], v184 offset:9216\n"
"ds_read_b128 v[128:131], v184 offset:2048\n"
"ds_read_b128 v[144:147], v184 offset:6144\n"
"ds_read_b128 v[160:163], v184 offset:10240\n"
"v_cndmask_b32_e64 v52, v56, v52, s[38:39]\n"
"v_cndmask_b32_e64 v53, v57, v53, s[38:39]\n"
"v_cndmask_b32_e64 v54, v58, v54, s[38:39]\n"
"v_cndmask_b32_e64 v55, v59, v55, s[38:39]\n"
"s_waitcnt lgkmcnt(11)\n"
"v_pk_add_f32 v[52:53], v[52:53], v[132:133]\n"
"v_pk_add_f32 v[54:55], v[54:55], v[134:135]\n"
"s_waitcnt lgkmcnt(10)\n"
"v_pk_add_f32 v[52:53], v[52:53], v[148:149]\n"
"v_pk_add_f32 v[54:55], v[54:55], v[150:151]\n"
"s_waitcnt lgkmcnt(9)\n"
"v_pk_add_f32 v[52:53], v[52:53], v[164:165]\n"
"v_pk_add_f32 v[54:55], v[54:55], v[166:167]\n"
"v_mov_b32 v56, v52\n"
"v_mov_b32 v57, v53\n"
"v_mov_b32 v58, v54\n"
"v_mov_b32 v59, v55\n"
"v_permlane32_swap_b32 v52, v56\n"
"v_permlane32_swap_b32 v53, v57\n"
"v_permlane32_swap_b32 v54, v58\n"
"v_permlane32_swap_b32 v55, v59\n"
"v_pk_add_f32 v[52:53], v[52:53], v[56:57]\n"
"v_pk_add_f32 v[54:55], v[54:55], v[58:59]\n"
"v_pk_fma_f32 v[32:33], v[52:53], v[38:39], v[32:33] op_sel_hi:[1,0,1]\n"
"v_pk_fma_f32 v[34:35], v[54:55], v[38:39], v[34:35] op_sel_hi:[1,0,1]\n"
"s_waitcnt lgkmcnt(8)\n"
"v_pk_add_f32 v[40:41], v[40:41], v[120:121]\n"
"v_pk_add_f32 v[42:43], v[42:43], v[122:123]\n"
"s_waitcnt lgkmcnt(7)\n"
"v_pk_add_f32 v[40:41], v[40:41], v[136:137]\n"
"v_pk_add_f32 v[42:43], v[42:43], v[138:139]\n"
"s_waitcnt lgkmcnt(6)\n"
"v_pk_add_f32 v[40:41], v[40:41], v[152:153]\n"
"v_pk_add_f32 v[42:43], v[42:43], v[154:155]\n"
"v_pk_fma_f32 v[20:21], v[40:41], v[38:39], v[20:21] op_sel_hi:[1,0,1]\n"
"v_pk_fma_f32 v[22:23], v[42:43], v[38:39], v[22:23] op_sel_hi:[1,0,1]\n"
"s_waitcnt lgkmcnt(5)\n"
"v_pk_add_f32 v[44:45], v[44:45], v[124:125]\n"
"v_pk_add_f32 v[46:47], v[46:47], v[126:127]\n"
"s_waitcnt lgkmcnt(4)\n"
"v_pk_add_f32 v[44:45], v[44:45], v[140:141]\n"
"v_pk_add_f32 v[46:47], v[46:47], v[142:143]\n"
"s_waitcnt lgkmcnt(3)\n"
"v_pk_add_f32 v[44:45], v[44:45], v[156:157]\n"
"v_pk_add_f32 v[46:47], v[46:47], v[158:159]\n"
"v_pk_fma_f32 v[24:25], v[44:45], v[38:39], v[24:25] op_sel_hi:[1,0,1]\n"
"v_pk_fma_f32 v[26:27], v[46:47], v[38:39], v[26:27] op_sel_hi:[1,0,1]\n"
"s_waitcnt lgkmcnt(2)\n"
"v_pk_add_f32 v[48:49], v[48:49], v[128:129]\n"
"v_pk_add_f32 v[50:51], v[50:51], v[130:131]\n"
"s_waitcnt lgkmcnt(1)\n"
"v_pk_add_f32 v[48:49], v[48:49], v[144:145]\n"
"v_pk_add_f32 v[50:51], v[50:51], v[146:147]\n"
"s_waitcnt lgkmcnt(0)\n"
"v_pk_add_f32 v[48:49], v[48:49], v[160:161]\n"
"v_pk_add_f32 v[50:51], v[50:51], v[162:163]\n"
"v_pk_fma_f32 v[28:29], v[48:49], v[38:39], v[28:29] op_sel_hi:[1,0,1]\n"
"v_pk_fma_f32 v[30:31], v[50:51], v[38:39], v[30:31] op_sel_hi:[1,0,1]\n"
"v_cvt_pk_bf16_f32 v60, v20, v21\n"
"v_cvt_pk_bf16_f32 v61, v22, v23\n"
"v_cvt_pk_bf16_f32 v62, v24, v25\n"
"v_cvt_pk_bf16_f32 v63, v26, v27\n"
"buffer_store_dwordx4 v[60:63], v9, s[28:31], 0 offen\n"
"v_cvt_pk_bf16_f32 v64, v28, v29\n"
"v_cvt_pk_bf16_f32 v65, v30, v31\n"
"v_cvt_pk_bf16_f32 v66, v32, v33\n"
"v_cvt_pk_bf16_f32 v67, v34, v35\n"
"v_mov_b32 v69, v64\n"
"v_mov_b32 v71, v65\n"
"s_nop 0\n"
"v_permlane16_swap_b32 v69, v68\n"
"v_permlane16_swap_b32 v71, v70\n"
"v_cndmask_b32_e64 v66, v68, v66, s[38:39]\n"
"v_cndmask_b32_e64 v67, v70, v67, s[38:39]\n"
"s_mov_b64 exec, s[40:41]\n"
"buffer_store_dwordx4 v[64:67], v9, s[28:31], 0 offen offset:64\n"
"s_mov_b64 exec, -1\n"
"s_branch .Lkend_70\n"
".Lkfallback_33:\n"
"s_waitcnt lgkmcnt(0)\n"
"v_mov_b32 v88, 0xf149f2ca\n"
"v_mov_b32 v89, 0xf149f2ca\n"
"v_lshrrev_b32 v90, 4, v0\n"
"v_and_b32 v92, 15, v90\n"
"v_lshrrev_b32 v90, 4, v90\n"
"v_lshlrev_b32 v90, 2, v90\n"
"v_sub_u32 v90, v92, v90\n"
"v_add_u32 v90, 0x100, v90\n"
"v_mov_b32 v91, v79\n"
"s_lshl_b32 s48, s97, 2\n"
"s_add_u32 s48, s48, s96\n"
"s_sub_u32 s48, s48, 8\n"
"s_mov_b32 s49, 0\n"
".Lkfbloop_34:\n"
"s_cmp_lt_i32 s48, 0\n"
"s_cbranch_scc1 .Lkfbnext_35\n"
"ds_read_b128 v[80:83], v91\n"
"ds_read_b128 v[84:87], v91 offset:1024\n"
"ds_read_b128 v[96:99], v91 offset:2048\n"
"ds_read_b128 v[100:103], v91 offset:3072\n"
"s_waitcnt lgkmcnt(0)\n"
"v_mfma_f32_16x16x32_bf16 v[128:131], v[80:83], v[38:41], 0\n"
"v_mfma_f32_16x16x32_bf16 v[132:135], v[84:87], v[38:41], 0\n"
"v_mfma_f32_16x16x32_bf16 v[136:139], v[80:83], v[42:45], 0\n"
"v_mfma_f32_16x16x32_bf16 v[140:143], v[84:87], v[42:45], 0\n"
"v_add_u32 v92, 0, v90\n"
"v_cmp_gt_u32 vcc, 0x100, v92\n"
"v_mov_b32 v93, 0xf149f2ca\n"
"s_nop 1\n"
"v_cndmask_b32_e64 v128, v93, v128, vcc\n"
"v_max_f32 v88, v88, v128\n"
"v_add_u32 v92, -1, v90\n"
"v_cmp_gt_u32 vcc, 0x100, v92\n"
"v_mov_b32 v93, 0xf149f2ca\n"
"s_nop 0\n"
"v_cndmask_b32_e64 v129, v93, v129, vcc\n"
"v_max_f32 v88, v88, v129\n"
"v_add_u32 v92, -2, v90\n"
"v_cmp_gt_u32 vcc, 0x100, v92\n"
"v_mov_b32 v93, 0xf149f2ca\n"
"s_nop 0\n"
"v_cndmask_b32_e64 v130, v93, v130, vcc\n"
"v_max_f32 v88, v88, v130\n"
"v_add_u32 v92, -3, v90\n"
"v_cmp_gt_u32 vcc, 0x100, v92\n"
"v_mov_b32 v93, 0xf149f2ca\n"
"s_nop 0\n"
"v_cndmask_b32_e64 v131, v93, v131, vcc\n"
"v_max_f32 v88, v88, v131\n"
"v_add_u32 v92, -16, v90\n"
"v_cmp_gt_u32 vcc, 0x100, v92\n"
"v_mov_b32 v93, 0xf149f2ca\n"
"s_nop 0\n"
"v_cndmask_b32_e64 v132, v93, v132, vcc\n"
"v_max_f32 v88, v88, v132\n"
"v_add_u32 v92, 0xffffffef, v90\n"
"v_cmp_gt_u32 vcc, 0x100, v92\n"
"v_mov_b32 v93, 0xf149f2ca\n"
"s_nop 0\n"
"v_cndmask_b32_e64 v133, v93, v133, vcc\n"
"v_max_f32 v88, v88, v133\n"
"v_add_u32 v92, 0xffffffee, v90\n"
"v_cmp_gt_u32 vcc, 0x100, v92\n"
"v_mov_b32 v93, 0xf149f2ca\n"
"s_nop 0\n"
"v_cndmask_b32_e64 v134, v93, v134, vcc\n"
"v_max_f32 v88, v88, v134\n"
"v_add_u32 v92, 0xffffffed, v90\n"
"v_cmp_gt_u32 vcc, 0x100, v92\n"
"v_mov_b32 v93, 0xf149f2ca\n"
"s_nop 0\n"
"v_cndmask_b32_e64 v135, v93, v135, vcc\n"
"v_max_f32 v88, v88, v135\n"
"v_add_u32 v92, 16, v90\n"
"v_cmp_gt_u32 vcc, 0x100, v92\n"
"v_mov_b32 v93, 0xf149f2ca\n"
"s_nop 0\n"
"v_cndmask_b32_e64 v136, v93, v136, vcc\n"
"v_max_f32 v89, v89, v136\n"
"v_add_u32 v92, 15, v90\n"
"v_cmp_gt_u32 vcc, 0x100, v92\n"
"v_mov_b32 v93, 0xf149f2ca\n"
"s_nop 0\n"
"v_cndmask_b32_e64 v137, v93, v137, vcc\n"
"v_max_f32 v89, v89, v137\n"
"v_add_u32 v92, 14, v90\n"
"v_cmp_gt_u32 vcc, 0x100, v92\n"
"v_mov_b32 v93, 0xf149f2ca\n"
"s_nop 0\n"
"v_cndmask_b32_e64 v138, v93, v138, vcc\n"
"v_max_f32 v89, v89, v138\n"
"v_add_u32 v92, 13, v90\n"
"v_cmp_gt_u32 vcc, 0x100, v92\n"
"v_mov_b32 v93, 0xf149f2ca\n"
"s_nop 0\n"
"v_cndmask_b32_e64 v139, v93, v139, vcc\n"
"v_max_f32 v89, v89, v139\n"
"v_add_u32 v92, 0, v90\n"
"v_cmp_gt_u32 vcc, 0x100, v92\n"
"v_mov_b32 v93, 0xf149f2ca\n"
"s_nop 0\n"
"v_cndmask_b32_e64 v140, v93, v140, vcc\n"
"v_max_f32 v89, v89, v140\n"
"v_add_u32 v92, -1, v90\n"
"v_cmp_gt_u32 vcc, 0x100, v92\n"
"v_mov_b32 v93, 0xf149f2ca\n"
"s_nop 0\n"
"v_cndmask_b32_e64 v141, v93, v141, vcc\n"
"v_max_f32 v89, v89, v141\n"
"v_add_u32 v92, -2, v90\n"
"v_cmp_gt_u32 vcc, 0x100, v92\n"
"v_mov_b32 v93, 0xf149f2ca\n"
"s_nop 0\n"
"v_cndmask_b32_e64 v142, v93, v142, vcc\n"
"v_max_f32 v89, v89, v142\n"
"v_add_u32 v92, -3, v90\n"
"v_cmp_gt_u32 vcc, 0x100, v92\n"
"v_mov_b32 v93, 0xf149f2ca\n"
"s_nop 0\n"
"v_cndmask_b32_e64 v143, v93, v143, vcc\n"
"v_max_f32 v89, v89, v143\n"
"s_nop 7\n"
".Lkfbnext_35:\n"
"v_add_u32 v91, 0x1000, v91\n"
"v_add_u32 v90, 0xffffffe0, v90\n"
"s_add_u32 s48, s48, 1\n"
"s_add_u32 s49, s49, 1\n"
"s_cmp_lt_u32 s49, 9\n"
"s_cbranch_scc1 .Lkfbloop_34\n"
"v_mov_b32 v92, v88\n"
"s_nop 1\n"
"v_permlane16_swap_b32 v88, v92\n"
"v_max_f32 v88, v88, v92\n"
"v_mov_b32 v92, v88\n"
"s_nop 1\n"
"v_permlane32_swap_b32 v88, v92\n"
"v_max_f32 v88, v88, v92\n"
"v_mov_b32 v92, v89\n"
"s_nop 1\n"
"v_permlane16_swap_b32 v89, v92\n"
"v_max_f32 v89, v89, v92\n"
"v_mov_b32 v92, v89\n"
"s_nop 1\n"
"v_permlane32_swap_b32 v89, v92\n"
"v_max_f32 v89, v89, v92\n"
"v_mov_b32 v152, 0\n"
"v_mov_b32 v153, 0\n"
"v_mov_b32 v154, 0\n"
"v_mov_b32 v155, 0\n"
"v_mov_b32 v156, 0\n"
"v_mov_b32 v157, 0\n"
"v_mov_b32 v158, 0\n"
"v_mov_b32 v159, 0\n"
"v_mov_b32 v160, 0\n"
"v_mov_b32 v161, 0\n"
"v_mov_b32 v162, 0\n"
"v_mov_b32 v163, 0\n"
"v_mov_b32 v164, 0\n"
"v_mov_b32 v165, 0\n"
"v_mov_b32 v166, 0\n"
"v_mov_b32 v167, 0\n"
"v_lshrrev_b32 v90, 4, v0\n"
"v_and_b32 v92, 15, v90\n"
"v_lshrrev_b32 v90, 4, v90\n"
"v_lshlrev_b32 v90, 2, v90\n"
"v_sub_u32 v90, v92, v90\n"
"v_add_u32 v90, 0x100, v90\n"
"v_mov_b32 v91, v79\n"
"s_lshl_b32 s48, s97, 2\n"
"s_add_u32 s48, s48, s96\n"
"s_sub_u32 s48, s48, 8\n"
"s_mov_b32 s49, 0\n"
".Lkfbloop_36:\n"
"s_cmp_lt_i32 s48, 0\n"
"s_cbranch_scc1 .Lkfbnext_37\n"
"ds_read_b128 v[80:83], v91\n"
"ds_read_b128 v[84:87], v91 offset:1024\n"
"ds_read_b128 v[96:99], v91 offset:2048\n"
"ds_read_b128 v[100:103], v91 offset:3072\n"
"s_waitcnt lgkmcnt(0)\n"
"v_mfma_f32_16x16x32_bf16 v[128:131], v[80:83], v[38:41], 0\n"
"v_mfma_f32_16x16x32_bf16 v[132:135], v[84:87], v[38:41], 0\n"
"v_mfma_f32_16x16x32_bf16 v[136:139], v[80:83], v[42:45], 0\n"
"v_mfma_f32_16x16x32_bf16 v[140:143], v[84:87], v[42:45], 0\n"
"v_add_u32 v92, 0, v90\n"
"v_cmp_gt_u32 vcc, 0x100, v92\n"
"v_mov_b32 v93, 0xf149f2ca\n"
"s_nop 1\n"
"v_cndmask_b32_e64 v128, v93, v128, vcc\n"
"v_sub_f32 v128, v128, v88\n"
"v_exp_f32 v128, v128\n"
"v_add_u32 v92, -1, v90\n"
"v_cmp_gt_u32 vcc, 0x100, v92\n"
"v_mov_b32 v93, 0xf149f2ca\n"
"s_nop 0\n"
"v_cndmask_b32_e64 v129, v93, v129, vcc\n"
"v_sub_f32 v129, v129, v88\n"
"v_exp_f32 v129, v129\n"
"v_add_u32 v92, -2, v90\n"
"v_cmp_gt_u32 vcc, 0x100, v92\n"
"v_mov_b32 v93, 0xf149f2ca\n"
"s_nop 0\n"
"v_cndmask_b32_e64 v130, v93, v130, vcc\n"
"v_sub_f32 v130, v130, v88\n"
"v_exp_f32 v130, v130\n"
"v_add_u32 v92, -3, v90\n"
"v_cmp_gt_u32 vcc, 0x100, v92\n"
"v_mov_b32 v93, 0xf149f2ca\n"
"s_nop 0\n"
"v_cndmask_b32_e64 v131, v93, v131, vcc\n"
"v_sub_f32 v131, v131, v88\n"
"v_exp_f32 v131, v131\n"
"v_add_u32 v92, -16, v90\n"
"v_cmp_gt_u32 vcc, 0x100, v92\n"
"v_mov_b32 v93, 0xf149f2ca\n"
"s_nop 0\n"
"v_cndmask_b32_e64 v132, v93, v132, vcc\n"
"v_sub_f32 v132, v132, v88\n"
"v_exp_f32 v132, v132\n"
"v_add_u32 v92, 0xffffffef, v90\n"
"v_cmp_gt_u32 vcc, 0x100, v92\n"
"v_mov_b32 v93, 0xf149f2ca\n"
"s_nop 0\n"
"v_cndmask_b32_e64 v133, v93, v133, vcc\n"
"v_sub_f32 v133, v133, v88\n"
"v_exp_f32 v133, v133\n"
"v_add_u32 v92, 0xffffffee, v90\n"
"v_cmp_gt_u32 vcc, 0x100, v92\n"
"v_mov_b32 v93, 0xf149f2ca\n"
"s_nop 0\n"
"v_cndmask_b32_e64 v134, v93, v134, vcc\n"
"v_sub_f32 v134, v134, v88\n"
"v_exp_f32 v134, v134\n"
"v_add_u32 v92, 0xffffffed, v90\n"
"v_cmp_gt_u32 vcc, 0x100, v92\n"
"v_mov_b32 v93, 0xf149f2ca\n"
"s_nop 0\n"
"v_cndmask_b32_e64 v135, v93, v135, vcc\n"
"v_sub_f32 v135, v135, v88\n"
"v_exp_f32 v135, v135\n"
"v_cvt_pk_bf16_f32 v144, v128, v129\n"
"v_cvt_pk_bf16_f32 v145, v130, v131\n"
"v_cvt_pk_bf16_f32 v146, v132, v133\n"
"v_cvt_pk_bf16_f32 v147, v134, v135\n"
"s_nop 1\n"
"v_mfma_f32_16x16x32_bf16 v[152:155], v[96:99], v[144:147], v[152:155]\n"
"v_mfma_f32_16x16x32_bf16 v[156:159], v[100:103], v[144:147], v[156:159]\n"
"v_add_u32 v92, 16, v90\n"
"v_cmp_gt_u32 vcc, 0x100, v92\n"
"v_mov_b32 v93, 0xf149f2ca\n"
"s_nop 0\n"
"v_cndmask_b32_e64 v136, v93, v136, vcc\n"
"v_sub_f32 v136, v136, v89\n"
"v_exp_f32 v136, v136\n"
"v_add_u32 v92, 15, v90\n"
"v_cmp_gt_u32 vcc, 0x100, v92\n"
"v_mov_b32 v93, 0xf149f2ca\n"
"s_nop 0\n"
"v_cndmask_b32_e64 v137, v93, v137, vcc\n"
"v_sub_f32 v137, v137, v89\n"
"v_exp_f32 v137, v137\n"
"v_add_u32 v92, 14, v90\n"
"v_cmp_gt_u32 vcc, 0x100, v92\n"
"v_mov_b32 v93, 0xf149f2ca\n"
"s_nop 0\n"
"v_cndmask_b32_e64 v138, v93, v138, vcc\n"
"v_sub_f32 v138, v138, v89\n"
"v_exp_f32 v138, v138\n"
"v_add_u32 v92, 13, v90\n"
"v_cmp_gt_u32 vcc, 0x100, v92\n"
"v_mov_b32 v93, 0xf149f2ca\n"
"s_nop 0\n"
"v_cndmask_b32_e64 v139, v93, v139, vcc\n"
"v_sub_f32 v139, v139, v89\n"
"v_exp_f32 v139, v139\n"
"v_add_u32 v92, 0, v90\n"
"v_cmp_gt_u32 vcc, 0x100, v92\n"
"v_mov_b32 v93, 0xf149f2ca\n"
"s_nop 0\n"
"v_cndmask_b32_e64 v140, v93, v140, vcc\n"
"v_sub_f32 v140, v140, v89\n"
"v_exp_f32 v140, v140\n"
"v_add_u32 v92, -1, v90\n"
"v_cmp_gt_u32 vcc, 0x100, v92\n"
"v_mov_b32 v93, 0xf149f2ca\n"
"s_nop 0\n"
"v_cndmask_b32_e64 v141, v93, v141, vcc\n"
"v_sub_f32 v141, v141, v89\n"
"v_exp_f32 v141, v141\n"
"v_add_u32 v92, -2, v90\n"
"v_cmp_gt_u32 vcc, 0x100, v92\n"
"v_mov_b32 v93, 0xf149f2ca\n"
"s_nop 0\n"
"v_cndmask_b32_e64 v142, v93, v142, vcc\n"
"v_sub_f32 v142, v142, v89\n"
"v_exp_f32 v142, v142\n"
"v_add_u32 v92, -3, v90\n"
"v_cmp_gt_u32 vcc, 0x100, v92\n"
"v_mov_b32 v93, 0xf149f2ca\n"
"s_nop 0\n"
"v_cndmask_b32_e64 v143, v93, v143, vcc\n"
"v_sub_f32 v143, v143, v89\n"
"v_exp_f32 v143, v143\n"
"v_cvt_pk_bf16_f32 v144, v136, v137\n"
"v_cvt_pk_bf16_f32 v145, v138, v139\n"
"v_cvt_pk_bf16_f32 v146, v140, v141\n"
"v_cvt_pk_bf16_f32 v147, v142, v143\n"
"s_nop 1\n"
"v_mfma_f32_16x16x32_bf16 v[160:163], v[96:99], v[144:147], v[160:163]\n"
"v_mfma_f32_16x16x32_bf16 v[164:167], v[100:103], v[144:147], v[164:167]\n"
"s_nop 7\n"
".Lkfbnext_37:\n"
"v_add_u32 v91, 0x1000, v91\n"
"v_add_u32 v90, 0xffffffe0, v90\n"
"s_add_u32 s48, s48, 1\n"
"s_add_u32 s49, s49, 1\n"
"s_cmp_lt_u32 s49, 9\n"
"s_cbranch_scc1 .Lkfbloop_36\n"
"v_mov_b32 v114, v156\n"
"v_mov_b32 v115, v164\n"
"s_nop 1\n"
"v_permlane16_swap_b32 v114, v115\n"
"v_mov_b32 v112, v115\n"
"s_nop 1\n"
"v_permlane32_swap_b32 v112, v115\n"
"s_nop 1\n"
"s_branch .Lknorm_32\n"
".Lkfallback_56:\n"
"s_waitcnt lgkmcnt(0)\n"
"v_mov_b32 v88, 0xf149f2ca\n"
"v_mov_b32 v89, 0xf149f2ca\n"
"v_lshrrev_b32 v90, 4, v0\n"
"v_and_b32 v92, 15, v90\n"
"v_lshrrev_b32 v90, 4, v90\n"
"v_lshlrev_b32 v90, 2, v90\n"
"v_sub_u32 v90, v92, v90\n"
"v_add_u32 v90, 0x100, v90\n"
"v_mov_b32 v91, v79\n"
"s_lshl_b32 s48, s97, 2\n"
"s_add_u32 s48, s48, s96\n"
"s_sub_u32 s48, s48, 8\n"
"s_mov_b32 s49, 0\n"
".Lkfbloop_57:\n"
"s_cmp_lt_i32 s48, 0\n"
"s_cbranch_scc1 .Lkfbnext_58\n"
"ds_read_b128 v[80:83], v91\n"
"ds_read_b128 v[84:87], v91 offset:1024\n"
"ds_read_b128 v[96:99], v91 offset:2048\n"
"ds_read_b128 v[100:103], v91 offset:3072\n"
"s_waitcnt lgkmcnt(0)\n"
"v_mfma_f32_16x16x32_bf16 v[128:131], v[80:83], v[38:41], 0\n"
"v_mfma_f32_16x16x32_bf16 v[132:135], v[84:87], v[38:41], 0\n"
"v_mfma_f32_16x16x32_bf16 v[136:139], v[80:83], v[42:45], 0\n"
"v_mfma_f32_16x16x32_bf16 v[140:143], v[84:87], v[42:45], 0\n"
"v_add_u32 v92, 0, v90\n"
"v_cmp_gt_u32 vcc, 0x100, v92\n"
"v_mov_b32 v93, 0xf149f2ca\n"
"s_nop 1\n"
"v_cndmask_b32_e64 v128, v93, v128, vcc\n"
"v_max_f32 v88, v88, v128\n"
"v_add_u32 v92, -1, v90\n"
"v_cmp_gt_u32 vcc, 0x100, v92\n"
"v_mov_b32 v93, 0xf149f2ca\n"
"s_nop 0\n"
"v_cndmask_b32_e64 v129, v93, v129, vcc\n"
"v_max_f32 v88, v88, v129\n"
"v_add_u32 v92, -2, v90\n"
"v_cmp_gt_u32 vcc, 0x100, v92\n"
"v_mov_b32 v93, 0xf149f2ca\n"
"s_nop 0\n"
"v_cndmask_b32_e64 v130, v93, v130, vcc\n"
"v_max_f32 v88, v88, v130\n"
"v_add_u32 v92, -3, v90\n"
"v_cmp_gt_u32 vcc, 0x100, v92\n"
"v_mov_b32 v93, 0xf149f2ca\n"
"s_nop 0\n"
"v_cndmask_b32_e64 v131, v93, v131, vcc\n"
"v_max_f32 v88, v88, v131\n"
"v_add_u32 v92, -16, v90\n"
"v_cmp_gt_u32 vcc, 0x100, v92\n"
"v_mov_b32 v93, 0xf149f2ca\n"
"s_nop 0\n"
"v_cndmask_b32_e64 v132, v93, v132, vcc\n"
"v_max_f32 v88, v88, v132\n"
"v_add_u32 v92, 0xffffffef, v90\n"
"v_cmp_gt_u32 vcc, 0x100, v92\n"
"v_mov_b32 v93, 0xf149f2ca\n"
"s_nop 0\n"
"v_cndmask_b32_e64 v133, v93, v133, vcc\n"
"v_max_f32 v88, v88, v133\n"
"v_add_u32 v92, 0xffffffee, v90\n"
"v_cmp_gt_u32 vcc, 0x100, v92\n"
"v_mov_b32 v93, 0xf149f2ca\n"
"s_nop 0\n"
"v_cndmask_b32_e64 v134, v93, v134, vcc\n"
"v_max_f32 v88, v88, v134\n"
"v_add_u32 v92, 0xffffffed, v90\n"
"v_cmp_gt_u32 vcc, 0x100, v92\n"
"v_mov_b32 v93, 0xf149f2ca\n"
"s_nop 0\n"
"v_cndmask_b32_e64 v135, v93, v135, vcc\n"
"v_max_f32 v88, v88, v135\n"
"v_add_u32 v92, 16, v90\n"
"v_cmp_gt_u32 vcc, 0x100, v92\n"
"v_mov_b32 v93, 0xf149f2ca\n"
"s_nop 0\n"
"v_cndmask_b32_e64 v136, v93, v136, vcc\n"
"v_max_f32 v89, v89, v136\n"
"v_add_u32 v92, 15, v90\n"
"v_cmp_gt_u32 vcc, 0x100, v92\n"
"v_mov_b32 v93, 0xf149f2ca\n"
"s_nop 0\n"
"v_cndmask_b32_e64 v137, v93, v137, vcc\n"
"v_max_f32 v89, v89, v137\n"
"v_add_u32 v92, 14, v90\n"
"v_cmp_gt_u32 vcc, 0x100, v92\n"
"v_mov_b32 v93, 0xf149f2ca\n"
"s_nop 0\n"
"v_cndmask_b32_e64 v138, v93, v138, vcc\n"
"v_max_f32 v89, v89, v138\n"
"v_add_u32 v92, 13, v90\n"
"v_cmp_gt_u32 vcc, 0x100, v92\n"
"v_mov_b32 v93, 0xf149f2ca\n"
"s_nop 0\n"
"v_cndmask_b32_e64 v139, v93, v139, vcc\n"
"v_max_f32 v89, v89, v139\n"
"v_add_u32 v92, 0, v90\n"
"v_cmp_gt_u32 vcc, 0x100, v92\n"
"v_mov_b32 v93, 0xf149f2ca\n"
"s_nop 0\n"
"v_cndmask_b32_e64 v140, v93, v140, vcc\n"
"v_max_f32 v89, v89, v140\n"
"v_add_u32 v92, -1, v90\n"
"v_cmp_gt_u32 vcc, 0x100, v92\n"
"v_mov_b32 v93, 0xf149f2ca\n"
"s_nop 0\n"
"v_cndmask_b32_e64 v141, v93, v141, vcc\n"
"v_max_f32 v89, v89, v141\n"
"v_add_u32 v92, -2, v90\n"
"v_cmp_gt_u32 vcc, 0x100, v92\n"
"v_mov_b32 v93, 0xf149f2ca\n"
"s_nop 0\n"
"v_cndmask_b32_e64 v142, v93, v142, vcc\n"
"v_max_f32 v89, v89, v142\n"
"v_add_u32 v92, -3, v90\n"
"v_cmp_gt_u32 vcc, 0x100, v92\n"
"v_mov_b32 v93, 0xf149f2ca\n"
"s_nop 0\n"
"v_cndmask_b32_e64 v143, v93, v143, vcc\n"
"v_max_f32 v89, v89, v143\n"
"s_nop 7\n"
".Lkfbnext_58:\n"
"v_add_u32 v91, 0x1000, v91\n"
"v_add_u32 v90, 0xffffffe0, v90\n"
"s_add_u32 s48, s48, 1\n"
"s_add_u32 s49, s49, 1\n"
"s_cmp_lt_u32 s49, 9\n"
"s_cbranch_scc1 .Lkfbloop_57\n"
"v_mov_b32 v92, v88\n"
"s_nop 1\n"
"v_permlane16_swap_b32 v88, v92\n"
"v_max_f32 v88, v88, v92\n"
"v_mov_b32 v92, v88\n"
"s_nop 1\n"
"v_permlane32_swap_b32 v88, v92\n"
"v_max_f32 v88, v88, v92\n"
"v_mov_b32 v92, v89\n"
"s_nop 1\n"
"v_permlane16_swap_b32 v89, v92\n"
"v_max_f32 v89, v89, v92\n"
"v_mov_b32 v92, v89\n"
"s_nop 1\n"
"v_permlane32_swap_b32 v89, v92\n"
"v_max_f32 v89, v89, v92\n"
"v_mov_b32 v152, 0\n"
"v_mov_b32 v153, 0\n"
"v_mov_b32 v154, 0\n"
"v_mov_b32 v155, 0\n"
"v_mov_b32 v156, 0\n"
"v_mov_b32 v157, 0\n"
"v_mov_b32 v158, 0\n"
"v_mov_b32 v159, 0\n"
"v_mov_b32 v160, 0\n"
"v_mov_b32 v161, 0\n"
"v_mov_b32 v162, 0\n"
"v_mov_b32 v163, 0\n"
"v_mov_b32 v164, 0\n"
"v_mov_b32 v165, 0\n"
"v_mov_b32 v166, 0\n"
"v_mov_b32 v167, 0\n"
"v_lshrrev_b32 v90, 4, v0\n"
"v_and_b32 v92, 15, v90\n"
"v_lshrrev_b32 v90, 4, v90\n"
"v_lshlrev_b32 v90, 2, v90\n"
"v_sub_u32 v90, v92, v90\n"
"v_add_u32 v90, 0x100, v90\n"
"v_mov_b32 v91, v79\n"
"s_lshl_b32 s48, s97, 2\n"
"s_add_u32 s48, s48, s96\n"
"s_sub_u32 s48, s48, 8\n"
"s_mov_b32 s49, 0\n"
".Lkfbloop_59:\n"
"s_cmp_lt_i32 s48, 0\n"
"s_cbranch_scc1 .Lkfbnext_60\n"
"ds_read_b128 v[80:83], v91\n"
"ds_read_b128 v[84:87], v91 offset:1024\n"
"ds_read_b128 v[96:99], v91 offset:2048\n"
"ds_read_b128 v[100:103], v91 offset:3072\n"
"s_waitcnt lgkmcnt(0)\n"
"v_mfma_f32_16x16x32_bf16 v[128:131], v[80:83], v[38:41], 0\n"
"v_mfma_f32_16x16x32_bf16 v[132:135], v[84:87], v[38:41], 0\n"
"v_mfma_f32_16x16x32_bf16 v[136:139], v[80:83], v[42:45], 0\n"
"v_mfma_f32_16x16x32_bf16 v[140:143], v[84:87], v[42:45], 0\n"
"v_add_u32 v92, 0, v90\n"
"v_cmp_gt_u32 vcc, 0x100, v92\n"
"v_mov_b32 v93, 0xf149f2ca\n"
"s_nop 1\n"
"v_cndmask_b32_e64 v128, v93, v128, vcc\n"
"v_sub_f32 v128, v128, v88\n"
"v_exp_f32 v128, v128\n"
"v_add_u32 v92, -1, v90\n"
"v_cmp_gt_u32 vcc, 0x100, v92\n"
"v_mov_b32 v93, 0xf149f2ca\n"
"s_nop 0\n"
"v_cndmask_b32_e64 v129, v93, v129, vcc\n"
"v_sub_f32 v129, v129, v88\n"
"v_exp_f32 v129, v129\n"
"v_add_u32 v92, -2, v90\n"
"v_cmp_gt_u32 vcc, 0x100, v92\n"
"v_mov_b32 v93, 0xf149f2ca\n"
"s_nop 0\n"
"v_cndmask_b32_e64 v130, v93, v130, vcc\n"
"v_sub_f32 v130, v130, v88\n"
"v_exp_f32 v130, v130\n"
"v_add_u32 v92, -3, v90\n"
"v_cmp_gt_u32 vcc, 0x100, v92\n"
"v_mov_b32 v93, 0xf149f2ca\n"
"s_nop 0\n"
"v_cndmask_b32_e64 v131, v93, v131, vcc\n"
"v_sub_f32 v131, v131, v88\n"
"v_exp_f32 v131, v131\n"
"v_add_u32 v92, -16, v90\n"
"v_cmp_gt_u32 vcc, 0x100, v92\n"
"v_mov_b32 v93, 0xf149f2ca\n"
"s_nop 0\n"
"v_cndmask_b32_e64 v132, v93, v132, vcc\n"
"v_sub_f32 v132, v132, v88\n"
"v_exp_f32 v132, v132\n"
"v_add_u32 v92, 0xffffffef, v90\n"
"v_cmp_gt_u32 vcc, 0x100, v92\n"
"v_mov_b32 v93, 0xf149f2ca\n"
"s_nop 0\n"
"v_cndmask_b32_e64 v133, v93, v133, vcc\n"
"v_sub_f32 v133, v133, v88\n"
"v_exp_f32 v133, v133\n"
"v_add_u32 v92, 0xffffffee, v90\n"
"v_cmp_gt_u32 vcc, 0x100, v92\n"
"v_mov_b32 v93, 0xf149f2ca\n"
"s_nop 0\n"
"v_cndmask_b32_e64 v134, v93, v134, vcc\n"
"v_sub_f32 v134, v134, v88\n"
"v_exp_f32 v134, v134\n"
"v_add_u32 v92, 0xffffffed, v90\n"
"v_cmp_gt_u32 vcc, 0x100, v92\n"
"v_mov_b32 v93, 0xf149f2ca\n"
"s_nop 0\n"
"v_cndmask_b32_e64 v135, v93, v135, vcc\n"
"v_sub_f32 v135, v135, v88\n"
"v_exp_f32 v135, v135\n"
"v_cvt_pk_bf16_f32 v144, v128, v129\n"
"v_cvt_pk_bf16_f32 v145, v130, v131\n"
"v_cvt_pk_bf16_f32 v146, v132, v133\n"
"v_cvt_pk_bf16_f32 v147, v134, v135\n"
"s_nop 1\n"
"v_mfma_f32_16x16x32_bf16 v[152:155], v[96:99], v[144:147], v[152:155]\n"
"v_mfma_f32_16x16x32_bf16 v[156:159], v[100:103], v[144:147], v[156:159]\n"
"v_add_u32 v92, 16, v90\n"
"v_cmp_gt_u32 vcc, 0x100, v92\n"
"v_mov_b32 v93, 0xf149f2ca\n"
"s_nop 0\n"
"v_cndmask_b32_e64 v136, v93, v136, vcc\n"
"v_sub_f32 v136, v136, v89\n"
"v_exp_f32 v136, v136\n"
"v_add_u32 v92, 15, v90\n"
"v_cmp_gt_u32 vcc, 0x100, v92\n"
"v_mov_b32 v93, 0xf149f2ca\n"
"s_nop 0\n"
"v_cndmask_b32_e64 v137, v93, v137, vcc\n"
"v_sub_f32 v137, v137, v89\n"
"v_exp_f32 v137, v137\n"
"v_add_u32 v92, 14, v90\n"
"v_cmp_gt_u32 vcc, 0x100, v92\n"
"v_mov_b32 v93, 0xf149f2ca\n"
"s_nop 0\n"
"v_cndmask_b32_e64 v138, v93, v138, vcc\n"
"v_sub_f32 v138, v138, v89\n"
"v_exp_f32 v138, v138\n"
"v_add_u32 v92, 13, v90\n"
"v_cmp_gt_u32 vcc, 0x100, v92\n"
"v_mov_b32 v93, 0xf149f2ca\n"
"s_nop 0\n"
"v_cndmask_b32_e64 v139, v93, v139, vcc\n"
"v_sub_f32 v139, v139, v89\n"
"v_exp_f32 v139, v139\n"
"v_add_u32 v92, 0, v90\n"
"v_cmp_gt_u32 vcc, 0x100, v92\n"
"v_mov_b32 v93, 0xf149f2ca\n"
"s_nop 0\n"
"v_cndmask_b32_e64 v140, v93, v140, vcc\n"
"v_sub_f32 v140, v140, v89\n"
"v_exp_f32 v140, v140\n"
"v_add_u32 v92, -1, v90\n"
"v_cmp_gt_u32 vcc, 0x100, v92\n"
"v_mov_b32 v93, 0xf149f2ca\n"
"s_nop 0\n"
"v_cndmask_b32_e64 v141, v93, v141, vcc\n"
"v_sub_f32 v141, v141, v89\n"
"v_exp_f32 v141, v141\n"
"v_add_u32 v92, -2, v90\n"
"v_cmp_gt_u32 vcc, 0x100, v92\n"
"v_mov_b32 v93, 0xf149f2ca\n"
"s_nop 0\n"
"v_cndmask_b32_e64 v142, v93, v142, vcc\n"
"v_sub_f32 v142, v142, v89\n"
"v_exp_f32 v142, v142\n"
"v_add_u32 v92, -3, v90\n"
"v_cmp_gt_u32 vcc, 0x100, v92\n"
"v_mov_b32 v93, 0xf149f2ca\n"
"s_nop 0\n"
"v_cndmask_b32_e64 v143, v93, v143, vcc\n"
"v_sub_f32 v143, v143, v89\n"
"v_exp_f32 v143, v143\n"
"v_cvt_pk_bf16_f32 v144, v136, v137\n"
"v_cvt_pk_bf16_f32 v145, v138, v139\n"
"v_cvt_pk_bf16_f32 v146, v140, v141\n"
"v_cvt_pk_bf16_f32 v147, v142, v143\n"
"s_nop 1\n"
"v_mfma_f32_16x16x32_bf16 v[160:163], v[96:99], v[144:147], v[160:163]\n"
"v_mfma_f32_16x16x32_bf16 v[164:167], v[100:103], v[144:147], v[164:167]\n"
"s_nop 7\n"
".Lkfbnext_60:\n"
"v_add_u32 v91, 0x1000, v91\n"
"v_add_u32 v90, 0xffffffe0, v90\n"
"s_add_u32 s48, s48, 1\n"
"s_add_u32 s49, s49, 1\n"
"s_cmp_lt_u32 s49, 9\n"
"s_cbranch_scc1 .Lkfbloop_59\n"
"v_mov_b32 v114, v156\n"
"v_mov_b32 v115, v164\n"
"s_nop 1\n"
"v_permlane16_swap_b32 v114, v115\n"
"v_mov_b32 v112, v115\n"
"s_nop 1\n"
"v_permlane32_swap_b32 v112, v115\n"
"s_nop 1\n"
"s_branch .Lknorm_55\n"
".Lkend_70:\n"

      :
      : "s"(ws), "s"(xin), "s"(out), "s"(blockIdx.x), "s"(w), "s"(wqkv), "s"(wo), "s"(w1), "s"(w2)
      : "memory", "vcc", "scc", "v0", "v1", "v2", "v3", "v4", "v5", "v6", "v7", "v8", "v9", "v10", "v11", "v12", "v13", "v14", "v15", "v16", "v17", "v18", "v19", "v20", "v21", "v22", "v23", "v24", "v25", "v26", "v27", "v28", "v29", "v30", "v31", "v32", "v33", "v34", "v35", "v36", "v37", "v38", "v39", "v40", "v41", "v42", "v43", "v44", "v45", "v46", "v47", "v48", "v49", "v50", "v51", "v52", "v53", "v54", "v55", "v56", "v57", "v58", "v59", "v60", "v61", "v62", "v63", "v64", "v65", "v66", "v67", "v68", "v69", "v70", "v71", "v72", "v73", "v74", "v75", "v76", "v77", "v78", "v79", "v80", "v81", "v82", "v83", "v84", "v85", "v86", "v87", "v88", "v89", "v90", "v91", "v92", "v93", "v94", "v95", "v96", "v97", "v98", "v99", "v100", "v101", "v102", "v103", "v104", "v105", "v106", "v107", "v108", "v109", "v110", "v111", "v112", "v113", "v114", "v115", "v116", "v117", "v118", "v119", "v120", "v121", "v122", "v123", "v124", "v125", "v126", "v127", "v128", "v129", "v130", "v131", "v132", "v133", "v134", "v135", "v136", "v137", "v138", "v139", "v140", "v141", "v142", "v143", "v144", "v145", "v146", "v147", "v148", "v149", "v150", "v151", "v152", "v153", "v154", "v155", "v156", "v157", "v158", "v159", "v160", "v161", "v162", "v163", "v164", "v165", "v166", "v167", "v168", "v169", "v170", "v171", "v172", "v173", "v174", "v175", "v176", "v177", "v178", "v179", "v180", "v181", "v182", "v183", "v184", "v185", "v186", "v187", "v188", "v189", "v190", "v191", "v192", "v193", "v194", "v195", "v196", "v197", "v198", "v199", "v200", "v201", "v202", "v203", "v204", "v205", "v206", "v207", "v208", "v209", "v210", "v211", "v212", "v213", "v214", "v215", "v216", "v217", "v218", "v219", "v220", "v221", "v222", "v223", "v224", "v225", "v226", "v227", "v228", "v229", "v230", "v231", "v232", "v233", "v234", "v235", "v236", "v237", "v238", "v239", "v240", "v241", "v242", "v243", "v244", "v245", "v246", "v247", "v248", "v249", "v250", "v251", "v252", "v253", "v254", "v255", "a0", "a1", "a2", "a3", "a4", "a5", "a6", "a7", "a8", "a9", "a10", "a11", "a12", "a13", "a14", "a15", "a16", "a17", "a18", "a19", "a20", "a21", "a22", "a23", "a24", "a25", "a26", "a27", "a28", "a29", "a30", "a31", "a32", "a33", "a34", "a35", "a36", "a37", "a38", "a39", "a40", "a41", "a42", "a43", "a44", "a45", "a46", "a47", "a48", "a49", "a50", "a51", "a52", "a53", "a54", "a55", "a56", "a57", "a58", "a59", "a60", "a61", "a62", "a63", "a64", "a65", "a66", "a67", "a68", "a69", "a70", "a71", "a72", "a73", "a74", "a75", "a76", "a77", "a78", "a79", "a80", "a81", "a82", "a83", "a84", "a85", "a86", "a87", "a88", "a89", "a90", "a91", "a92", "a93", "a94", "a95", "a96", "a97", "a98", "a99", "a100", "a101", "a102", "a103", "a104", "a105", "a106", "a107", "a108", "a109", "a110", "a111", "a112", "a113", "a114", "a115", "a116", "a117", "a118", "a119", "a120", "a121", "a122", "a123", "a124", "a125", "a126", "a127", "a128", "a129", "a130", "a131", "a132", "a133", "a134", "a135", "a136", "a137", "a138", "a139", "a140", "a141", "a142", "a143", "a144", "a145", "a146", "a147", "a148", "a149", "a150", "a151", "a152", "a153", "a154", "a155", "a156", "a157", "a158", "a159", "a160", "a161", "a162", "a163", "a164", "a165", "a166", "a167", "a168", "a169", "a170", "a171", "a172", "a173", "a174", "a175", "a176", "a177", "a178", "a179", "a180", "a181", "a182", "a183", "a184", "a185", "a186", "a187", "a188", "a189", "a190", "a191", "a192", "a193", "a194", "a195", "a196", "a197", "a198", "a199", "a200", "a201", "a202", "a203", "a204", "a205", "a206", "a207", "a208", "a209", "a210", "a211", "a212", "a213", "a214", "a215", "a216", "a217", "a218", "a219", "a220", "a221", "a222", "a223", "a224", "a225", "a226", "a227", "a228", "a229", "a230", "a231", "a232", "a233", "a234", "a235", "a236", "a237", "a238", "a239", "a240", "a241", "a242", "a243", "a244", "a245", "a246", "a247", "a248", "a249", "a250", "a251", "a252", "a253", "a254", "a255", "s20", "s21", "s22", "s23", "s24", "s25", "s26", "s27", "s28", "s29", "s30", "s31", "s34", "s35", "s36", "s37", "s38", "s39", "s40", "s41", "s42", "s43", "s44", "s45", "s46", "s47", "s48", "s49", "s50", "s51", "s52", "s53", "s54", "s55", "s56", "s57", "s58", "s59", "s60", "s61", "s62", "s63", "s64", "s65", "s66", "s67", "s68", "s69", "s70", "s71", "s72", "s73", "s74", "s75", "s76", "s77", "s78", "s79", "s80", "s81", "s82", "s83", "s84", "s85", "s86", "s87", "s88", "s89", "s90", "s91", "s92", "s93", "s94", "s95", "s96", "s97", "s98", "s99");
}

extern "C" void kernel_launch(void* const* d_in, const int* in_sizes, int n_in, void* d_out, int out_size, void* d_ws, size_t ws_size,
                              hipStream_t stream) {
  (void)hipFuncSetAttribute((const void*)k_main, hipFuncAttributeMaxDynamicSharedMemorySize, LDS_BYTES);
  k_main<<<256, 256, LDS_BYTES, stream>>>(d_in[0], d_out, d_ws, d_in[1], d_in[2], d_in[4], d_in[5]);
}
```
